```python
import torch

def _softmax(x, axis=-1):
    return torch.softmax(x, dim=axis)
def _gelu(x):
    return torch.nn.functional.gelu(x, approximate="tanh")
def _silu(x):
    return torch.nn.functional.silu(x)
def _sigmoid(x):
    return torch.sigmoid(x)
def _layernorm(x, axis=-1, eps=1e-5):
    m = x.mean(dim=axis, keepdim=True)
    v = x.var(dim=axis, keepdim=True, unbiased=False)
    return (x - m) / torch.sqrt(v + eps)
def _scatter_add(tgt, idx, src, axis=0):
    return tgt.clone().index_add_(axis, idx, src)
def _segment_sum(src, seg, n):
    out = torch.zeros((n,) + src.shape[1:], dtype=src.dtype)
    return out.index_add_(0, seg, src)
def _pad(x, pw):
    flat = tuple(v for pair in reversed(pw) for v in pair)
    return torch.nn.functional.pad(x, flat)

kernel_name = 'recurrent_scan__spectral_gfnet__mixer_channel_mlp__prompt'

def setup_inputs():

    _w = (lambda a: (lambda t: t.to(torch.bfloat16) if t.is_floating_point() else t)(torch.as_tensor(a)))
    def _A_setup():
        B, S, D = 1, 2048, 576
        Df = D // 2 + 1
        return {
            "x": _w((lambda *s: torch.randn(*s, dtype=torch.bfloat16) * 0.05)(B, S, D)),
            "w_r": _w((lambda *s: torch.randn(*s, dtype=torch.bfloat16) * 0.8)(S, Df) * 0.02),
            "w_i": _w((lambda *s: torch.randn(*s, dtype=torch.bfloat16) * 0.8)(S, Df) * 0.02),
        }
    _A_in = _A_setup()

    def _B_setup():
        B, S, D, F = 1, 2048, 576, 4608
        return {
            "x": _w((lambda *s: torch.randn(*s, dtype=torch.bfloat16) * 0.05)(B, S, D)),
            "w1": _w((lambda *s: torch.randn(*s, dtype=torch.bfloat16) * 0.8)(D, F) * (D ** -0.5)),
            "b1": _w((lambda *s: torch.randn(*s, dtype=torch.bfloat16) * 0.8)(F)),
            "w2": _w((lambda *s: torch.randn(*s, dtype=torch.bfloat16) * 0.8)(F, D) * (F ** -0.5)),
            "b2": _w((lambda *s: torch.randn(*s, dtype=torch.bfloat16) * 0.2)(D)),
        }
    _B_in = _B_setup()

    return {
        "A_x": _A_in["x"],
        "A_w_r": _A_in["w_r"],
        "A_w_i": _A_in["w_i"],
        "B_x": _B_in["x"],
        "B_w1": _B_in["w1"],
        "B_b1": _B_in["b1"],
        "B_w2": _B_in["w2"],
        "B_b2": _B_in["b2"],
    }

def reference(A_x, A_w_r, A_w_i, B_x, B_w1, B_b1, B_w2, B_b2):
    def _A_ref(x, w_r, w_i):
        B, S, D = x.shape
        X = (lambda x, axes=(-2,-1): torch.fft.rfft2(x.float(), dim=axes))(x, axes=(-2, -1))
        W = w_r + 1j * w_i
        X = X * W
        y = (lambda x, s, axes=(-2,-1): torch.fft.irfft2(x, s=s, dim=axes).to(torch.bfloat16))(X, (S, D), axes=(-2, -1))
        return y

    def _B_ref(x, w1, b1, w2, b2):
        y = _layernorm(x, axis=-1)
        h = _gelu(torch.einsum("bsd,df->bsf", y, w1) + b1)
        return torch.einsum("bsf,fd->bsd", h, w2) + b2

    x = A_x
    xn = _layernorm(x, axis=-1)
    s = _A_ref(xn, A_w_r, A_w_i)
    g = _silu(_B_ref(xn, B_w1, B_b1, B_w2, B_b2))
    return x + s * g
```

```cpp
#include <hip/hip_runtime.h>
#include <hip/hip_bf16.h>
#include <stdint.h>

#define S_ 2048
#define D_ 576
#define DF_ 289
#define F_ 4608

__device__ __forceinline__ float bf2f(uint16_t v) { return __uint_as_float(((uint32_t)v) << 16); }
__device__ __forceinline__ uint16_t f2bf(float f) {
    __hip_bfloat16 b = __float2bfloat16(f);
    return *reinterpret_cast<uint16_t*>(&b);
}
__device__ __forceinline__ float rbf(float f) { return bf2f(f2bf(f)); }

__device__ __forceinline__ float wave_sum(float v) {
    for (int o = 32; o > 0; o >>= 1) v += __shfl_xor(v, o);
    return v;
}

__global__ void k_ln(const uint16_t* __restrict__ x, float* __restrict__ xn, uint16_t* __restrict__ y) {
    int row = blockIdx.x * 4 + (threadIdx.x >> 6);
    int lane = threadIdx.x & 63;
    float v[9];
    float s = 0.f;
    for (int i = 0; i < 9; i++) { v[i] = bf2f(x[row * D_ + i * 64 + lane]); s += v[i]; }
    for (int pass = 0; pass < 2; pass++) {
        s = 0.f;
        for (int i = 0; i < 9; i++) s += v[i];
        s = wave_sum(s);
        float mean = s * (1.0f / D_);
        float q = 0.f;
        for (int i = 0; i < 9; i++) { float d = v[i] - mean; q += d * d; }
        q = wave_sum(q);
        float var = rbf(q * (1.0f / D_));
        float m = rbf(mean);
        float sd = rbf(sqrtf(rbf(var + 1e-5f)));
        for (int i = 0; i < 9; i++) v[i] = rbf(rbf(v[i] - m) / sd);
        if (pass == 0) for (int i = 0; i < 9; i++) xn[row * D_ + i * 64 + lane] = v[i];
        else for (int i = 0; i < 9; i++) y[row * D_ + i * 64 + lane] = f2bf(v[i]);
    }
}

__global__ void k_rowdft(const float* __restrict__ xn, float2* __restrict__ U) {
    __shared__ float xs[D_];
    __shared__ float2 tw[D_];
    int row = blockIdx.x;
    for (int i = threadIdx.x; i < D_; i += blockDim.x) {
        xs[i] = xn[row * D_ + i];
        float sn, cs; sincospif(-2.0f * i / D_, &sn, &cs); tw[i] = make_float2(cs, sn);
    }
    __syncthreads();
    int k = threadIdx.x;
    if (k < DF_) {
        float re = 0.f, im = 0.f; int idx = 0;
        for (int d = 0; d < D_; d++) {
            float2 t = tw[idx]; re += xs[d] * t.x; im += xs[d] * t.y;
            idx += k; if (idx >= D_) idx -= D_;
        }
        U[(size_t)k * S_ + row] = make_float2(re, im);
    }
}

__global__ void k_col(const float2* __restrict__ U, const uint16_t* __restrict__ wr, const uint16_t* __restrict__ wi, float2* __restrict__ Z) {
    __shared__ float2 a[S_];
    __shared__ float2 b[S_];
    __shared__ float2 tw[S_];
    int k = blockIdx.x;
    for (int i = threadIdx.x; i < S_; i += blockDim.x) {
        a[i] = U[(size_t)k * S_ + i];
        float sn, cs; sincospif(-2.0f * i / S_, &sn, &cs); tw[i] = make_float2(cs, sn);
    }
    __syncthreads();
    for (int q = threadIdx.x; q < S_; q += blockDim.x) {
        float re = 0.f, im = 0.f;
        for (int s = 0; s < S_; s++) {
            float2 t = tw[(q * s) & (S_ - 1)]; float2 v = a[s];
            re += v.x * t.x - v.y * t.y; im += v.x * t.y + v.y * t.x;
        }
        float w_r = bf2f(wr[q * DF_ + k]), w_i = bf2f(wi[q * DF_ + k]);
        b[q] = make_float2(re * w_r - im * w_i, re * w_i + im * w_r);
    }
    __syncthreads();
    for (int s = threadIdx.x; s < S_; s += blockDim.x) {
        float re = 0.f, im = 0.f;
        for (int q = 0; q < S_; q++) {
            float2 t = tw[(q * s) & (S_ - 1)]; float2 v = b[q];
            re += v.x * t.x + v.y * t.y; im += -v.x * t.y + v.y * t.x;
        }
        Z[(size_t)k * S_ + s] = make_float2(re * (1.0f / S_), im * (1.0f / S_));
    }
}

template <int MODE>
__global__ void k_gemm(const uint16_t* __restrict__ A, const uint16_t* __restrict__ B, const uint16_t* __restrict__ bias,
                       uint16_t* __restrict__ Cb, float* __restrict__ Cf, int M, int N, int K) {
    __shared__ float As[32][33];
    __shared__ float Bs[32][33];
    int tx = threadIdx.x & 31, ty = threadIdx.x >> 5;
    int bm = blockIdx.y * 32, bn = blockIdx.x * 32;
    float acc[4] = {0, 0, 0, 0};
    for (int k0 = 0; k0 < K; k0 += 32) {
        for (int i = 0; i < 4; i++) {
            As[ty + i * 8][tx] = bf2f(A[(size_t)(bm + ty + i * 8) * K + k0 + tx]);
            Bs[ty + i * 8][tx] = bf2f(B[(size_t)(k0 + ty + i * 8) * N + bn + tx]);
        }
        __syncthreads();
        for (int kk = 0; kk < 32; kk++)
            for (int i = 0; i < 4; i++) acc[i] += As[ty + i * 8][kk] * Bs[kk][tx];
        __syncthreads();
    }
    for (int i = 0; i < 4; i++) {
        int r = bm + ty + i * 8, c = bn + tx;
        float v = acc[i];
        if (MODE == 0) {
            v = v + bf2f(bias[c]);
            float u = 0.7978845608028654f * (v + 0.044715f * v * v * v);
            float g = 0.5f * v * (1.0f + tanhf(u));
            Cb[(size_t)r * N + c] = f2bf(g);
        } else {
            Cf[(size_t)r * N + c] = v;
        }
    }
}

__global__ void k_final(const uint16_t* __restrict__ x, const float2* __restrict__ Z, const float* __restrict__ O,
                        const uint16_t* __restrict__ b2, uint16_t* __restrict__ out) {
    __shared__ float2 zs[DF_];
    __shared__ float2 tw[D_];
    int row = blockIdx.x;
    for (int i = threadIdx.x; i < D_; i += blockDim.x) {
        if (i < DF_) zs[i] = Z[(size_t)i * S_ + row];
        float sn, cs; sincospif(2.0f * i / D_, &sn, &cs); tw[i] = make_float2(cs, sn);
    }
    __syncthreads();
    int d = threadIdx.x;
    if (d < D_) {
        float acc = 0.f; int idx = d;
        for (int k = 1; k < 288; k++) {
            float2 t = tw[idx]; acc += zs[k].x * t.x - zs[k].y * t.y;
            idx += d; if (idx >= D_) idx -= D_;
        }
        float s = (2.0f * acc + zs[0].x + ((d & 1) ? -zs[288].x : zs[288].x)) * (1.0f / D_);
        s = rbf(s);
        float o = rbf(rbf(O[row * D_ + d]) + bf2f(b2[d]));
        float g = rbf(o / (1.0f + expf(-o)));
        float sg = rbf(s * g);
        out[row * D_ + d] = f2bf(bf2f(x[row * D_ + d]) + sg);
    }
}

extern "C" void kernel_launch(void* const* d_in, const int* in_sizes, int n_in,
                              void* d_out, int out_size, void* d_ws, size_t ws_size,
                              hipStream_t stream) {
    const uint16_t* x = (const uint16_t*)d_in[0];
    const uint16_t* wr = (const uint16_t*)d_in[1];
    const uint16_t* wi = (const uint16_t*)d_in[2];
    const uint16_t* w1 = (const uint16_t*)d_in[4];
    const uint16_t* b1 = (const uint16_t*)d_in[5];
    const uint16_t* w2 = (const uint16_t*)d_in[6];
    const uint16_t* b2 = (const uint16_t*)d_in[7];
    char* ws = (char*)d_ws;
    float* xn = (float*)ws; ws += (size_t)S_ * D_ * 4;
    uint16_t* y = (uint16_t*)ws; ws += (size_t)S_ * D_ * 2;
    float2* U = (float2*)ws; ws += (size_t)DF_ * S_ * 8;
    float2* Z = (float2*)ws; ws += (size_t)DF_ * S_ * 8;
    uint16_t* h = (uint16_t*)ws; ws += (size_t)S_ * F_ * 2;
    float* O = (float*)ws; ws += (size_t)S_ * D_ * 4;
    k_ln<<<S_ / 4, 256, 0, stream>>>(x, xn, y);
    k_rowdft<<<S_, 320, 0, stream>>>(xn, U);
    k_col<<<DF_, 1024, 0, stream>>>(U, wr, wi, Z);
    k_gemm<0><<<dim3(F_ / 32, S_ / 32), 256, 0, stream>>>(y, w1, b1, h, nullptr, S_, F_, D_);
    k_gemm<1><<<dim3(D_ / 32, S_ / 32), 256, 0, stream>>>(h, w2, nullptr, nullptr, O, S_, D_, F_);
    k_final<<<S_, 576, 0, stream>>>(x, Z, O, b2, (uint16_t*)d_out);
}
```

```cpp
#include <hip/hip_runtime.h>
#include <stdint.h>
#include <type_traits>

typedef __attribute__((ext_vector_type(8))) __bf16 bf16x8;
typedef __attribute__((ext_vector_type(2))) __bf16 bf16x2;
typedef __attribute__((ext_vector_type(4))) short s16x4;
typedef __attribute__((ext_vector_type(4))) float f32x4;
typedef __attribute__((ext_vector_type(16))) float f32x16;
typedef __attribute__((ext_vector_type(4))) unsigned u32x4;
typedef __attribute__((ext_vector_type(2))) unsigned u32x2;
#define LAS __attribute__((address_space(3)))
#define DI __device__ __forceinline__

constexpr int RING_SLOT = 24576;
constexpr int LDS_BIAS = 6 * RING_SLOT;
constexpr int LDS_JUNK = LDS_BIAS + 2304;
constexpr int LDS_TOTAL = LDS_JUNK + 256;
constexpr int LDS_Y = 3 * RING_SLOT;
constexpr int LDS_H = 0;

DI void glds16(const char* sbase, unsigned voff, unsigned ldsaddr) {
    asm volatile("s_mov_b32 m0, %2\n\ts_nop 0\n\tglobal_load_lds_dwordx4 %0, %1" ::"v"(voff), "s"(sbase), "s"(ldsaddr) : "memory");
}
DI void glds4(const char* sbase, unsigned voff, unsigned ldsaddr) {
    asm volatile("s_mov_b32 m0, %2\n\ts_nop 0\n\tglobal_load_lds_dword %0, %1" ::"v"(voff), "s"(sbase), "s"(ldsaddr) : "memory");
}
#define SB0() __builtin_amdgcn_sched_barrier(0)
#define PIN(x) asm volatile("" : "+v"(x))
DI s16x4 tr_rd(LAS char* p) { return __builtin_amdgcn_ds_read_tr16_b64_v4i16((LAS s16x4*)p); }
DI unsigned pack_bf16(float a, float b) {
    bf16x2 p; p[0] = (__bf16)a; p[1] = (__bf16)b;
    return __builtin_bit_cast(unsigned, p);
}
#define STAMP(i)
#define MFMA32(a, b, c) __builtin_amdgcn_mfma_f32_32x32x16_bf16((a), (b), (c), 0, 0, 0)
template <int N> DI void waitv() { asm volatile("s_waitcnt vmcnt(%0)" ::"i"(N) : "memory"); }
#define WAITL0() asm volatile("s_waitcnt lgkmcnt(0)" ::: "memory")
#define BAR() do { __builtin_amdgcn_s_barrier(); SB0(); } while (0)
#define ROT 1
#define WARM 1
template <int V> using IC = std::integral_constant<int, V>;
template <int A, int B, typename F> DI void static_for(F&& f) {
    if constexpr (A < B) { f(IC<A>{}); static_for<A + 1, B>(f); }
}

__global__ __launch_bounds__(512, 2) void k_mlp(const uint16_t* __restrict__ yf, const uint16_t* __restrict__ w1,
                                                const uint16_t* __restrict__ b1, const uint16_t* __restrict__ w2,
                                                uint16_t* __restrict__ part
                                                ) {
    extern __shared__ __attribute__((aligned(16))) char smem_g[];
    LAS char* smem = (LAS char*)smem_g;
    const int tid = threadIdx.x, lane = tid & 63;
    const int w = __builtin_amdgcn_readfirstlane(tid >> 6);
    const int fs = blockIdx.x & 7, t = blockIdx.x >> 3;
    const unsigned lds0 = (unsigned)(uintptr_t)smem;
    const int rh = w & 1, ct = w >> 1;
    const int hh = lane >> 5;
    STAMP(0);
    const int hb = ROT ? 2 * ((t >> 2) % 5) : 8, r4 = ROT ? (t & 3) : 0;
    int sbk[5];
#pragma unroll
    for (int i = 0; i < 4; i++) { int ii = (i + r4) & 3; sbk[i] = 2 * ii + (2 * ii >= hb ? 1 : 0); }
    sbk[4] = hb;
    const char* w1b = (const char*)w1 + fs * 1152;
    const char* w2b = (const char*)w2 + (size_t)fs * 576 * 1152;
    const unsigned ldsw = lds0 + w * 1024;
    auto mkvo = [&](bool full, unsigned pitch) {
        int ln = tid; PIN(ln); ln &= 63;
        unsigned r = full ? 4 * w + (ln >> 4) : 8 * w + (ln >> 3);
        unsigned c = full ? ((((ln >> 2) & 3) ^ (ln >> 4)) * 64) + (ln & 3) * 16 : ((((ln >> 2) & 1) ^ ((ln >> 4) & 1)) * 64) + (ln & 3) * 16;
        return r * pitch + c;
    };
    unsigned vo = mkvo(true, 9216);
    auto gl = [&](auto U_, auto J_) {
        constexpr int U = decltype(U_)::value, J = decltype(J_)::value;
        constexpr int ph = U / 27, v = U % 27;
        const unsigned dst = ldsw + (U % 6) * RING_SLOT + J * 8192;
        if constexpr (U < 54) {
            if constexpr (ph == 0) {
                if constexpr (v < 24) glds16(w1b + (unsigned)(sbk[v / 6] * 128 + (96 * (v % 6) + 32 * J) * 9216), vo, dst);
                else glds16(w1b + (unsigned)(sbk[4] * 128 + (192 * (v - 24) + 64 * J) * 9216), vo, dst);
            } else {
                if constexpr (v < 24) {
                    constexpr int R = 96 * (v % 6) + 32 * J;
                    glds16(w2b + (unsigned)(sbk[v / 6] * 128 + (64 * sbk[R / 128] + R % 128) * 1152), vo, dst);
                } else {
                    constexpr int R = 192 * (v - 24) + 64 * J;
                    glds16(w2b + (unsigned)(sbk[4] * 128 + (64 * sbk[R / 128] + R % 128) * 1152), vo, dst);
                }
            }
        }
    };
    auto setvo = [&](auto U_) {
        constexpr int U = decltype(U_)::value;
        if constexpr (U == 24) vo = mkvo(false, 9216);
        if constexpr (U == 27) vo = mkvo(true, 1152);
        if constexpr (U == 51) vo = mkvo(false, 1152);
    };

    {
        const char* ysrc = (const char*)yf + (size_t)t * 73728;
#pragma unroll
        for (int j = 0; j < 9; j++) glds16(ysrc + (w + 8 * j) * 1024, lane * 16, lds0 + LDS_Y + (w + 8 * j) * 1024);
        static_for<0, 3>([&](auto U_) { static_for<0, 3>([&](auto J_) { gl(U_, J_); }); });
        int li = w * 21 + (lane % 21); if (li > 161) li = 161;
        int rr = li / 9, cc = li % 9;
        glds4(w1b, WARM ? (18 * t + rr) * 9216 + cc * 128 : 0, lds0 + LDS_JUNK);
        glds4(w2b, WARM ? (18 * t + rr) * 1152 + cc * 128 : 0, lds0 + LDS_JUNK);
        if (tid >= 384 && tid < 384 + 72) {
            int i = tid - 384;
            u32x4 bb = *(const u32x4*)((const char*)b1 + fs * 1152 + i * 16);
            f32x4 lo, hi;
            lo[0] = __uint_as_float(bb[0] << 16); lo[1] = __uint_as_float(bb[0] & 0xffff0000u);
            lo[2] = __uint_as_float(bb[1] << 16); lo[3] = __uint_as_float(bb[1] & 0xffff0000u);
            hi[0] = __uint_as_float(bb[2] << 16); hi[1] = __uint_as_float(bb[2] & 0xffff0000u);
            hi[2] = __uint_as_float(bb[3] << 16); hi[3] = __uint_as_float(bb[3] & 0xffff0000u);
            *(LAS f32x4*)(smem + LDS_BIAS + i * 32) = lo;
            *(LAS f32x4*)(smem + LDS_BIAS + i * 32 + 16) = hi;
        }
        waitv<8>();
        WAITL0();
    }
    BAR();
    STAMP(1);

    LAS char* trc;
    auto settr = [&](auto U_) {
        constexpr int U = decltype(U_)::value, v = U % 27;
        if constexpr (v == 0 || v == 24) {
            int ln = tid; PIN(ln); ln &= 63;
            int G = ln >> 4, q = (ln >> 2) & 3, p = ln & 3;
            if constexpr (v == 0) trc = smem + (4 * (G >> 1) + q) * 256 + ((ct ^ q) * 64) + (G & 1) * 32 + p * 8 + ((U % 6) / 2) * 2 * RING_SLOT;
            else trc = smem + (4 * (G >> 1) + q) * 128 + (((ct & 1) ^ ((q >> 1) & 1)) * 64) + (G & 1) * 32 + p * 8 + ((U % 6) / 2) * 2 * RING_SLOT;
            PIN(trc);
        } else if constexpr (U % 2 == 0) {
            trc += (U % 6 == 0) ? -4 * RING_SLOT : 2 * RING_SLOT;
            PIN(trc);
        }
    };
    bf16x8 af[36];
    {
        LAS char* ab = smem + LDS_Y + rh * 36 * 1024 + lane * 16; PIN(ab);
#pragma unroll
        for (int j = 0; j < 36; j++) af[j] = *(LAS bf16x8*)(ab + j * 1024);
    }
    LAS char* biasp = smem + LDS_BIAS + (32 * ct + 4 * hh) * 4;
    uint16_t* pbase = part + ((size_t)(fs * 32 + t) * 36 + 2 * ct + rh) * 1024;
    unsigned plane = 0;

    f32x16 acc[2];
    s16x4 bl[6], bh[6];
    float gt[4];
    unsigned hq[5][8];
    auto ldbias = [&](auto P_, auto Q_, int blk) {
        constexpr int P = decltype(P_)::value, Q = decltype(Q_)::value;
        f32x4 b = *(LAS f32x4*)(biasp + blk * 256 + Q * 32);
        acc[P][4 * Q] = b[0]; acc[P][4 * Q + 1] = b[1]; acc[P][4 * Q + 2] = b[2]; acc[P][4 * Q + 3] = b[3];
    };
    auto gop = [&](auto C_, auto K_) {
        constexpr int C = decltype(C_)::value, K = decltype(K_)::value, P = C & 1;
        const float c0 = -2.0f * 0.7978845608028654f * 1.4426950408889634f;
        const float c1 = c0 * 0.044715f;
        if constexpr (K < 124) {
            constexpr int Q = K / 31, k = K % 31;
            if constexpr (k < 28) {
                constexpr int st = k / 4, e = k % 4;
                float x = acc[P][4 * Q + e];
                if constexpr (st == 0) gt[e] = x * x;
                else if constexpr (st == 1) gt[e] = __builtin_fmaf(gt[e], c1, c0);
                else if constexpr (st == 2) gt[e] = x * gt[e];
                else if constexpr (st == 3) gt[e] = __builtin_amdgcn_exp2f(gt[e]);
                else if constexpr (st == 4) gt[e] = 1.0f + gt[e];
                else if constexpr (st == 5) gt[e] = __builtin_amdgcn_rcpf(gt[e]);
                else gt[e] = x * gt[e];
                PIN(gt[e]);
            } else if constexpr (k == 28) { hq[C][2 * Q] = pack_bf16(gt[0], gt[1]); PIN(hq[C][2 * Q]); }
            else if constexpr (k == 29) { hq[C][2 * Q + 1] = pack_bf16(gt[2], gt[3]); PIN(hq[C][2 * Q + 1]); }
            else { if constexpr (C + 2 <= 4) ldbias(IC<P>{}, IC<Q>{}, sbk[C + 2]); }
        }
    };
    auto grange = [&](auto C_, auto A_, auto B_) {
        static_for<decltype(A_)::value, decltype(B_)::value>([&](auto K_) { gop(C_, K_); });
    };
    auto pstore = [&](auto P_, auto H_, int blk) {
        constexpr int P = decltype(P_)::value, H = decltype(H_)::value;
        u32x4 o;
        o[0] = pack_bf16(acc[P][8 * H + 0], acc[P][8 * H + 1]); o[1] = pack_bf16(acc[P][8 * H + 2], acc[P][8 * H + 3]);
        o[2] = pack_bf16(acc[P][8 * H + 4], acc[P][8 * H + 5]); o[3] = pack_bf16(acc[P][8 * H + 6], acc[P][8 * H + 7]);
        *(u32x4*)((char*)(pbase + (unsigned)(blk * 4096 + H * 512)) + plane) = o;
    };
    const f32x16 zero16 = {0, 0, 0, 0, 0, 0, 0, 0, 0, 0, 0, 0, 0, 0, 0, 0};
    auto bfrag = [&](auto J_) {
        constexpr int J = decltype(J_)::value;
        return __builtin_bit_cast(bf16x8, __builtin_shufflevector(bl[J], bh[J], 0, 1, 2, 3, 4, 5, 6, 7));
    };
#define SLOTSTAMP()
    auto fslot = [&](auto U_, auto LO_, auto GI_, auto&& fill) {
        constexpr int U = decltype(U_)::value, ph = U / 27, v = U % 27, c = v / 6, s = v % 6;
        constexpr bool LO = decltype(LO_)::value;
        constexpr int PB = (c + ph) & 1;
        constexpr int PA = s == 0 ? PB ^ 1 : PB;
        constexpr int sp = (s + 5) % 6;
        settr(U_);
        LAS char* sb = trc + (U % 2) * RING_SLOT;
        setvo(GI_);
        static_for<0, 6>([&](auto G_) {
            constexpr int G = decltype(G_)::value;
            if constexpr (G < 3) {
                if constexpr (LO) acc[PA] = MFMA32(bfrag(IC<G + 3>{}), af[6 * sp + G + 3], acc[PA]);
            } else {
                acc[PB] = MFMA32(bfrag(IC<G - 3>{}), af[6 * s + G - 3], (ph == 1 && s == 0 && G == 3) ? zero16 : acc[PB]);
            }
            SB0();
            bl[G] = tr_rd(sb + G * 4096); bh[G] = tr_rd(sb + G * 4096 + 2048);
            if constexpr (G < 3) gl(GI_, G_);
            fill(G_);
            SB0();
        });
    };
    auto hslot = [&](auto U_, auto GI_, auto&& fill) {
        constexpr int U = decltype(U_)::value, ph = U / 27, s = U % 27 - 24;
        constexpr int PB = (4 + ph) & 1;
        constexpr int PA = s == 0 ? PB ^ 1 : PB;
        settr(U_);
        LAS char* sb = trc + (U % 2) * RING_SLOT;
        setvo(GI_);
        static_for<0, 12>([&](auto G_) {
            constexpr int G = decltype(G_)::value;
            if constexpr (G < 3) {
                acc[PA] = MFMA32(bfrag(IC<G + 3>{}), af[(s == 0 ? 33 : 12 * s - 3) + G], acc[PA]);
            } else {
                acc[PB] = MFMA32(bfrag(IC<(G - 3) % 6>{}), af[12 * s + G - 3], (ph == 1 && s == 0 && G == 3) ? zero16 : acc[PB]);
            }
            SB0();
            bl[G % 6] = tr_rd(sb + G * 2048); bh[G % 6] = tr_rd(sb + G * 2048 + 1024);
            if constexpr (G < 6 && G % 2 == 0) gl(GI_, IC<G / 2>{});
            fill(G_);
            SB0();
        });
    };
    using T = std::true_type; using F = std::false_type;
    auto nofill = [&](auto) {};

    static_for<0, 4>([&](auto Q_) { ldbias(IC<0>{}, Q_, sbk[0]); ldbias(IC<1>{}, Q_, sbk[1]); });
    static_for<0, 24>([&](auto U_) {
        constexpr int U = decltype(U_)::value, c = U / 6, s = U % 6;
        auto fill = [&](auto G_) {
            constexpr int G = decltype(G_)::value;
            constexpr int gi = 6 * s + G - 4;
            if constexpr (c >= 1 && gi >= 0) grange(IC<(c >= 1 ? c - 1 : 0)>{}, IC<(gi * 124) / 34>{}, IC<((gi + 1) * 124) / 34>{});
            if constexpr (c >= 2 && s == 0 && G < 2) grange(IC<(c >= 2 ? c - 2 : 0)>{}, IC<((32 + G) * 124) / 34>{}, IC<((33 + G) * 124) / 34>{});
            if constexpr (U == 1 && G >= 3) gl(IC<4>{}, IC<G - 3>{});
        };
        if constexpr (U == 0) { fslot(U_, F{}, IC<99>{}, fill); WAITL0(); waitv<5>(); }
        else if constexpr (U == 1) { fslot(U_, T{}, IC<3>{}, fill); waitv<8>(); }
        else if constexpr (U == 2) { fslot(U_, T{}, IC<5>{}, fill); waitv<6>(); }
        else { fslot(U_, T{}, IC<U + 3>{}, fill); waitv<6>(); }
        BAR(); SLOTSTAMP();
    });
    if (w < 4) {
        static_for<24, 27>([&](auto U_) {
            constexpr int U = decltype(U_)::value, s = U - 24;
            hslot(U_, IC<U + 3>{}, [&](auto G_) {
                constexpr int G = decltype(G_)::value;
                constexpr int gi = 12 * s + G - 4;
                if constexpr (s == 0 && G < 2) grange(IC<2>{}, IC<((32 + G) * 124) / 34>{}, IC<((33 + G) * 124) / 34>{});
                if constexpr (gi >= 0) grange(IC<3>{}, IC<(gi * 124) / 32>{}, IC<((gi + 1) * 124) / 32>{});
            });
            if constexpr (U < 26) { waitv<6>(); BAR(); SLOTSTAMP(); }
        });
        static_for<0, 3>([&](auto G_) { constexpr int G = decltype(G_)::value; acc[0] = MFMA32(bfrag(IC<G + 3>{}), af[33 + G], acc[0]); });
    } else {
        static_for<0, 3>([&](auto G_) { constexpr int G = decltype(G_)::value; acc[1] = MFMA32(bfrag(IC<G + 3>{}), af[33 + G], acc[1]); });
        setvo(IC<27>{});
        static_for<0, 3>([&](auto J_) { gl(IC<27>{}, J_); });
        grange(IC<2>{}, IC<(32 * 124) / 34>{}, IC<124>{});
        grange(IC<3>{}, IC<0>{}, IC<62>{});
        waitv<6>(); BAR(); SLOTSTAMP();
        static_for<0, 3>([&](auto J_) { gl(IC<28>{}, J_); });
        grange(IC<3>{}, IC<62>{}, IC<124>{});
        waitv<6>(); BAR(); SLOTSTAMP();
        static_for<0, 3>([&](auto J_) { gl(IC<29>{}, J_); });
    }
    STAMP(2);
    WAITL0();
    BAR();
    SLOTSTAMP();
    {
        waitv<0>();
        int ln = tid; PIN(ln); ln &= 63;
        plane = ln * 16;
        LAS char* ab = smem + rh * 36 * 1024 + ln * 16;
        LAS char* hw = ab + LDS_H + 2 * ct * 1024;
        static_for<0, 4>([&](auto C_) {
            constexpr int C = decltype(C_)::value;
            u32x4 o0 = {hq[C][0], hq[C][1], hq[C][2], hq[C][3]}, o1 = {hq[C][4], hq[C][5], hq[C][6], hq[C][7]};
            *(LAS u32x4*)(hw + C * 8192) = o0; *(LAS u32x4*)(hw + C * 8192 + 1024) = o1;
        });
        if (w < 4) {
            grange(IC<4>{}, IC<0>{}, IC<124>{});
            u32x4 o0 = {hq[4][0], hq[4][1], hq[4][2], hq[4][3]}, o1 = {hq[4][4], hq[4][5], hq[4][6], hq[4][7]};
            *(LAS u32x4*)(hw + 4 * 8192) = o0; *(LAS u32x4*)(hw + 4 * 8192 + 1024) = o1;
        }
        WAITL0();
        BAR();
        SLOTSTAMP();
#pragma unroll
        for (int j = 0; j < 36; j++) af[j] = *(LAS bf16x8*)(ab + LDS_H + j * 1024);
    }
    STAMP(3);
    static_for<27, 51>([&](auto U_) {
        constexpr int U = decltype(U_)::value, v = U - 27, c = v / 6, s = v % 6;
        constexpr int PP = (c + 1 + 1) & 1;
        auto fill = [&](auto G_) {
            constexpr int G = decltype(G_)::value;
            if constexpr (c >= 1 && s == 0 && G == 5) pstore(IC<PP>{}, IC<0>{}, sbk[c >= 1 ? c - 1 : 0]);
            if constexpr (c >= 1 && s == 1 && G == 5) pstore(IC<PP>{}, IC<1>{}, sbk[c >= 1 ? c - 1 : 0]);
            if constexpr (U == 28 && G >= 3) gl(IC<31>{}, IC<G - 3>{});
        };
        constexpr int ns = (c >= 1) ? ((s == 0 ? 1 : 0) + (s == 1 ? 2 : 0) + (s == 2 ? 2 : 0) + (s == 3 ? 1 : 0)) : 0;
        if constexpr (U == 27) { fslot(U_, F{}, IC<99>{}, fill); WAITL0(); }
        else if constexpr (U == 28) { fslot(U_, T{}, IC<30>{}, fill); }
        else if constexpr (U == 29) { fslot(U_, T{}, IC<32>{}, fill); waitv<6>(); }
        else { fslot(U_, T{}, IC<U + 3>{}, fill); waitv<6 + ns>(); }
        BAR(); SLOTSTAMP();
    });
    if (w < 4) {
        static_for<51, 54>([&](auto U_) {
            constexpr int U = decltype(U_)::value, s = U - 51;
            hslot(U_, IC<99>{}, [&](auto G_) {
                constexpr int G = decltype(G_)::value;
                if constexpr (s == 0 && G == 6) pstore(IC<0>{}, IC<0>{}, sbk[3]);
                if constexpr (s == 0 && G == 9) pstore(IC<0>{}, IC<1>{}, sbk[3]);
            });
            if constexpr (U == 51) { waitv<5>(); BAR(); SLOTSTAMP(); }
            if constexpr (U == 52) { waitv<2>(); BAR(); SLOTSTAMP(); }
        });
        static_for<0, 3>([&](auto G_) { constexpr int G = decltype(G_)::value; acc[1] = MFMA32(bfrag(IC<G + 3>{}), af[33 + G], acc[1]); });
        pstore(IC<1>{}, IC<0>{}, sbk[4]);
        pstore(IC<1>{}, IC<1>{}, sbk[4]);
    } else {
        static_for<0, 3>([&](auto G_) { constexpr int G = decltype(G_)::value; acc[0] = MFMA32(bfrag(IC<G + 3>{}), af[33 + G], acc[0]); });
        waitv<3>(); BAR(); SLOTSTAMP();
        waitv<0>(); BAR(); SLOTSTAMP();
        pstore(IC<0>{}, IC<0>{}, sbk[3]);
        pstore(IC<0>{}, IC<1>{}, sbk[3]);
    }
    STAMP(4);
}
#define MFMA16(a, b, c) __builtin_amdgcn_mfma_f32_16x16x32_bf16((a), (b), (c), 0, 0, 0)
DI float bf_lo(unsigned u) { return __uint_as_float(u << 16); }
DI float bf_hi(unsigned u) { return __uint_as_float(u & 0xffff0000u); }
DI float rbf(float f) { return bf_lo(pack_bf16(f, 0.f)); }
DI void split2(float a, float b, unsigned& hi, unsigned& lo) {
    hi = pack_bf16(a, b);
    lo = pack_bf16(a - bf_lo(hi), b - bf_hi(hi));
}
DI bf16x8 mk8(unsigned a, unsigned b, unsigned c, unsigned d) { u32x4 v = {a, b, c, d}; return __builtin_bit_cast(bf16x8, v); }
DI int rho(int a, int hh) { return (a & 3) + 8 * (a >> 2) + 4 * hh; }

struct Dft24 {
    bf16x8 Ph[3], Pl[3], Qh[3], Ql[3];
    float twc[12], tws[12];
};
DI void dft24_setup(Dft24& m, int lane) {
    const int col = lane & 31, hh = lane >> 5;
    float c[12], s[12];
#pragma unroll
    for (int a = 0; a < 12; a++) {
        int pr = rho(a, hh) * col;
        float ang = (float)(pr % 24) * (1.0f / 24.0f);
        c[a] = __builtin_amdgcn_cosf(ang); s[a] = __builtin_amdgcn_sinf(ang);
        float an2 = (float)pr * (1.0f / 576.0f);
        m.twc[a] = __builtin_amdgcn_cosf(an2); m.tws[a] = __builtin_amdgcn_sinf(an2);
    }
    unsigned ch[6], cl[6], sh[6], sl[6];
#pragma unroll
    for (int i = 0; i < 6; i++) { split2(c[2 * i], c[2 * i + 1], ch[i], cl[i]); split2(s[2 * i], s[2 * i + 1], sh[i], sl[i]); }
    const unsigned N = 0x80008000u;
    m.Ph[0] = mk8(ch[0], ch[1], ch[2], ch[3]); m.Ph[1] = mk8(ch[4], ch[5], sh[0], sh[1]); m.Ph[2] = mk8(sh[2], sh[3], sh[4], sh[5]);
    m.Pl[0] = mk8(cl[0], cl[1], cl[2], cl[3]); m.Pl[1] = mk8(cl[4], cl[5], sl[0], sl[1]); m.Pl[2] = mk8(sl[2], sl[3], sl[4], sl[5]);
    m.Qh[0] = mk8(sh[0] ^ N, sh[1] ^ N, sh[2] ^ N, sh[3] ^ N); m.Qh[1] = mk8(sh[4] ^ N, sh[5] ^ N, ch[0], ch[1]); m.Qh[2] = mk8(ch[2], ch[3], ch[4], ch[5]);
    m.Ql[0] = mk8(sl[0] ^ N, sl[1] ^ N, sl[2] ^ N, sl[3] ^ N); m.Ql[1] = mk8(sl[4] ^ N, sl[5] ^ N, cl[0], cl[1]); m.Ql[2] = mk8(cl[2], cl[3], cl[4], cl[5]);
}
template <bool ASA>
DI void dft24_stage(const Dft24& m, float (&re)[12], float (&im)[12]) {
    unsigned h[12], l[12];
#pragma unroll
    for (int i = 0; i < 6; i++) { split2(re[2 * i], re[2 * i + 1], h[i], l[i]); split2(im[2 * i], im[2 * i + 1], h[6 + i], l[6 + i]); }
    f32x16 dr = {0, 0, 0, 0, 0, 0, 0, 0, 0, 0, 0, 0, 0, 0, 0, 0}, di = dr;
#pragma unroll
    for (int s = 0; s < 3; s++) {
        bf16x8 xh = mk8(h[4 * s], h[4 * s + 1], h[4 * s + 2], h[4 * s + 3]);
        bf16x8 xl = mk8(l[4 * s], l[4 * s + 1], l[4 * s + 2], l[4 * s + 3]);
        if (ASA) {
            dr = MFMA32(xl, m.Ph[s], dr); di = MFMA32(xl, m.Qh[s], di);
            dr = MFMA32(xh, m.Pl[s], dr); di = MFMA32(xh, m.Ql[s], di);
            dr = MFMA32(xh, m.Ph[s], dr); di = MFMA32(xh, m.Qh[s], di);
        } else {
            dr = MFMA32(m.Ph[s], xl, dr); di = MFMA32(m.Qh[s], xl, di);
            dr = MFMA32(m.Pl[s], xh, dr); di = MFMA32(m.Ql[s], xh, di);
            dr = MFMA32(m.Ph[s], xh, dr); di = MFMA32(m.Qh[s], xh, di);
        }
    }
#pragma unroll
    for (int a = 0; a < 12; a++) { re[a] = dr[a]; im[a] = di[a]; }
}
DI void fft576(const Dft24& m, float (&re)[12], float (&im)[12]) {
    dft24_stage<true>(m, re, im);
#pragma unroll
    for (int a = 0; a < 12; a++) {
        float r = re[a] * m.twc[a] + im[a] * m.tws[a];
        float i = im[a] * m.twc[a] - re[a] * m.tws[a];
        re[a] = r; im[a] = i;
    }
    dft24_stage<false>(m, re, im);
}

struct Dft16 { bf16x8 Ph, Pl, Qh, Ql; float twc[4], tws[4]; };
DI void dft16_setup(Dft16& m, int lane) {
    const int col = lane & 15, g = lane >> 4;
    float c[4], s[4];
#pragma unroll
    for (int e = 0; e < 4; e++) {
        int pr = (4 * g + e) * col;
        float ang = (float)(pr & 15) * (1.0f / 16.0f);
        c[e] = __builtin_amdgcn_cosf(ang); s[e] = __builtin_amdgcn_sinf(ang);
        float an2 = (float)pr * (1.0f / 256.0f);
        m.twc[e] = __builtin_amdgcn_cosf(an2); m.tws[e] = __builtin_amdgcn_sinf(an2);
    }
    unsigned ch[2], cl[2], sh[2], sl[2];
    split2(c[0], c[1], ch[0], cl[0]); split2(c[2], c[3], ch[1], cl[1]);
    split2(s[0], s[1], sh[0], sl[0]); split2(s[2], s[3], sh[1], sl[1]);
    const unsigned N = 0x80008000u;
    m.Ph = mk8(ch[0], ch[1], sh[0], sh[1]); m.Pl = mk8(cl[0], cl[1], sl[0], sl[1]);
    m.Qh = mk8(sh[0] ^ N, sh[1] ^ N, ch[0], ch[1]); m.Ql = mk8(sl[0] ^ N, sl[1] ^ N, cl[0], cl[1]);
}
template <bool ASA>
DI void dft16_stage(const Dft16& m, float (&re)[4], float (&im)[4]) {
    unsigned h[4], l[4];
    split2(re[0], re[1], h[0], l[0]); split2(re[2], re[3], h[1], l[1]);
    split2(im[0], im[1], h[2], l[2]); split2(im[2], im[3], h[3], l[3]);
    bf16x8 xh = mk8(h[0], h[1], h[2], h[3]), xl = mk8(l[0], l[1], l[2], l[3]);
    f32x4 dr = {0, 0, 0, 0}, di = dr;
    if (ASA) {
        dr = MFMA16(xl, m.Ph, dr); di = MFMA16(xl, m.Qh, di);
        dr = MFMA16(xh, m.Pl, dr); di = MFMA16(xh, m.Ql, di);
        dr = MFMA16(xh, m.Ph, dr); di = MFMA16(xh, m.Qh, di);
    } else {
        dr = MFMA16(m.Ph, xl, dr); di = MFMA16(m.Qh, xl, di);
        dr = MFMA16(m.Pl, xh, dr); di = MFMA16(m.Ql, xh, di);
        dr = MFMA16(m.Ph, xh, dr); di = MFMA16(m.Qh, xh, di);
    }
#pragma unroll
    for (int e = 0; e < 4; e++) { re[e] = dr[e]; im[e] = di[e]; }
}
DI void cmul_neg(float (&re)[4], float (&im)[4], const float (&c)[4], const float (&s)[4]) {
#pragma unroll
    for (int e = 0; e < 4; e++) {
        float r = re[e] * c[e] + im[e] * s[e];
        float i = im[e] * c[e] - re[e] * s[e];
        re[e] = r; im[e] = i;
    }
}

DI float wave_sum48(float v) {
#pragma unroll
    for (int o = 32; o > 0; o >>= 1) v += __shfl_xor(v, o);
    return v;
}

__global__ __launch_bounds__(512) void k_pre(const uint16_t* __restrict__ x, uint16_t* __restrict__ yf, float2* __restrict__ Zv) {
    __shared__ __attribute__((aligned(16))) uint16_t xs[576 * 8];
    const int tid = threadIdx.x, lane = tid & 63;
    const int w = __builtin_amdgcn_readfirstlane(tid >> 6);
    const int j = blockIdx.x;
    Dft24 m;
    {
        const int row = j + 256 * w;
        const bool act = lane < 48;
        float v[12];
#pragma unroll
        for (int n = 0; n < 3; n++) {
            u32x2 q = {0, 0};
            if (act) q = *(const u32x2*)(x + (size_t)row * 576 + (lane + 48 * n) * 4);
            v[4 * n] = bf_lo(q[0]); v[4 * n + 1] = bf_hi(q[0]); v[4 * n + 2] = bf_lo(q[1]); v[4 * n + 3] = bf_hi(q[1]);
        }
        if (w < 4) dft24_setup(m, lane);
#pragma unroll
        for (int pass = 0; pass < 2; pass++) {
            float s = 0.f;
#pragma unroll
            for (int e = 0; e < 12; e++) s += v[e];
            s = wave_sum48(act ? s : 0.f);
            float mean = s * (1.0f / 576.0f);
            float q = 0.f;
#pragma unroll
            for (int e = 0; e < 12; e++) { float d = v[e] - mean; q += d * d; }
            q = wave_sum48(act ? q : 0.f);
            float var = rbf(q * (1.0f / 576.0f));
            float mb = rbf(mean);
            float sd = rbf(__builtin_sqrtf(rbf(var + 1e-5f)));
            float rs = 1.0f / sd;
#pragma unroll
            for (int e = 0; e < 12; e++) v[e] = rbf(rbf(v[e] - mb) * rs);
            if (pass == 0) {
                const int pos = (w == 0) ? 0 : (w == 4) ? 1 : (w == 2) ? 2 : (w == 6) ? 3 : (w == 1) ? 4 : (w == 3) ? 5 : (w == 5) ? 6 : 7;
                if (act) {
#pragma unroll
                    for (int n = 0; n < 3; n++)
#pragma unroll
                        for (int e = 0; e < 4; e++) xs[((lane + 48 * n) * 4 + e) * 8 + pos] = (uint16_t)(__float_as_uint(v[4 * n + e]) >> 16);
                }
            } else if (act) {
                const int t = j >> 3, mloc = 8 * (j & 7) + w;
#pragma unroll
                for (int n = 0; n < 3; n++) {
                    int Q = lane + 48 * n;
                    int ks = Q >> 2, hh = Q & 1, half = (Q >> 1) & 1;
                    u32x2 o; o[0] = pack_bf16(v[4 * n], v[4 * n + 1]); o[1] = pack_bf16(v[4 * n + 2], v[4 * n + 3]);
                    *(u32x2*)(yf + ((((size_t)(t * 2 + (mloc >> 5)) * 36 + ks) * 64 + (mloc & 31) + 32 * hh) * 8 + half * 4)) = o;
                }
            }
        }
    }
    __syncthreads();
    if (w >= 4) return;
    const unsigned PP = 0x3F803F80u, PM = 0xBF803F80u  , MP = 0x3F80BF80u  , MM = 0xBF80BF80u;
    unsigned A0, A1, A2, A3, B0, B1, B2, B3; float cc;
    const float c8 = 0.70710678118654752f;
    if (w == 0) { A0 = PP; A1 = PP; A2 = PP; A3 = PP; B0 = PP; B1 = PP; B2 = MM; B3 = MM; cc = 1.f; }
    else if (w == 1) { A0 = PM; A1 = 0; A2 = PM; A3 = MP; B0 = 0; B1 = MP; B2 = MM; B3 = PP; cc = c8; }
    else if (w == 2) { A0 = PP; A1 = MM; A2 = 0; A3 = 0; B0 = 0; B1 = 0; B2 = MP; B3 = MP; cc = 1.f; }
    else { A0 = PM; A1 = 0; A2 = MP; A3 = PM; B0 = 0; B1 = PM; B2 = MM; B3 = PP; cc = c8; }
    const int hh = lane >> 5; int n2 = lane & 31; if (n2 > 23) n2 = 23;
    float re[12], im[12];
    auto dot = [](unsigned a, unsigned b, float c) { return __builtin_amdgcn_fdot2_f32_bf16(__builtin_bit_cast(bf16x2, a), __builtin_bit_cast(bf16x2, b), c, false); };
#pragma unroll
    for (int a = 0; a < 12; a++) {
        u32x4 p = *(const u32x4*)(xs + (24 * rho(a, hh) + n2) * 8);
        re[a] = __builtin_fmaf(dot(p[2], A2, dot(p[3], A3, 0.f)), cc, dot(p[0], A0, dot(p[1], A1, 0.f)));
        im[a] = __builtin_fmaf(dot(p[2], B2, dot(p[3], B3, 0.f)), cc, dot(p[0], B0, dot(p[1], B1, 0.f)));
    }
    fft576(m, re, im);
    if ((lane & 31) < 24) {
        float2* dst = Zv + ((size_t)j * 4 + w) * 576 + (lane & 31);
#pragma unroll
        for (int a = 0; a < 12; a++) dst[24 * rho(a, hh)] = make_float2(re[a], im[a]);
    }
}

__global__ __launch_bounds__(320) void k_col(const float2* __restrict__ Zv, const uint16_t* __restrict__ wr, const uint16_t* __restrict__ wi,
                                             float2* __restrict__ Vp) {
    const int tid = threadIdx.x, lane = tid & 63;
    const int w = __builtin_amdgcn_readfirstlane(tid >> 6);
    const int r = blockIdx.x & 7, grp = blockIdx.x >> 3;
    const int n2 = lane & 15, g = lane >> 4;
    float re[2][4], im[2][4], wre[2][4], wim[2][4];
    const float sc = 1.0f / (2048.0f * 576.0f);
#pragma unroll
    for (int c = 0; c < 2; c++) {
        const int k = 9 * grp + 2 * w + c;
        const int km = k == 0 ? 0 : 576 - k;
#pragma unroll
        for (int e = 0; e < 4; e++) {
            const int jj = 64 * g + 16 * e + n2;
            const float2* row = Zv + (size_t)jj * 4 * 576;
            if (r >= 1 && r <= 3) { float2 a = row[r * 576 + k]; re[c][e] = a.x; im[c][e] = a.y; }
            else if (r >= 5) { float2 b = row[(8 - r) * 576 + km]; re[c][e] = b.x; im[c][e] = -b.y; }
            else {
                float2 a = row[k], b = row[km];
                if (r == 0) { re[c][e] = 0.5f * (a.x + b.x); im[c][e] = 0.5f * (a.y - b.y); }
                else { re[c][e] = 0.5f * (a.y + b.y); im[c][e] = 0.5f * (b.x - a.x); }
            }
            const int q = 8 * (n2 + 16 * (4 * g + e)) + r;
            wre[c][e] = bf_lo(wr[q * 289 + k]) * sc; wim[c][e] = bf_lo(wi[q * 289 + k]) * sc;
        }
    }
    Dft16 m; dft16_setup(m, lane);
    float jc[4], js[4];
#pragma unroll
    for (int e = 0; e < 4; e++) {
        float ang = (float)((64 * g + 16 * e + n2) * r) * (1.0f / 2048.0f);
        jc[e] = __builtin_amdgcn_cosf(ang); js[e] = __builtin_amdgcn_sinf(ang);
    }
#pragma unroll
    for (int c = 0; c < 2; c++) {
        cmul_neg(re[c], im[c], jc, js);
        dft16_stage<true>(m, re[c], im[c]);
        cmul_neg(re[c], im[c], m.twc, m.tws);
        dft16_stage<false>(m, re[c], im[c]);
#pragma unroll
        for (int e = 0; e < 4; e++) {
            float a = re[c][e] * wre[c][e] - im[c][e] * wim[c][e];
            float b = -(re[c][e] * wim[c][e] + im[c][e] * wre[c][e]);
            re[c][e] = a; im[c][e] = b;
        }
        dft16_stage<true>(m, re[c], im[c]);
        cmul_neg(re[c], im[c], m.twc, m.tws);
        dft16_stage<false>(m, re[c], im[c]);
        cmul_neg(re[c], im[c], jc, js);
    }
#pragma unroll
    for (int e = 0; e < 4; e++) {
        const int jj = 64 * g + 16 * e + n2;
        f32x4 o = {re[0][e], im[0][e], re[1][e], im[1][e]};
        *(f32x4*)(Vp + ((size_t)(r * 32 + grp) * 256 + jj) * 10 + 2 * w) = o;
    }
}

__global__ __launch_bounds__(576) void k_fin(const uint16_t* __restrict__ x, const float2* __restrict__ Vp, const uint16_t* __restrict__ part,
                                             const uint16_t* __restrict__ b2, uint16_t* __restrict__ out) {
    __shared__ __attribute__((aligned(16))) float2 cb[4 * 576];
    const int tid = threadIdx.x, lane = tid & 63;
    const int w = __builtin_amdgcn_readfirstlane(tid >> 6);
    const int j = blockIdx.x, t = j >> 3, fs = j & 7;
    const int i = tid & 7, pc = tid >> 3, ehh = pc & 1, H = (pc >> 1) & 1, ctg = pc >> 2;
    const int mloc = 8 * fs + i;
    u32x4 pv[8];
#pragma unroll
    for (int f = 0; f < 8; f++)
        pv[f] = *(const u32x4*)(part + ((((size_t)(f * 32 + t) * 18 + ctg) * 2 + (mloc >> 5)) * 2 + H) * 512 + ((mloc & 31) + 32 * ehh) * 8);
    const int d0 = 32 * ctg + 16 * H + 4 * ehh;
    const size_t rowoff = (size_t)(j + 256 * i) * 576;
    u32x2 xa = *(const u32x2*)(x + rowoff + d0), xb = *(const u32x2*)(x + rowoff + d0 + 8);
    u32x2 ba = *(const u32x2*)(b2 + d0), bb = *(const u32x2*)(b2 + d0 + 8);
    const int hh = lane >> 5;
    float re[12], im[12];
    if (w < 4) {
        int n2 = lane & 31; if (n2 > 23) n2 = 23;
        auto G = [&](int r, int k) { int gq = k / 9; if (gq > 31) gq = 31; return Vp[((size_t)(r * 32 + gq) * 256 + j) * 10 + (k - 9 * gq)]; };
#pragma unroll
        for (int a = 0; a < 12; a++) {
            const int k = 24 * rho(a, hh) + n2;
            const bool mir = k > 288; const int kk = mir ? 576 - k : k;
            const bool spec = (k == 0 || k == 288);
            if (w == 0) {
                float2 A = G(0, kk), B = G(4, kk);
                if (mir) { A.y = -A.y; B.y = -B.y; }
                if (spec) { A.y = 0.f; B.y = 0.f; }
                re[a] = A.x + B.y; im[a] = A.y - B.x;
            } else {
                float2 A = G(mir ? 8 - w : w, kk);
                if (mir) A.y = -A.y;
                if (a == 0 || a == 4) {
                    float2 B = G(8 - w, kk);
                    if (spec) { A.x = 0.5f * (A.x + B.x); A.y = 0.5f * (A.y - B.y); }
                }
                re[a] = A.x; im[a] = A.y;
            }
        }
    }
    float o[8];
#pragma unroll
    for (int e = 0; e < 4; e++) {
        float lo = 0.f, hi = 0.f;
#pragma unroll
        for (int f = 0; f < 8; f++) { lo += bf_lo(pv[f][e]); hi += bf_hi(pv[f][e]); }
        o[2 * e] = lo; o[2 * e + 1] = hi;
        PIN(o[2 * e]); PIN(o[2 * e + 1]);
    }
    if (w < 4) {
        Dft24 m; dft24_setup(m, lane);
        fft576(m, re, im);
        if ((lane & 31) < 24) {
#pragma unroll
            for (int a = 0; a < 12; a++) cb[w * 576 + (lane & 31) + 24 * rho(a, hh)] = make_float2(re[a], -im[a]);
        }
    }
    float cr[4], ci[4];
    cr[0] = 1.f; ci[0] = (i & 1) ? -1.f : 1.f;
#pragma unroll
    for (int r = 1; r < 4; r++) {
        float ang = (float)((i * r) & 7) * 0.125f;
        cr[r] = 2.f * __builtin_amdgcn_cosf(ang); ci[r] = -2.f * __builtin_amdgcn_sinf(ang);
    }
    __syncthreads();
    float xv[8] = {bf_lo(xa[0]), bf_hi(xa[0]), bf_lo(xa[1]), bf_hi(xa[1]), bf_lo(xb[0]), bf_hi(xb[0]), bf_lo(xb[1]), bf_hi(xb[1])};
    float bv[8] = {bf_lo(ba[0]), bf_hi(ba[0]), bf_lo(ba[1]), bf_hi(ba[1]), bf_lo(bb[0]), bf_hi(bb[0]), bf_lo(bb[1]), bf_hi(bb[1])};
    float res[8];
#pragma unroll
    for (int e = 0; e < 8; e++) {
        const int d = d0 + (e & 3) + 8 * (e >> 2);
        float s = 0.f;
#pragma unroll
        for (int r = 0; r < 4; r++) { float2 c = cb[r * 576 + d]; s += cr[r] * c.x + ci[r] * c.y; }
        s = rbf(s);
        float ov = rbf(rbf(o[e]) + bv[e]);
        float gv = rbf(ov * __builtin_amdgcn_rcpf(1.0f + __builtin_amdgcn_exp2f(-1.4426950408889634f * ov)));
        res[e] = xv[e] + rbf(s * gv);
    }
    u32x2 oa = {pack_bf16(res[0], res[1]), pack_bf16(res[2], res[3])}, ob = {pack_bf16(res[4], res[5]), pack_bf16(res[6], res[7])};
    *(u32x2*)(out + rowoff + d0) = oa;
    *(u32x2*)(out + rowoff + d0 + 8) = ob;
}

extern "C" void kernel_launch(void* const* d_in, const int* in_sizes, int n_in,
                              void* d_out, int out_size, void* d_ws, size_t ws_size,
                              hipStream_t stream) {
    const uint16_t* x = (const uint16_t*)d_in[0];
    const uint16_t* wr = (const uint16_t*)d_in[1];
    const uint16_t* wi = (const uint16_t*)d_in[2];
    const uint16_t* w1 = (const uint16_t*)d_in[4];
    const uint16_t* b1 = (const uint16_t*)d_in[5];
    const uint16_t* w2 = (const uint16_t*)d_in[6];
    const uint16_t* b2 = (const uint16_t*)d_in[7];
    char* ws = (char*)d_ws;
    uint16_t* yf = (uint16_t*)ws; ws += (size_t)2048 * 576 * 2;
    float2* Zv = (float2*)ws; ws += (size_t)256 * 4 * 576 * 8;
    float2* Vp = (float2*)ws; ws += (size_t)8 * 32 * 256 * 80;
    uint16_t* part = (uint16_t*)ws;
    (void)hipFuncSetAttribute((const void*)k_mlp, hipFuncAttributeMaxDynamicSharedMemorySize, LDS_TOTAL);
    k_pre<<<256, 512, 0, stream>>>(x, yf, Zv);
    k_col<<<256, 320, 0, stream>>>(Zv, wr, wi, Vp);
    k_mlp<<<256, 512, LDS_TOTAL, stream>>>(yf, w1, b1, w2, part);
    k_fin<<<256, 576, 0, stream>>>(x, Vp, part, b2, (uint16_t*)d_out);
}
```

```cpp
#include <hip/hip_runtime.h>
#include <stdint.h>
#include <type_traits>

typedef __attribute__((ext_vector_type(8))) __bf16 bf16x8;
typedef __attribute__((ext_vector_type(2))) __bf16 bf16x2;
typedef __attribute__((ext_vector_type(8))) _Float16 f16x8;
typedef __attribute__((ext_vector_type(2))) _Float16 f16x2;
typedef __attribute__((ext_vector_type(4))) short s16x4;
typedef __attribute__((ext_vector_type(2))) float f32x2;
typedef __attribute__((ext_vector_type(4))) float f32x4;
typedef __attribute__((ext_vector_type(16))) float f32x16;
typedef __attribute__((ext_vector_type(4))) unsigned u32x4;
typedef __attribute__((ext_vector_type(2))) unsigned u32x2;
typedef __attribute__((ext_vector_type(2))) unsigned short u16x2;
typedef unsigned long long u64;
#define LAS __attribute__((address_space(3)))
#define GAS __attribute__((address_space(1)))
#define DI __device__ __forceinline__

constexpr int RING_SLOT = 24576;
constexpr int LDS_Y = 3 * RING_SLOT;
constexpr int LDS_H = 0;
constexpr int LDS_CB = 0;
constexpr int LDS_BIAS = 6 * RING_SLOT;
constexpr int LDS_JUNK = LDS_BIAS + 2304;
constexpr int LDS_TAG = LDS_JUNK + 256;
constexpr int LDS_XS = LDS_TAG + 16;
constexpr int LDS_TOTAL = LDS_XS + 8 * 1152 + 64;
constexpr unsigned WS_EPOCH = 0;
constexpr unsigned WS_ROWF = 256;
constexpr unsigned WS_COLF = 4096;
constexpr unsigned WS_PARTF = 8192;
constexpr unsigned ZV_PITCH = 580 * 8;
constexpr unsigned WS_ZV = 16384;
constexpr unsigned WS_VP = WS_ZV + 256 * 4 * ZV_PITCH;
constexpr unsigned WS_PART = WS_VP + 8 * 32 * 256 * 96;
constexpr unsigned WS_END = WS_PART + 8u * 2048 * 576 * 2;

template <int IMM>
DI void glds16(const char* sbase, unsigned voff, unsigned ldsbase) {
    asm volatile("s_add_u32 m0, %2, %3\n\ts_nop 0\n\tglobal_load_lds_dwordx4 %0, %1" ::"v"(voff), "s"(sbase), "s"(ldsbase), "i"(IMM) : "memory", "scc");
}
DI void glds4(const char* sbase, unsigned voff, unsigned ldsaddr) {
    asm volatile("s_mov_b32 m0, %2\n\ts_nop 0\n\tglobal_load_lds_dword %0, %1" ::"v"(voff), "s"(sbase), "s"(ldsaddr) : "memory");
}
#define SB0() __builtin_amdgcn_sched_barrier(0)
#define PIN(x) asm volatile("" : "+v"(x))
DI s16x4 tr_rd(LAS char* p) { return __builtin_amdgcn_ds_read_tr16_b64_v4i16((LAS s16x4*)p); }
DI unsigned pack_bf16(float a, float b) { bf16x2 p; p[0] = (__bf16)a; p[1] = (__bf16)b; return __builtin_bit_cast(unsigned, p); }
DI unsigned pack_f16(float a, float b) { f16x2 p; p[0] = (_Float16)a; p[1] = (_Float16)b; return __builtin_bit_cast(unsigned, p); }
DI float bf_lo(unsigned u) { return __uint_as_float(u << 16); }
DI float bf_hi(unsigned u) { return __uint_as_float(u & 0xffff0000u); }
DI float rbf(float f) { return bf_lo(pack_bf16(f, 0.f)); }
#define STAMP(i)
#define MFMA32(a, b, c) __builtin_amdgcn_mfma_f32_32x32x16_bf16((a), (b), (c), 0, 0, 0)
#define MFMA32H(a, b, c) __builtin_amdgcn_mfma_f32_32x32x16_f16((a), (b), (c), 0, 0, 0)
#define MFMA16(a, b, c) __builtin_amdgcn_mfma_f32_16x16x32_bf16((a), (b), (c), 0, 0, 0)
template <int N> DI void waitv() { asm volatile("s_waitcnt vmcnt(%0)" ::"i"(N) : "memory"); }
#define WAITL0() asm volatile("s_waitcnt lgkmcnt(0)" ::: "memory")
#define BAR() do { __builtin_amdgcn_s_barrier(); SB0(); } while (0)
#define RLX_AGENT __ATOMIC_RELAXED, __HIP_MEMORY_SCOPE_AGENT
template <int V> using IC = std::integral_constant<int, V>;
template <int A, int B, typename F> DI void static_for(F&& f) {
    if constexpr (A < B) { f(IC<A>{}); static_for<A + 1, B>(f); }
}
DI void split2(float a, float b, unsigned& hi, unsigned& lo) {
    hi = pack_bf16(a, b);
    lo = pack_bf16(a - bf_lo(hi), b - bf_hi(hi));
}
DI bf16x8 mk8(unsigned a, unsigned b, unsigned c, unsigned d) { u32x4 v = {a, b, c, d}; return __builtin_bit_cast(bf16x8, v); }
DI f16x8 mk8h(unsigned a, unsigned b, unsigned c, unsigned d) { u32x4 v = {a, b, c, d}; return __builtin_bit_cast(f16x8, v); }
DI int rho(int a, int hh) { return (a & 3) + 8 * (a >> 2) + 4 * hh; }
DI float red8(float v) {
    v += __builtin_bit_cast(float, __builtin_amdgcn_update_dpp(0, __builtin_bit_cast(int, v), 0x128, 0xf, 0xf, false));
    { auto r = __builtin_amdgcn_permlane16_swap(__float_as_uint(v), __float_as_uint(v), false, false); v = __uint_as_float(r[0]) + __uint_as_float(r[1]); }
    { auto r = __builtin_amdgcn_permlane32_swap(__float_as_uint(v), __float_as_uint(v), false, false); v = __uint_as_float(r[0]) + __uint_as_float(r[1]); }
    return v;
}

struct Dft24 { bf16x8 Ph[3], Pl[3], Qh[3], Ql[3]; };
DI void dft24_setup(Dft24& m, int lane) {
    const int col = lane & 31, hh = lane >> 5;
    float c[12], s[12];
#pragma unroll
    for (int a = 0; a < 12; a++) {
        float ang = (float)((rho(a, hh) * col) % 24) * (1.0f / 24.0f);
        c[a] = __builtin_amdgcn_cosf(ang); s[a] = __builtin_amdgcn_sinf(ang);
    }
    unsigned ch[6], cl[6], sh[6], sl[6];
#pragma unroll
    for (int i = 0; i < 6; i++) { split2(c[2 * i], c[2 * i + 1], ch[i], cl[i]); split2(s[2 * i], s[2 * i + 1], sh[i], sl[i]); }
    const unsigned N = 0x80008000u;
    m.Ph[0] = mk8(ch[0], ch[1], ch[2], ch[3]); m.Ph[1] = mk8(ch[4], ch[5], sh[0], sh[1]); m.Ph[2] = mk8(sh[2], sh[3], sh[4], sh[5]);
    m.Pl[0] = mk8(cl[0], cl[1], cl[2], cl[3]); m.Pl[1] = mk8(cl[4], cl[5], sl[0], sl[1]); m.Pl[2] = mk8(sl[2], sl[3], sl[4], sl[5]);
    m.Qh[0] = mk8(sh[0] ^ N, sh[1] ^ N, sh[2] ^ N, sh[3] ^ N); m.Qh[1] = mk8(sh[4] ^ N, sh[5] ^ N, ch[0], ch[1]); m.Qh[2] = mk8(ch[2], ch[3], ch[4], ch[5]);
    m.Ql[0] = mk8(sl[0] ^ N, sl[1] ^ N, sl[2] ^ N, sl[3] ^ N); m.Ql[1] = mk8(sl[4] ^ N, sl[5] ^ N, cl[0], cl[1]); m.Ql[2] = mk8(cl[2], cl[3], cl[4], cl[5]);
}
template <bool ASA>
DI void dft24_stage(const Dft24& m, float (&re)[12], float (&im)[12]) {
    unsigned h[12], l[12];
#pragma unroll
    for (int i = 0; i < 6; i++) { split2(re[2 * i], re[2 * i + 1], h[i], l[i]); split2(im[2 * i], im[2 * i + 1], h[6 + i], l[6 + i]); }
    f32x16 dr = {0, 0, 0, 0, 0, 0, 0, 0, 0, 0, 0, 0, 0, 0, 0, 0}, di = dr;
#pragma unroll
    for (int s = 0; s < 3; s++) {
        bf16x8 xh = mk8(h[4 * s], h[4 * s + 1], h[4 * s + 2], h[4 * s + 3]);
        bf16x8 xl = mk8(l[4 * s], l[4 * s + 1], l[4 * s + 2], l[4 * s + 3]);
        if (ASA) {
            dr = MFMA32(xl, m.Ph[s], dr); di = MFMA32(xl, m.Qh[s], di);
            dr = MFMA32(xh, m.Pl[s], dr); di = MFMA32(xh, m.Ql[s], di);
            dr = MFMA32(xh, m.Ph[s], dr); di = MFMA32(xh, m.Qh[s], di);
        } else {
            dr = MFMA32(m.Ph[s], xl, dr); di = MFMA32(m.Qh[s], xl, di);
            dr = MFMA32(m.Pl[s], xh, dr); di = MFMA32(m.Ql[s], xh, di);
            dr = MFMA32(m.Ph[s], xh, dr); di = MFMA32(m.Qh[s], xh, di);
        }
    }
#pragma unroll
    for (int a = 0; a < 12; a++) { re[a] = dr[a]; im[a] = di[a]; }
}
DI void tw576(float (&re)[12], float (&im)[12], int lane) {
    const int col = lane & 31, hh = lane >> 5;
#pragma unroll
    for (int a = 0; a < 12; a++) {
        float an = (float)(rho(a, hh) * col) * (1.0f / 576.0f);
        float c = __builtin_amdgcn_cosf(an), s = __builtin_amdgcn_sinf(an);
        float r = re[a] * c + im[a] * s;
        float i = im[a] * c - re[a] * s;
        re[a] = r; im[a] = i;
    }
}
struct Dft24h { f16x8 P[3], Q[3]; };
DI void dft24h_setup(Dft24h& m, int lane) {
    const int col = lane & 31, hh = lane >> 5;
    unsigned ch[6], sh[6];
#pragma unroll
    for (int i = 0; i < 6; i++) {
        float a0 = (float)((rho(2 * i, hh) * col) % 24) * (1.0f / 24.0f), a1 = (float)((rho(2 * i + 1, hh) * col) % 24) * (1.0f / 24.0f);
        ch[i] = pack_f16(__builtin_amdgcn_cosf(a0), __builtin_amdgcn_cosf(a1));
        sh[i] = pack_f16(__builtin_amdgcn_sinf(a0), __builtin_amdgcn_sinf(a1));
    }
    const unsigned N = 0x80008000u;
    m.P[0] = mk8h(ch[0], ch[1], ch[2], ch[3]); m.P[1] = mk8h(ch[4], ch[5], sh[0], sh[1]); m.P[2] = mk8h(sh[2], sh[3], sh[4], sh[5]);
    m.Q[0] = mk8h(sh[0] ^ N, sh[1] ^ N, sh[2] ^ N, sh[3] ^ N); m.Q[1] = mk8h(sh[4] ^ N, sh[5] ^ N, ch[0], ch[1]); m.Q[2] = mk8h(ch[2], ch[3], ch[4], ch[5]);
}
template <bool ASA>
DI void dft24h_stage(const Dft24h& m, float (&re)[12], float (&im)[12]) {
    unsigned h[12];
#pragma unroll
    for (int i = 0; i < 6; i++) { h[i] = pack_f16(re[2 * i], re[2 * i + 1]); h[6 + i] = pack_f16(im[2 * i], im[2 * i + 1]); }
    f32x16 dr = {0, 0, 0, 0, 0, 0, 0, 0, 0, 0, 0, 0, 0, 0, 0, 0}, di = dr;
#pragma unroll
    for (int s = 0; s < 3; s++) {
        f16x8 xh = mk8h(h[4 * s], h[4 * s + 1], h[4 * s + 2], h[4 * s + 3]);
        if (ASA) { dr = MFMA32H(xh, m.P[s], dr); di = MFMA32H(xh, m.Q[s], di); }
        else { dr = MFMA32H(m.P[s], xh, dr); di = MFMA32H(m.Q[s], xh, di); }
    }
#pragma unroll
    for (int a = 0; a < 12; a++) { re[a] = dr[a]; im[a] = di[a]; }
}
struct Dft16 { bf16x8 Ph, Pl, Qh, Ql; float twc[4], tws[4]; };
DI void dft16_setup(Dft16& m, int lane) {
    const int col = lane & 15, g = lane >> 4;
    float c[4], s[4];
#pragma unroll
    for (int e = 0; e < 4; e++) {
        int pr = (4 * g + e) * col;
        float ang = (float)(pr & 15) * (1.0f / 16.0f);
        c[e] = __builtin_amdgcn_cosf(ang); s[e] = __builtin_amdgcn_sinf(ang);
        float an2 = (float)pr * (1.0f / 256.0f);
        m.twc[e] = __builtin_amdgcn_cosf(an2); m.tws[e] = __builtin_amdgcn_sinf(an2);
    }
    unsigned ch[2], cl[2], sh[2], sl[2];
    split2(c[0], c[1], ch[0], cl[0]); split2(c[2], c[3], ch[1], cl[1]);
    split2(s[0], s[1], sh[0], sl[0]); split2(s[2], s[3], sh[1], sl[1]);
    const unsigned N = 0x80008000u;
    m.Ph = mk8(ch[0], ch[1], sh[0], sh[1]); m.Pl = mk8(cl[0], cl[1], sl[0], sl[1]);
    m.Qh = mk8(sh[0] ^ N, sh[1] ^ N, ch[0], ch[1]); m.Ql = mk8(sl[0] ^ N, sl[1] ^ N, cl[0], cl[1]);
}
template <bool ASA>
DI void dft16_stage(const Dft16& m, float (&re)[4], float (&im)[4]) {
    unsigned h[4], l[4];
    split2(re[0], re[1], h[0], l[0]); split2(re[2], re[3], h[1], l[1]);
    split2(im[0], im[1], h[2], l[2]); split2(im[2], im[3], h[3], l[3]);
    bf16x8 xh = mk8(h[0], h[1], h[2], h[3]), xl = mk8(l[0], l[1], l[2], l[3]);
    f32x4 dr = {0, 0, 0, 0}, di = dr;
    if (ASA) {
        dr = MFMA16(xl, m.Ph, dr); di = MFMA16(xl, m.Qh, di);
        dr = MFMA16(xh, m.Pl, dr); di = MFMA16(xh, m.Ql, di);
        dr = MFMA16(xh, m.Ph, dr); di = MFMA16(xh, m.Qh, di);
    } else {
        dr = MFMA16(m.Ph, xl, dr); di = MFMA16(m.Qh, xl, di);
        dr = MFMA16(m.Pl, xh, dr); di = MFMA16(m.Ql, xh, di);
        dr = MFMA16(m.Ph, xh, dr); di = MFMA16(m.Qh, xh, di);
    }
#pragma unroll
    for (int e = 0; e < 4; e++) { re[e] = dr[e]; im[e] = di[e]; }
}
DI void cmul_neg(float (&re)[4], float (&im)[4], const float (&c)[4], const float (&s)[4]) {
#pragma unroll
    for (int e = 0; e < 4; e++) {
        float r = re[e] * c[e] + im[e] * s[e];
        float i = im[e] * c[e] - re[e] * s[e];
        re[e] = r; im[e] = i;
    }
}
template <int NW>
DI void wait_flags(GAS u64* f, u64 tag, int idx) {
    for (unsigned spins = 0; spins < (1u << 20); spins++) {
        bool ok = true;
#pragma unroll
        for (int k = 0; k < NW; k++) ok &= (__hip_atomic_load(f + idx + 64 * k, RLX_AGENT) == tag);
        if (__all(ok)) break;
        __builtin_amdgcn_s_sleep(2);
    }
    __builtin_amdgcn_fence(__ATOMIC_ACQUIRE, "wavefront");
}

__global__ __launch_bounds__(512, 2) void k_fused(const uint16_t* __restrict__ x, const uint16_t* __restrict__ wr, const uint16_t* __restrict__ wi,
                                                  const uint16_t* __restrict__ w1, const uint16_t* __restrict__ b1, const uint16_t* __restrict__ w2,
                                                  const uint16_t* __restrict__ b2, uint16_t* __restrict__ out, char* __restrict__ ws
                                                  ) {
    extern __shared__ __attribute__((aligned(16))) char smem_g[];
    LAS char* smem = (LAS char*)smem_g;
    const int tid = threadIdx.x, lane = tid & 63;
    const int w = __builtin_amdgcn_readfirstlane(tid >> 6);
    const int jb = blockIdx.x, fs = jb & 7, t = jb >> 3;
    const unsigned lds0 = (unsigned)(uintptr_t)smem;
    const int rh = w & 1, ct = w >> 1;
    const __amdgpu_buffer_rsrc_t rs = __builtin_amdgcn_make_buffer_rsrc(ws, 0, WS_END, 0x00020000);
    STAMP(0);
    const int hb = 2 * ((t >> 2) % 5), r4 = t & 3;
    int sbk[5];
#pragma unroll
    for (int i = 0; i < 4; i++) { int ii = (i + r4) & 3; sbk[i] = __builtin_amdgcn_readfirstlane(2 * ii + (2 * ii >= hb ? 1 : 0)); }
    sbk[4] = __builtin_amdgcn_readfirstlane(hb);
    const char* w1b = (const char*)w1 + fs * 1152;
    const char* w2b = (const char*)w2 + (size_t)fs * 576 * 1152;
    const unsigned ldsw = lds0 + w * 1024;
    auto mkvo = [&](bool full, unsigned pitch) {
        int ln = tid; PIN(ln); ln &= 63;
        unsigned r = full ? 4 * w + (ln >> 4) : 8 * w + (ln >> 3);
        unsigned c = full ? ((((ln >> 2) & 3) ^ (ln >> 4)) * 64) + (ln & 3) * 16 : ((((ln >> 2) & 1) ^ ((ln >> 4) & 1)) * 64) + (ln & 3) * 16;
        return r * pitch + c;
    };
    unsigned vo;
    auto gl = [&](auto U_, auto J_) {
        constexpr int U = decltype(U_)::value, J = decltype(J_)::value;
        constexpr int ph = U / 27, v = U % 27;
        constexpr int DST = (U % 6) * RING_SLOT + J * 8192;
        if constexpr (U < 54) {
            constexpr int R = v < 3 ? 192 * v + 64 * J : 96 * ((v - 3) % 6) + 32 * J;
            const int blk = sbk[v < 3 ? 4 : (v - 3) / 6];
            if constexpr (ph == 0) glds16<DST>(w1b + (unsigned)__builtin_amdgcn_readfirstlane(blk * 128 + R * 9216), vo, ldsw);
            else glds16<DST>(w2b + (unsigned)__builtin_amdgcn_readfirstlane(blk * 128 + (64 * sbk[R / 128] + R % 128) * 1152), vo, ldsw);
        }
    };
    auto setvo = [&](auto U_) {
        constexpr int U = decltype(U_)::value;
        if constexpr (U == 0) vo = mkvo(false, 9216);
        if constexpr (U == 3) vo = mkvo(true, 9216);
        if constexpr (U == 27) vo = mkvo(false, 1152);
        if constexpr (U == 30) vo = mkvo(true, 1152);
    };
    auto gl3 = [&](auto U_) { setvo(U_); static_for<0, 3>([&](auto J_) { gl(U_, J_); }); };

    {
        const int i = lane & 7, c = lane >> 3;
        const uint16_t* xr = x + (size_t)(8 * t + w + 256 * i) * 576 + c * 8;
        u32x4 q[9];
#pragma unroll
        for (int n = 0; n < 9; n++) q[n] = *(const u32x4*)(xr + n * 64);
        u64 ep = 0;
        if (tid == 0) ep = __hip_atomic_load((GAS u64*)(ws + WS_EPOCH), RLX_AGENT);
        u32x4 bb = {0, 0, 0, 0};
        const bool dob = tid >= 384 && tid < 384 + 72;
        if (dob) bb = *(const u32x4*)((const char*)b1 + fs * 1152 + (tid - 384) * 16);
        SB0();
        gl3(IC<0>{}); gl3(IC<1>{}); gl3(IC<2>{});
        SB0();
        f32x2 d[36];
        f32x2 s2 = {0.f, 0.f};
#pragma unroll
        for (int n = 0; n < 9; n++)
#pragma unroll
            for (int e = 0; e < 4; e++) { f32x2 v = {bf_lo(q[n][e]), bf_hi(q[n][e])}; d[4 * n + e] = v; s2 += v; }
        const float mu = red8(s2[0] + s2[1]) * (1.0f / 576.0f);
        const f32x2 mu2 = {mu, mu};
        f32x2 q2 = {0.f, 0.f};
#pragma unroll
        for (int e = 0; e < 36; e++) { d[e] -= mu2; q2 = __builtin_elementwise_fma(d[e], d[e], q2); }
        const float var = red8(q2[0] + q2[1]) * (1.0f / 576.0f);
        const float m1 = rbf(mu);
        const float is1 = 1.0f / rbf(__builtin_sqrtf(rbf(rbf(var) + 1e-5f)));
        const float mean2 = (mu - m1) * is1;
        const float var2 = var * is1 * is1;
        const float is2 = 1.0f / rbf(__builtin_sqrtf(rbf(rbf(var2) + 1e-5f)));
        const float ya = is1 * is2, yb = (mean2 - rbf(mean2)) * is2;
        const f32x2 ya2 = {ya, ya}, yb2 = {yb, yb};
        const int mloc = 8 * w + i;
        LAS char* yw = smem + LDS_Y + (mloc >> 5) * 36 * 1024 + (c >> 1) * 1024 + ((mloc & 31) + 32 * (c & 1)) * 16;
#pragma unroll
        for (int n = 0; n < 9; n++) {
            u32x4 o;
#pragma unroll
            for (int e = 0; e < 4; e++) { f32x2 v = __builtin_elementwise_fma(d[4 * n + e], ya2, yb2); o[e] = pack_bf16(v[0], v[1]); }
            *(LAS u32x4*)(yw + n * 4096) = o;
        }
        if (w == fs) {
            const f32x2 xa = {is1, is1}, xb = {mean2, mean2};
            const int pos = (i & 1) ? 4 + (i >> 1) : ((i >> 2) | (i & 2));
            LAS char* xw = smem + LDS_XS + pos * 1152 + c * 16;
#pragma unroll
            for (int n = 0; n < 9; n++) {
                u32x4 o;
#pragma unroll
                for (int e = 0; e < 4; e++) { f32x2 v = __builtin_elementwise_fma(d[4 * n + e], xa, xb); o[e] = pack_bf16(v[0], v[1]); }
                *(LAS u32x4*)(xw + n * 128) = o;
            }
        }
        if (dob) {
            f32x4 lo = {bf_lo(bb[0]), bf_hi(bb[0]), bf_lo(bb[1]), bf_hi(bb[1])}, hi = {bf_lo(bb[2]), bf_hi(bb[2]), bf_lo(bb[3]), bf_hi(bb[3])};
            *(LAS f32x4*)(smem + LDS_BIAS + (tid - 384) * 32) = lo;
            *(LAS f32x4*)(smem + LDS_BIAS + (tid - 384) * 32 + 16) = hi;
        }
        if (tid == 0) *(LAS u64*)(smem + LDS_TAG) = ep;
        waitv<0>();
        WAITL0();
        int li = w * 21 + (lane % 21); if (li > 161) li = 161;
        int rr = li / 9, cc = li % 9;
        glds4(w1b, (18 * t + rr) * 9216 + cc * 128, lds0 + LDS_JUNK);
        glds4(w2b, (18 * t + rr) * 1152 + cc * 128, lds0 + LDS_JUNK);
    }
    BAR();
    STAMP(1);
    u64 epoch;
    {
        u32x2 e2 = *(LAS u32x2*)(smem + LDS_TAG);
        epoch = ((u64)(unsigned)__builtin_amdgcn_readfirstlane(e2[1]) << 32) | (unsigned)__builtin_amdgcn_readfirstlane(e2[0]);
    }
    const u64 tag = (epoch + 1) * 0x9E3779B97F4A7C15ull + 0x632BE59BD9B4E019ull;

    auto put_flag = [&](bool me, unsigned off, u64 val) {
        u32x2 o = {(unsigned)val, (unsigned)(val >> 32)};
        __builtin_amdgcn_raw_buffer_store_b64(o, rs, me ? 0u : 0xF0000000u, off, 16);
    };
    LAS char* trc;
    auto settr = [&](auto U_) {
        constexpr int U = decltype(U_)::value, v = U % 27, ph = U / 27;
        if constexpr (v == 0 || v == 3) {
            int ln = tid; PIN(ln); ln &= 63;
            int G = ln >> 4, q = (ln >> 2) & 3, p = ln & 3;
            int row = (ph == 0 ? 8 : 4) * (G >> 1) + q;
            if constexpr (v == 3) trc = smem + row * 256 + ((ct ^ q) * 64) + (G & 1) * 32 + p * 8 + ((U % 6) / 2) * 2 * RING_SLOT;
            else trc = smem + row * 128 + (((ct & 1) ^ ((q >> 1) & 1)) * 64) + (G & 1) * 32 + p * 8 + ((U % 6) / 2) * 2 * RING_SLOT;
            PIN(trc);
        } else if constexpr (U % 2 == 0) {
            trc += (U % 6 == 0) ? -4 * RING_SLOT : 2 * RING_SLOT;
            PIN(trc);
        }
    };
    bf16x8 af[36];
    {
        LAS char* ab = smem + LDS_Y + rh * 36 * 1024 + lane * 16; PIN(ab);
#pragma unroll
        for (int j = 0; j < 36; j++) af[j] = *(LAS bf16x8*)(ab + j * 1024);
    }
    LAS char* biasp = smem + LDS_BIAS + (32 * ct + 4 * (lane >> 5)) * 4;
    const unsigned pbase = WS_PART + (unsigned)(((fs * 32 + t) * 36 + 2 * ct + rh) * 2048);
    unsigned plane = 0;

    f32x16 acc[2];
    s16x4 bl[6], bh[6];
    float gt[4];
    unsigned hq[5][8];
    auto ldbias = [&](auto P_, auto Q_, int blk) {
        constexpr int P = decltype(P_)::value, Q = decltype(Q_)::value;
        f32x4 b = *(LAS f32x4*)(biasp + blk * 256 + Q * 32);
        acc[P][4 * Q] = b[0]; acc[P][4 * Q + 1] = b[1]; acc[P][4 * Q + 2] = b[2]; acc[P][4 * Q + 3] = b[3];
    };
    auto gop = [&](auto C_, auto K_) {
        constexpr int C = decltype(C_)::value, K = decltype(K_)::value, P = C == 4 ? 1 : (C & 1);
        constexpr int NB = C == 4 ? 1 : C + 2;
        const float c0 = -2.0f * 0.7978845608028654f * 1.4426950408889634f;
        const float c1 = c0 * 0.044715f;
        if constexpr (K < 124) {
            constexpr int Q = K / 31, k = K % 31;
            if constexpr (k < 28) {
                constexpr int st = k / 4, e = k % 4;
                float xx = acc[P][4 * Q + e];
                if constexpr (st == 0) gt[e] = xx * xx;
                else if constexpr (st == 1) gt[e] = __builtin_fmaf(gt[e], c1, c0);
                else if constexpr (st == 2) gt[e] = xx * gt[e];
                else if constexpr (st == 3) gt[e] = __builtin_amdgcn_exp2f(gt[e]);
                else if constexpr (st == 4) gt[e] = 1.0f + gt[e];
                else if constexpr (st == 5) gt[e] = __builtin_amdgcn_rcpf(gt[e]);
                else gt[e] = xx * gt[e];
                PIN(gt[e]);
            } else if constexpr (k == 28) { hq[C][2 * Q] = pack_bf16(gt[0], gt[1]); PIN(hq[C][2 * Q]); }
            else if constexpr (k == 29) { hq[C][2 * Q + 1] = pack_bf16(gt[2], gt[3]); PIN(hq[C][2 * Q + 1]); }
            else { if constexpr (NB <= 3) ldbias(IC<P>{}, IC<Q>{}, sbk[NB]); }
        }
    };
    auto grange = [&](auto C_, auto A_, auto B_) {
        static_for<decltype(A_)::value, decltype(B_)::value>([&](auto K_) { gop(C_, K_); });
    };
    auto pstore = [&](auto P_, auto H_, int blk) {
        constexpr int P = decltype(P_)::value, H = decltype(H_)::value;
        u32x4 o;
        o[0] = pack_bf16(acc[P][8 * H + 0], acc[P][8 * H + 1]); o[1] = pack_bf16(acc[P][8 * H + 2], acc[P][8 * H + 3]);
        o[2] = pack_bf16(acc[P][8 * H + 4], acc[P][8 * H + 5]); o[3] = pack_bf16(acc[P][8 * H + 6], acc[P][8 * H + 7]);
        __builtin_amdgcn_raw_buffer_store_b128(o, rs, plane, pbase + blk * 8192 + H * 1024, 16);
    };
    const f32x16 zero16 = {0, 0, 0, 0, 0, 0, 0, 0, 0, 0, 0, 0, 0, 0, 0, 0};
    auto bfrag = [&](auto J_) {
        constexpr int J = decltype(J_)::value;
        return __builtin_bit_cast(bf16x8, __builtin_shufflevector(bl[J], bh[J], 0, 1, 2, 3, 4, 5, 6, 7));
    };
    auto fslot = [&](auto U_, auto LO_, auto GI_, auto&& fill) {
        constexpr int U = decltype(U_)::value, ph = U / 27, v = U % 27 - 3, c = v / 6, s = v % 6;
        constexpr bool LO = decltype(LO_)::value;
        constexpr int PB = c & 1;
        constexpr int PA = s == 0 ? PB ^ 1 : PB;
        constexpr int sp = (s + 5) % 6;
        constexpr int R2 = ph == 0 ? 1024 : 2048;
        settr(U_);
        LAS char* sb = trc + (U % 2) * RING_SLOT;
        setvo(GI_);
        static_for<0, 6>([&](auto G_) {
            constexpr int G = decltype(G_)::value;
            if constexpr (G < 3) {
                if constexpr (LO) acc[PA] = MFMA32(bfrag(IC<G + 3>{}), af[6 * sp + G + 3], acc[PA]);
            } else {
                acc[PB] = MFMA32(bfrag(IC<G - 3>{}), af[6 * s + G - 3], (ph == 1 && s == 0 && G == 3) ? zero16 : acc[PB]);
            }
            SB0();
            bl[G] = tr_rd(sb + G * 4096); bh[G] = tr_rd(sb + G * 4096 + R2);
            if constexpr (G < 3) gl(GI_, G_);
            fill(G_);
            SB0();
        });
    };
    auto hslot = [&](auto U_, auto GI_, auto GI2_) {
        constexpr int U = decltype(U_)::value, ph = U / 27, s = U % 27;
        constexpr int R2 = ph == 0 ? 512 : 1024;
        settr(U_);
        LAS char* sb = trc + (U % 2) * RING_SLOT;
        setvo(GI_);
        static_for<0, 12>([&](auto G_) {
            constexpr int G = decltype(G_)::value;
            if constexpr (G < 3) {
                if constexpr (s > 0) acc[1] = MFMA32(bfrag(IC<G + 3>{}), af[12 * s - 3 + G], acc[1]);
            } else {
                acc[1] = MFMA32(bfrag(IC<(G - 3) % 6>{}), af[12 * s + G - 3], (ph == 1 && s == 0 && G == 3) ? zero16 : acc[1]);
            }
            SB0();
            bl[G % 6] = tr_rd(sb + G * 2048); bh[G % 6] = tr_rd(sb + G * 2048 + R2);
            if constexpr (G < 6 && G % 2 == 0) gl(GI_, IC<G / 2>{});
            if constexpr (G >= 6 && G % 2 == 0) gl(GI2_, IC<G / 2 - 3>{});
            SB0();
        });
    };
    using T = std::true_type; using F = std::false_type;
    auto nofill = [&](auto) {};

    if (w < 4) {
        static_for<0, 4>([&](auto Q_) { ldbias(IC<1>{}, Q_, sbk[4]); ldbias(IC<0>{}, Q_, sbk[0]); });
        hslot(IC<0>{}, IC<99>{}, IC<99>{}); WAITL0(); BAR();
        hslot(IC<1>{}, IC<3>{}, IC<4>{}); BAR();
        hslot(IC<2>{}, IC<5>{}, IC<99>{}); waitv<6>(); BAR();
        static_for<3, 9>([&](auto U_) {
            constexpr int U = decltype(U_)::value, s = U - 3;
            fslot(U_, T{}, IC<U + 3>{}, [&](auto G_) {
                constexpr int gi = 6 * s + decltype(G_)::value - 4;
                if constexpr (gi >= 0) grange(IC<4>{}, IC<(gi * 124) / 32>{}, IC<((gi + 1) * 124) / 32>{});
            });
            waitv<6>(); BAR();
        });
    } else {
        const int v = w - 4;
        Dft24h m; dft24h_setup(m, lane);
        WAITL0(); BAR();
        gl3(IC<3>{}); gl3(IC<4>{});
        const unsigned PP = 0x3F803F80u, PM = 0xBF803F80u  , MP = 0x3F80BF80u  , MM = 0xBF80BF80u;
        unsigned A0, A1, A2, A3, B0, B1, B2, B3; float cc;
        const float c8 = 0.70710678118654752f;
        if (v == 0) { A0 = PP; A1 = PP; A2 = PP; A3 = PP; B0 = PP; B1 = PP; B2 = MM; B3 = MM; cc = 1.f; }
        else if (v == 1) { A0 = PM; A1 = 0; A2 = PM; A3 = MP; B0 = 0; B1 = MP; B2 = MM; B3 = PP; cc = c8; }
        else if (v == 2) { A0 = PP; A1 = MM; A2 = 0; A3 = 0; B0 = 0; B1 = 0; B2 = MP; B3 = MP; cc = 1.f; }
        else { A0 = PM; A1 = 0; A2 = MP; A3 = PM; B0 = 0; B1 = PM; B2 = MM; B3 = PP; cc = c8; }
        float re[12], im[12];
        auto dot = [](unsigned a, unsigned b, float c) { return __builtin_amdgcn_fdot2_f32_bf16(__builtin_bit_cast(bf16x2, a), __builtin_bit_cast(bf16x2, b), c, false); };
        {
            const int G = lane >> 4, q = (lane >> 2) & 3, p = lane & 3;
            LAS char* xb = smem + LDS_XS + q * 1152 + (96 * (G >> 1) + 16 * (G & 1) + 4 * p) * 2;
#pragma unroll
            for (int a = 0; a < 12; a++) {
                u32x2 ev = __builtin_bit_cast(u32x2, tr_rd(xb + rho(a, 0) * 48));
                u32x2 od = __builtin_bit_cast(u32x2, tr_rd(xb + rho(a, 0) * 48 + 4 * 1152));
                re[a] = __builtin_fmaf(dot(od[0], A2, dot(od[1], A3, 0.f)), cc, dot(ev[0], A0, dot(ev[1], A1, 0.f)));
                im[a] = __builtin_fmaf(dot(od[0], B2, dot(od[1], B3, 0.f)), cc, dot(ev[0], B0, dot(ev[1], B1, 0.f)));
            }
        }
        dft24h_stage<true>(m, re, im);
        SB0(); BAR();
        gl3(IC<5>{});
        tw576(re, im, lane);
        dft24h_stage<false>(m, re, im);
        {
            const int hh = lane >> 5;
            const unsigned zo = (lane & 31) < 24 ? (unsigned)(((lane & 31) + 96 * hh) * 8) : 0xFFFFFF00u;
            const unsigned zb = WS_ZV + (unsigned)(jb * 4 + v) * ZV_PITCH;
#pragma unroll
            for (int a = 0; a < 12; a++) {
                u32x2 o = {__float_as_uint(re[a]), __float_as_uint(im[a])};
                __builtin_amdgcn_raw_buffer_store_b64(o, rs, zo, zb + rho(a, 0) * 192, 16);
            }
            u32x2 o = {__float_as_uint(re[0]), __float_as_uint(im[0])};
            __builtin_amdgcn_raw_buffer_store_b64(o, rs, lane == 0 ? 0u : 0xFFFFFF00u, zb + 576 * 8, 16);
        }
        static_for<0, 4>([&](auto Q_) { ldbias(IC<0>{}, Q_, sbk[0]); ldbias(IC<1>{}, Q_, sbk[1]); });
        waitv<19>(); BAR();
        static_for<3, 9>([&](auto U_) {
            constexpr int U = decltype(U_)::value;
            if constexpr (U == 3) fslot(U_, F{}, IC<U + 3>{}, nofill); else fslot(U_, T{}, IC<U + 3>{}, nofill);
            if constexpr (U == 3) waitv<19>(); else if constexpr (U == 4) waitv<19>(); else waitv<6>();
            BAR();
            if constexpr (U == 5) __builtin_amdgcn_raw_buffer_store_b32((unsigned)(tag >> 32), rs, tid == 256 ? 0u : 0xF0000000u, WS_ROWF + jb * 4, 16);
        });
    }
    u32x4 rf;
    static_for<9, 27>([&](auto U_) {
        constexpr int U = decltype(U_)::value, c = (U - 3) / 6, s = (U - 3) % 6;
        fslot(U_, T{}, IC<U + 3>{}, [&](auto G_) {
            constexpr int gi = 6 * s + decltype(G_)::value - 4;
            if constexpr (gi >= 0) grange(IC<c - 1>{}, IC<(gi * 124) / 32>{}, IC<((gi + 1) * 124) / 32>{});
            if constexpr (U == 24 && decltype(G_)::value == 4) {
                int ln = tid; PIN(ln); ln &= 63;
                rf = __builtin_amdgcn_raw_buffer_load_b128(rs, ln * 16, WS_ROWF, 16);
            }
        });
        if constexpr (U < 24) { waitv<6>(); BAR(); }
        else if constexpr (U < 26) { waitv<7>(); BAR(); }
    });
    static_for<0, 3>([&](auto G_) { constexpr int G = decltype(G_)::value; acc[1] = MFMA32(bfrag(IC<G + 3>{}), af[33 + G], acc[1]); });
    STAMP(2);
    WAITL0();
    waitv<0>();
    BAR();
    u32x2 wqr[4], wqi[4];
    u32x4 za4[4], zb4[4]; u32x2 za2[4], zb2[4];
    float re[3][4], im[3][4];
    if (w >= 4) {
        int ln = tid; PIN(ln); ln &= 63;
        const int r = fs, n2 = ln & 15, g = ln >> 4;
        const int k0 = 9 * t + 3 * (w - 4);
        const unsigned tg = (unsigned)(tag >> 32);
        for (unsigned spins = 0; spins < (1u << 20); spins++) {
            if (__all(rf[0] == tg && rf[1] == tg && rf[2] == tg && rf[3] == tg)) break;
            __builtin_amdgcn_s_sleep(2);
            rf = __builtin_amdgcn_raw_buffer_load_b128(rs, ln * 16, WS_ROWF, 16);
        }
        __builtin_amdgcn_fence(__ATOMIC_ACQUIRE, "wavefront");
        put_flag(jb == 0 && tid == 256, WS_EPOCH, epoch + 1);
        const int vA = r <= 3 ? r : 0, vB = r >= 5 ? 8 - r : 0;
        const bool useA = r <= 4, useB = (r == 0 || r >= 4);
        const unsigned lo = (unsigned)(64 * g + n2) * 4 * ZV_PITCH;
        const unsigned loA = useA ? lo : 0xF0000000u, loB = useB ? lo : 0xF0000000u;
        const unsigned sA = WS_ZV + vA * ZV_PITCH + k0 * 8, sB = WS_ZV + vB * ZV_PITCH + (574 - k0) * 8;
#pragma unroll
        for (int e = 0; e < 4; e++) {
            za4[e] = __builtin_amdgcn_raw_buffer_load_b128(rs, loA, sA + e * 64 * ZV_PITCH, 16);
            za2[e] = __builtin_amdgcn_raw_buffer_load_b64(rs, loA, sA + e * 64 * ZV_PITCH + 16, 16);
            zb4[e] = __builtin_amdgcn_raw_buffer_load_b128(rs, loB, sB + e * 64 * ZV_PITCH, 16);
            zb2[e] = __builtin_amdgcn_raw_buffer_load_b64(rs, loB, sB + e * 64 * ZV_PITCH + 16, 16);
        }
        const int ks = k0 > 285 ? 285 : k0;
        const unsigned wo = (unsigned)((8 * (n2 + 64 * g) + r) * 289 + ks) * 2;
#pragma unroll
        for (int e = 0; e < 4; e++) {
            __builtin_memcpy(&wqr[e], (const char*)wr + wo + e * (128 * 289 * 2), 8);
            __builtin_memcpy(&wqi[e], (const char*)wi + wo + e * (128 * 289 * 2), 8);
        }
        SB0();
    }
    {
        int ln = tid; PIN(ln); ln &= 63;
        plane = ln * 16;
        LAS char* ab = smem + LDS_H + rh * 36 * 1024 + ln * 16;
        LAS char* hw = ab + 2 * ct * 1024;
        auto hst = [&](auto C_) {
            constexpr int C = decltype(C_)::value;
            u32x4 o0 = {hq[C][0], hq[C][1], hq[C][2], hq[C][3]}, o1 = {hq[C][4], hq[C][5], hq[C][6], hq[C][7]};
            *(LAS u32x4*)(hw + C * 8192) = o0; *(LAS u32x4*)(hw + C * 8192 + 1024) = o1;
        };
        hst(IC<0>{}); hst(IC<1>{}); hst(IC<2>{});
        if (w < 4) hst(IC<4>{});
        grange(IC<3>{}, IC<0>{}, IC<124>{});
        hst(IC<3>{});
        WAITL0();
        BAR();
        if (w >= 4) {
            const float sg = fs == 4 ? -1.f : 1.f;
#pragma unroll
            for (int e = 0; e < 4; e++) {
                re[0][e] = __builtin_fmaf(sg, __uint_as_float(zb2[e][0]), __uint_as_float(za4[e][0])); im[0][e] = __builtin_fmaf(-sg, __uint_as_float(zb2[e][1]), __uint_as_float(za4[e][1]));
                re[1][e] = __builtin_fmaf(sg, __uint_as_float(zb4[e][2]), __uint_as_float(za4[e][2])); im[1][e] = __builtin_fmaf(-sg, __uint_as_float(zb4[e][3]), __uint_as_float(za4[e][3]));
                re[2][e] = __builtin_fmaf(sg, __uint_as_float(zb4[e][0]), __uint_as_float(za2[e][0])); im[2][e] = __builtin_fmaf(-sg, __uint_as_float(zb4[e][1]), __uint_as_float(za2[e][1]));
#pragma unroll
                for (int c = 0; c < 3; c++) { PIN(re[c][e]); PIN(im[c][e]); }
            }
            SB0();
        }
#pragma unroll
        for (int j = 0; j < 36; j++) af[j] = *(LAS bf16x8*)(ab + j * 1024);
    }
    STAMP(3);
    if (w < 4) {
        hslot(IC<27>{}, IC<99>{}, IC<99>{}); WAITL0(); BAR();
        hslot(IC<28>{}, IC<30>{}, IC<31>{}); BAR();
        hslot(IC<29>{}, IC<32>{}, IC<99>{}); waitv<6>(); BAR();
        fslot(IC<30>{}, T{}, IC<33>{}, [&](auto G_) { if constexpr (decltype(G_)::value == 5) pstore(IC<1>{}, IC<0>{}, sbk[4]); }); waitv<7>(); BAR();
        fslot(IC<31>{}, T{}, IC<34>{}, [&](auto G_) { if constexpr (decltype(G_)::value == 5) pstore(IC<1>{}, IC<1>{}, sbk[4]); }); waitv<8>(); BAR();
        fslot(IC<32>{}, T{}, IC<35>{}, nofill); waitv<8>(); BAR();
        fslot(IC<33>{}, T{}, IC<36>{}, nofill); waitv<7>(); BAR();
    } else {
        const int r = fs, n2 = lane & 15, g = lane >> 4;
        Dft16 m; dft16_setup(m, lane);
        float jc[4], js[4];
        {
            float ic[4], is[4];
#pragma unroll
            for (int e = 0; e < 4; e++) {
                float ang = (float)((64 * g + 16 * e + n2) * r) * (1.0f / 2048.0f);
                jc[e] = __builtin_amdgcn_cosf(ang); js[e] = __builtin_amdgcn_sinf(ang);
                ic[e] = r == 0 ? 0.5f * jc[e] : r == 4 ? -0.5f * js[e] : jc[e];
                is[e] = r == 0 ? 0.5f * js[e] : r == 4 ? 0.5f * jc[e] : js[e];
            }
            WAITL0(); SB0(); BAR();
            gl3(IC<30>{}); gl3(IC<31>{});
#pragma unroll
            for (int c = 0; c < 3; c++) cmul_neg(re[c], im[c], ic, is);
        }
#pragma unroll
        for (int c = 0; c < 3; c++) dft16_stage<true>(m, re[c], im[c]);
#pragma unroll
        for (int c = 0; c < 3; c++) cmul_neg(re[c], im[c], m.twc, m.tws);
#pragma unroll
        for (int c = 0; c < 3; c++) dft16_stage<false>(m, re[c], im[c]);
#pragma unroll
        for (int c = 0; c < 3; c++)
#pragma unroll
            for (int e = 0; e < 4; e++) {
                const float sc = 1.0f / (2048.0f * 576.0f);
                const bool last = (t == 31 && w == 7);
                const float wre = (c == 0 ? (last ? bf_hi(wqr[e][1]) : bf_lo(wqr[e][0])) : c == 1 ? bf_hi(wqr[e][0]) : bf_lo(wqr[e][1])) * sc;
                const float wim = (c == 0 ? (last ? bf_hi(wqi[e][1]) : bf_lo(wqi[e][0])) : c == 1 ? bf_hi(wqi[e][0]) : bf_lo(wqi[e][1])) * sc;
                float a = re[c][e] * wre - im[c][e] * wim;
                float b = -(re[c][e] * wim + im[c][e] * wre);
                re[c][e] = a; im[c][e] = b;
            }
        SB0(); BAR();
        gl3(IC<32>{});
#pragma unroll
        for (int c = 0; c < 3; c++) dft16_stage<true>(m, re[c], im[c]);
#pragma unroll
        for (int c = 0; c < 3; c++) cmul_neg(re[c], im[c], m.twc, m.tws);
#pragma unroll
        for (int c = 0; c < 3; c++) dft16_stage<false>(m, re[c], im[c]);
#pragma unroll
        for (int c = 0; c < 3; c++) cmul_neg(re[c], im[c], jc, js);
        {
            const unsigned vo_ = (unsigned)(64 * g + n2) * 96;
            const unsigned vb = WS_VP + (unsigned)(r * 32 + t) * (256 * 96) + (w - 4) * 24;
#pragma unroll
            for (int e = 0; e < 4; e++) {
                u32x4 o4 = {__float_as_uint(re[0][e]), __float_as_uint(im[0][e]), __float_as_uint(re[1][e]), __float_as_uint(im[1][e])};
                u32x2 o2 = {__float_as_uint(re[2][e]), __float_as_uint(im[2][e])};
                __builtin_amdgcn_raw_buffer_store_b128(o4, rs, vo_, vb + e * 16 * 96, 16);
                __builtin_amdgcn_raw_buffer_store_b64(o2, rs, vo_, vb + e * 16 * 96 + 16, 16);
            }
        }
        waitv<14>(); BAR();
        fslot(IC<30>{}, F{}, IC<33>{}, nofill); waitv<14>(); BAR();
        fslot(IC<31>{}, T{}, IC<34>{}, nofill); waitv<14>(); BAR();
        fslot(IC<32>{}, T{}, IC<35>{}, nofill); waitv<6>(); BAR();
        put_flag(tid == 256, WS_COLF + (fs * 32 + t) * 8, tag);
        fslot(IC<33>{}, T{}, IC<36>{}, nofill); waitv<6>(); BAR();
    }
    static_for<34, 54>([&](auto U_) {
        constexpr int U = decltype(U_)::value, v = U - 30, c = v / 6, s = v % 6;
        auto fill = [&](auto G_) {
            constexpr int G = decltype(G_)::value;
            if constexpr (c >= 1 && s == 0 && G == 5) pstore(IC<(c + 1) & 1>{}, IC<0>{}, sbk[c >= 1 ? c - 1 : 0]);
            if constexpr (c >= 1 && s == 1 && G == 5) pstore(IC<(c + 1) & 1>{}, IC<1>{}, sbk[c >= 1 ? c - 1 : 0]);
        };
        constexpr int ns = (c >= 1) ? ((s == 0 ? 1 : 0) + (s == 1 ? 2 : 0) + (s == 2 ? 2 : 0) + (s == 3 ? 1 : 0)) : 0;
        fslot(U_, T{}, IC<U + 3>{}, fill);
        if constexpr (U <= 50) { waitv<6 + ns>(); BAR(); }
        else if constexpr (U == 51) { waitv<3 + ns>(); BAR(); }
        else if constexpr (U == 52) { waitv<0>(); BAR(); }
    });
    static_for<0, 3>([&](auto G_) { constexpr int G = decltype(G_)::value; acc[1] = MFMA32(bfrag(IC<G + 3>{}), af[33 + G], acc[1]); });
    pstore(IC<1>{}, IC<0>{}, sbk[3]);
    pstore(IC<1>{}, IC<1>{}, sbk[3]);
    STAMP(4);
    WAITL0();
    waitv<0>();
    BAR();
    STAMP(5);
    put_flag(tid == 0, WS_PARTF + (t * 16 + fs) * 8, tag);
    if (w >= 4) {
        const int v = w - 4, hh = lane >> 5;
        int n2 = lane & 31; if (n2 > 23) n2 = 23;
        const int rB = v == 0 ? 4 : 8 - v;
        wait_flags<1>((GAS u64*)(ws + WS_COLF), tag, (lane < 32 ? v : rB) * 32 + (lane & 31));
        float re[12], im[12];
        {
            u32x2 A[12], B[12];
#pragma unroll
            for (int a = 0; a < 12; a++) {
                const int k = 24 * rho(a, hh) + n2;
                const bool mir = k > 288; const int kk = mir ? 576 - k : k;
                int gq = (kk * 57) >> 9; if (gq > 31) gq = 31;
                const unsigned off = (unsigned)gq * (256 * 96) + (unsigned)(kk - 9 * gq) * 8;
                const unsigned rsel = (v != 0 && mir) ? rB : v;
                A[a] = __builtin_amdgcn_raw_buffer_load_b64(rs, off + rsel * (32 * 256 * 96), WS_VP + jb * 96, 16);
                const bool needB = v == 0 || k == 0 || k == 288;
                if (a == 0 || a == 4 || true) B[a] = __builtin_amdgcn_raw_buffer_load_b64(rs, needB ? off + rB * (32 * 256 * 96) : 0xF0000000u, WS_VP + jb * 96, 16);
            }
#pragma unroll
            for (int a = 0; a < 12; a++) {
                const int k = 24 * rho(a, hh) + n2;
                const bool mir = k > 288, spec = (k == 0 || k == 288);
                float ax = __uint_as_float(A[a][0]), ay = __uint_as_float(A[a][1]), bx = __uint_as_float(B[a][0]), by = __uint_as_float(B[a][1]);
                if (mir) { ay = -ay; by = -by; }
                if (v == 0) {
                    if (spec) { ay = 0.f; by = 0.f; }
                    re[a] = ax + by; im[a] = ay - bx;
                } else {
                    re[a] = spec ? 0.5f * (ax + bx) : ax; im[a] = spec ? 0.5f * (ay - by) : ay;
                }
            }
        }
        Dft24 m; dft24_setup(m, lane);
        dft24_stage<true>(m, re, im);
        tw576(re, im, lane);
        dft24_stage<false>(m, re, im);
        if ((lane & 31) < 24) {
#pragma unroll
            for (int a = 0; a < 12; a++) {
                f32x2 o = {re[a], -im[a]};
                *(LAS f32x2*)(smem + LDS_CB + (v * 576 + (lane & 31) + 24 * rho(a, hh)) * 8) = o;
            }
        }
        WAITL0();
    } else if (w == 0) {
        wait_flags<1>((GAS u64*)(ws + WS_PARTF) + t * 16, tag, lane & 7);
    }
    BAR();
    STAMP(6);
#pragma unroll
    for (int rep = 0; rep < 2; rep++) {
        if (rep == 1 && w != 0) break;
        const int pid = tid + 512 * rep;
        const int i = pid & 7, pc = pid >> 3, ehh = pc & 1, H = (pc >> 1) & 1, ctg = pc >> 2;
        const int mloc = 8 * fs + i;
        u32x4 pv[8];
        const unsigned po = (unsigned)((ctg * 2 + (mloc >> 5)) * 2 + H) * 1024 + ((mloc & 31) + 32 * ehh) * 16;
#pragma unroll
        for (int f = 0; f < 8; f++) pv[f] = __builtin_amdgcn_raw_buffer_load_b128(rs, po, WS_PART + (unsigned)(f * 32 + t) * 73728, 16);
        const int d0 = 32 * ctg + 16 * H + 4 * ehh;
        const size_t rowoff = (size_t)(jb + 256 * i) * 576;
        u32x2 xa = *(const u32x2*)(x + rowoff + d0), xb = *(const u32x2*)(x + rowoff + d0 + 8);
        u32x2 ba = *(const u32x2*)(b2 + d0), bb = *(const u32x2*)(b2 + d0 + 8);
        float cr[4], ci[4];
        cr[0] = 1.f; ci[0] = (i & 1) ? -1.f : 1.f;
#pragma unroll
        for (int r = 1; r < 4; r++) {
            float ang = (float)((i * r) & 7) * 0.125f;
            cr[r] = 2.f * __builtin_amdgcn_cosf(ang); ci[r] = -2.f * __builtin_amdgcn_sinf(ang);
        }
        float sv[8];
#pragma unroll
        for (int e = 0; e < 8; e++) {
            const int d = d0 + (e & 3) + 8 * (e >> 2);
            float s = 0.f;
#pragma unroll
            for (int r = 0; r < 4; r++) { f32x2 c = *(LAS f32x2*)(smem + LDS_CB + (r * 576 + d) * 8); s += cr[r] * c[0] + ci[r] * c[1]; }
            sv[e] = rbf(s);
        }
        float o[8];
#pragma unroll
        for (int e = 0; e < 4; e++) {
            float lo = 0.f, hi = 0.f;
#pragma unroll
            for (int f = 0; f < 8; f++) { lo += bf_lo(pv[f][e]); hi += bf_hi(pv[f][e]); }
            o[2 * e] = lo; o[2 * e + 1] = hi;
        }
        float xv[8] = {bf_lo(xa[0]), bf_hi(xa[0]), bf_lo(xa[1]), bf_hi(xa[1]), bf_lo(xb[0]), bf_hi(xb[0]), bf_lo(xb[1]), bf_hi(xb[1])};
        float bv[8] = {bf_lo(ba[0]), bf_hi(ba[0]), bf_lo(ba[1]), bf_hi(ba[1]), bf_lo(bb[0]), bf_hi(bb[0]), bf_lo(bb[1]), bf_hi(bb[1])};
        float res[8];
#pragma unroll
        for (int e = 0; e < 8; e++) {
            float ov = rbf(rbf(o[e]) + bv[e]);
            float gv = rbf(ov * __builtin_amdgcn_rcpf(1.0f + __builtin_amdgcn_exp2f(-1.4426950408889634f * ov)));
            res[e] = xv[e] + rbf(sv[e] * gv);
        }
        u32x2 oa = {pack_bf16(res[0], res[1]), pack_bf16(res[2], res[3])}, ob = {pack_bf16(res[4], res[5]), pack_bf16(res[6], res[7])};
        *(u32x2*)(out + rowoff + d0) = oa;
        *(u32x2*)(out + rowoff + d0 + 8) = ob;
    }
}

extern "C" void kernel_launch(void* const* d_in, const int* in_sizes, int n_in,
                              void* d_out, int out_size, void* d_ws, size_t ws_size,
                              hipStream_t stream) {
    (void)hipFuncSetAttribute((const void*)k_fused, hipFuncAttributeMaxDynamicSharedMemorySize, LDS_TOTAL);
    k_fused<<<256, 512, LDS_TOTAL, stream>>>((const uint16_t*)d_in[0], (const uint16_t*)d_in[1], (const uint16_t*)d_in[2],
                                             (const uint16_t*)d_in[4], (const uint16_t*)d_in[5], (const uint16_t*)d_in[6],
                                             (const uint16_t*)d_in[7], (uint16_t*)d_out, (char*)d_ws
                                             );
}
```

```cpp
#include <hip/hip_runtime.h>
#include <stdint.h>
#include <type_traits>

typedef __attribute__((ext_vector_type(8))) __bf16 bf16x8;
typedef __attribute__((ext_vector_type(2))) __bf16 bf16x2;
typedef __attribute__((ext_vector_type(8))) _Float16 f16x8;
typedef __attribute__((ext_vector_type(2))) _Float16 f16x2;
typedef __attribute__((ext_vector_type(4))) short s16x4;
typedef __attribute__((ext_vector_type(2))) float f32x2;
typedef __attribute__((ext_vector_type(4))) float f32x4;
typedef __attribute__((ext_vector_type(16))) float f32x16;
typedef __attribute__((ext_vector_type(4))) unsigned u32x4;
typedef __attribute__((ext_vector_type(2))) unsigned u32x2;
typedef __attribute__((ext_vector_type(2))) unsigned short u16x2;
typedef unsigned long long u64;
#define LAS __attribute__((address_space(3)))
#define GAS __attribute__((address_space(1)))
#define DI __device__ __forceinline__

constexpr int RING_SLOT = 24576;
constexpr int LDS_Y = 3 * RING_SLOT;
constexpr int LDS_H = 0;
constexpr int LDS_CB = 0;
constexpr int LDS_BIAS = 6 * RING_SLOT;
constexpr int LDS_JUNK = LDS_BIAS + 2304;
constexpr int LDS_TAG = LDS_JUNK + 256;
constexpr int LDS_XS = LDS_TAG + 16;
constexpr int LDS_TOTAL = LDS_XS + 8 * 1152 + 64;
constexpr unsigned CODE_BYTES = 57 * 1024;
constexpr unsigned WS_EPOCH = 0;
constexpr unsigned WS_ROWF = 256;
constexpr unsigned WS_COLF = 4096;
constexpr unsigned WS_PARTF = 8192;
constexpr unsigned ZV_PITCH = 580 * 8;
constexpr unsigned WS_ZV = 16384;
constexpr unsigned WS_VP = WS_ZV + 256 * 4 * ZV_PITCH;
constexpr unsigned WS_PART = WS_VP + 8 * 32 * 256 * 96;
constexpr unsigned WS_END = WS_PART + 8u * 2048 * 576 * 2;

template <int IMM>
DI void glds16(const char* sbase, unsigned voff, unsigned ldsbase) {
    asm volatile("s_add_u32 m0, %2, %3\n\ts_nop 0\n\tglobal_load_lds_dwordx4 %0, %1" ::"v"(voff), "s"(sbase), "s"(ldsbase), "i"(IMM) : "memory", "scc");
}
DI void glds4(const char* sbase, unsigned voff, unsigned ldsaddr) {
    asm volatile("s_mov_b32 m0, %2\n\ts_nop 0\n\tglobal_load_lds_dword %0, %1" ::"v"(voff), "s"(sbase), "s"(ldsaddr) : "memory");
}
#define SB0() __builtin_amdgcn_sched_barrier(0)
#define PIN(x) asm volatile("" : "+v"(x))
DI s16x4 tr_rd(LAS char* p) { return __builtin_amdgcn_ds_read_tr16_b64_v4i16((LAS s16x4*)p); }
DI unsigned pack_bf16(float a, float b) { bf16x2 p; p[0] = (__bf16)a; p[1] = (__bf16)b; return __builtin_bit_cast(unsigned, p); }
DI unsigned pack_f16(float a, float b) { f16x2 p; p[0] = (_Float16)a; p[1] = (_Float16)b; return __builtin_bit_cast(unsigned, p); }
DI float bf_lo(unsigned u) { return __uint_as_float(u << 16); }
DI float bf_hi(unsigned u) { return __uint_as_float(u & 0xffff0000u); }
DI float rbf(float f) { return bf_lo(pack_bf16(f, 0.f)); }
#define STAMP(i)
#define MFMA32(a, b, c) __builtin_amdgcn_mfma_f32_32x32x16_bf16((a), (b), (c), 0, 0, 0)
#define MFMA32H(a, b, c) __builtin_amdgcn_mfma_f32_32x32x16_f16((a), (b), (c), 0, 0, 0)
#define MFMA16(a, b, c) __builtin_amdgcn_mfma_f32_16x16x32_bf16((a), (b), (c), 0, 0, 0)
template <int N> DI void waitv() { asm volatile("s_waitcnt vmcnt(%0)" ::"i"(N) : "memory"); }
#define WAITL0() asm volatile("s_waitcnt lgkmcnt(0)" ::: "memory")
#define BAR() do { __builtin_amdgcn_s_barrier(); SB0(); } while (0)
#define RLX_AGENT __ATOMIC_RELAXED, __HIP_MEMORY_SCOPE_AGENT
template <int V> using IC = std::integral_constant<int, V>;
template <int A, int B, typename F> DI void static_for(F&& f) {
    if constexpr (A < B) { f(IC<A>{}); static_for<A + 1, B>(f); }
}
DI void split2(float a, float b, unsigned& hi, unsigned& lo) {
    hi = pack_bf16(a, b);
    lo = pack_bf16(a - bf_lo(hi), b - bf_hi(hi));
}
DI bf16x8 mk8(unsigned a, unsigned b, unsigned c, unsigned d) { u32x4 v = {a, b, c, d}; return __builtin_bit_cast(bf16x8, v); }
DI f16x8 mk8h(unsigned a, unsigned b, unsigned c, unsigned d) { u32x4 v = {a, b, c, d}; return __builtin_bit_cast(f16x8, v); }
DI int rho(int a, int hh) { return (a & 3) + 8 * (a >> 2) + 4 * hh; }
DI float red8(float v) {
    v += __builtin_bit_cast(float, __builtin_amdgcn_update_dpp(0, __builtin_bit_cast(int, v), 0x128, 0xf, 0xf, false));
    { auto r = __builtin_amdgcn_permlane16_swap(__float_as_uint(v), __float_as_uint(v), false, false); v = __uint_as_float(r[0]) + __uint_as_float(r[1]); }
    { auto r = __builtin_amdgcn_permlane32_swap(__float_as_uint(v), __float_as_uint(v), false, false); v = __uint_as_float(r[0]) + __uint_as_float(r[1]); }
    return v;
}

struct Dft24 { bf16x8 Ph[3], Pl[3], Qh[3], Ql[3]; };
DI void dft24_setup(Dft24& m, int lane) {
    const int col = lane & 31, hh = lane >> 5;
    float c[12], s[12];
#pragma unroll
    for (int a = 0; a < 12; a++) {
        float ang = (float)((rho(a, hh) * col) % 24) * (1.0f / 24.0f);
        c[a] = __builtin_amdgcn_cosf(ang); s[a] = __builtin_amdgcn_sinf(ang);
    }
    unsigned ch[6], cl[6], sh[6], sl[6];
#pragma unroll
    for (int i = 0; i < 6; i++) { split2(c[2 * i], c[2 * i + 1], ch[i], cl[i]); split2(s[2 * i], s[2 * i + 1], sh[i], sl[i]); }
    const unsigned N = 0x80008000u;
    m.Ph[0] = mk8(ch[0], ch[1], ch[2], ch[3]); m.Ph[1] = mk8(ch[4], ch[5], sh[0], sh[1]); m.Ph[2] = mk8(sh[2], sh[3], sh[4], sh[5]);
    m.Pl[0] = mk8(cl[0], cl[1], cl[2], cl[3]); m.Pl[1] = mk8(cl[4], cl[5], sl[0], sl[1]); m.Pl[2] = mk8(sl[2], sl[3], sl[4], sl[5]);
    m.Qh[0] = mk8(sh[0] ^ N, sh[1] ^ N, sh[2] ^ N, sh[3] ^ N); m.Qh[1] = mk8(sh[4] ^ N, sh[5] ^ N, ch[0], ch[1]); m.Qh[2] = mk8(ch[2], ch[3], ch[4], ch[5]);
    m.Ql[0] = mk8(sl[0] ^ N, sl[1] ^ N, sl[2] ^ N, sl[3] ^ N); m.Ql[1] = mk8(sl[4] ^ N, sl[5] ^ N, cl[0], cl[1]); m.Ql[2] = mk8(cl[2], cl[3], cl[4], cl[5]);
}
template <bool ASA>
DI void dft24_stage(const Dft24& m, float (&re)[12], float (&im)[12]) {
    unsigned h[12], l[12];
#pragma unroll
    for (int i = 0; i < 6; i++) { split2(re[2 * i], re[2 * i + 1], h[i], l[i]); split2(im[2 * i], im[2 * i + 1], h[6 + i], l[6 + i]); }
    f32x16 dr = {0, 0, 0, 0, 0, 0, 0, 0, 0, 0, 0, 0, 0, 0, 0, 0}, di = dr;
#pragma unroll
    for (int s = 0; s < 3; s++) {
        bf16x8 xh = mk8(h[4 * s], h[4 * s + 1], h[4 * s + 2], h[4 * s + 3]);
        bf16x8 xl = mk8(l[4 * s], l[4 * s + 1], l[4 * s + 2], l[4 * s + 3]);
        if (ASA) {
            dr = MFMA32(xl, m.Ph[s], dr); di = MFMA32(xl, m.Qh[s], di);
            dr = MFMA32(xh, m.Pl[s], dr); di = MFMA32(xh, m.Ql[s], di);
            dr = MFMA32(xh, m.Ph[s], dr); di = MFMA32(xh, m.Qh[s], di);
        } else {
            dr = MFMA32(m.Ph[s], xl, dr); di = MFMA32(m.Qh[s], xl, di);
            dr = MFMA32(m.Pl[s], xh, dr); di = MFMA32(m.Ql[s], xh, di);
            dr = MFMA32(m.Ph[s], xh, dr); di = MFMA32(m.Qh[s], xh, di);
        }
    }
#pragma unroll
    for (int a = 0; a < 12; a++) { re[a] = dr[a]; im[a] = di[a]; }
}
DI void tw576(float (&re)[12], float (&im)[12], int lane) {
    const int col = lane & 31, hh = lane >> 5;
#pragma unroll
    for (int a = 0; a < 12; a++) {
        float an = (float)(rho(a, hh) * col) * (1.0f / 576.0f);
        float c = __builtin_amdgcn_cosf(an), s = __builtin_amdgcn_sinf(an);
        float r = re[a] * c + im[a] * s;
        float i = im[a] * c - re[a] * s;
        re[a] = r; im[a] = i;
    }
}
struct Dft24h { f16x8 P[3], Q[3]; };
DI void dft24h_setup(Dft24h& m, int lane) {
    const int col = lane & 31, hh = lane >> 5;
    unsigned ch[6], sh[6];
#pragma unroll
    for (int i = 0; i < 6; i++) {
        float a0 = (float)((rho(2 * i, hh) * col) % 24) * (1.0f / 24.0f), a1 = (float)((rho(2 * i + 1, hh) * col) % 24) * (1.0f / 24.0f);
        ch[i] = pack_f16(__builtin_amdgcn_cosf(a0), __builtin_amdgcn_cosf(a1));
        sh[i] = pack_f16(__builtin_amdgcn_sinf(a0), __builtin_amdgcn_sinf(a1));
    }
    const unsigned N = 0x80008000u;
    m.P[0] = mk8h(ch[0], ch[1], ch[2], ch[3]); m.P[1] = mk8h(ch[4], ch[5], sh[0], sh[1]); m.P[2] = mk8h(sh[2], sh[3], sh[4], sh[5]);
    m.Q[0] = mk8h(sh[0] ^ N, sh[1] ^ N, sh[2] ^ N, sh[3] ^ N); m.Q[1] = mk8h(sh[4] ^ N, sh[5] ^ N, ch[0], ch[1]); m.Q[2] = mk8h(ch[2], ch[3], ch[4], ch[5]);
}
template <bool ASA>
DI void dft24h_stage(const Dft24h& m, float (&re)[12], float (&im)[12]) {
    unsigned h[12];
#pragma unroll
    for (int i = 0; i < 6; i++) { h[i] = pack_f16(re[2 * i], re[2 * i + 1]); h[6 + i] = pack_f16(im[2 * i], im[2 * i + 1]); }
    f32x16 dr = {0, 0, 0, 0, 0, 0, 0, 0, 0, 0, 0, 0, 0, 0, 0, 0}, di = dr;
#pragma unroll
    for (int s = 0; s < 3; s++) {
        f16x8 xh = mk8h(h[4 * s], h[4 * s + 1], h[4 * s + 2], h[4 * s + 3]);
        if (ASA) { dr = MFMA32H(xh, m.P[s], dr); di = MFMA32H(xh, m.Q[s], di); }
        else { dr = MFMA32H(m.P[s], xh, dr); di = MFMA32H(m.Q[s], xh, di); }
    }
#pragma unroll
    for (int a = 0; a < 12; a++) { re[a] = dr[a]; im[a] = di[a]; }
}
struct Dft16 { bf16x8 Ph, Pl, Qh, Ql; float twc[4], tws[4]; };
DI void dft16_setup(Dft16& m, int lane) {
    const int col = lane & 15, g = lane >> 4;
    float c[4], s[4];
#pragma unroll
    for (int e = 0; e < 4; e++) {
        int pr = (4 * g + e) * col;
        float ang = (float)(pr & 15) * (1.0f / 16.0f);
        c[e] = __builtin_amdgcn_cosf(ang); s[e] = __builtin_amdgcn_sinf(ang);
        float an2 = (float)pr * (1.0f / 256.0f);
        m.twc[e] = __builtin_amdgcn_cosf(an2); m.tws[e] = __builtin_amdgcn_sinf(an2);
    }
    unsigned ch[2], cl[2], sh[2], sl[2];
    split2(c[0], c[1], ch[0], cl[0]); split2(c[2], c[3], ch[1], cl[1]);
    split2(s[0], s[1], sh[0], sl[0]); split2(s[2], s[3], sh[1], sl[1]);
    const unsigned N = 0x80008000u;
    m.Ph = mk8(ch[0], ch[1], sh[0], sh[1]); m.Pl = mk8(cl[0], cl[1], sl[0], sl[1]);
    m.Qh = mk8(sh[0] ^ N, sh[1] ^ N, ch[0], ch[1]); m.Ql = mk8(sl[0] ^ N, sl[1] ^ N, cl[0], cl[1]);
}
template <bool ASA>
DI void dft16_stage(const Dft16& m, float (&re)[4], float (&im)[4]) {
    unsigned h[4], l[4];
    split2(re[0], re[1], h[0], l[0]); split2(re[2], re[3], h[1], l[1]);
    split2(im[0], im[1], h[2], l[2]); split2(im[2], im[3], h[3], l[3]);
    bf16x8 xh = mk8(h[0], h[1], h[2], h[3]), xl = mk8(l[0], l[1], l[2], l[3]);
    f32x4 dr = {0, 0, 0, 0}, di = dr;
    if (ASA) {
        dr = MFMA16(xl, m.Ph, dr); di = MFMA16(xl, m.Qh, di);
        dr = MFMA16(xh, m.Pl, dr); di = MFMA16(xh, m.Ql, di);
        dr = MFMA16(xh, m.Ph, dr); di = MFMA16(xh, m.Qh, di);
    } else {
        dr = MFMA16(m.Ph, xl, dr); di = MFMA16(m.Qh, xl, di);
        dr = MFMA16(m.Pl, xh, dr); di = MFMA16(m.Ql, xh, di);
        dr = MFMA16(m.Ph, xh, dr); di = MFMA16(m.Qh, xh, di);
    }
#pragma unroll
    for (int e = 0; e < 4; e++) { re[e] = dr[e]; im[e] = di[e]; }
}
DI void cmul_neg(float (&re)[4], float (&im)[4], const float (&c)[4], const float (&s)[4]) {
#pragma unroll
    for (int e = 0; e < 4; e++) {
        float r = re[e] * c[e] + im[e] * s[e];
        float i = im[e] * c[e] - re[e] * s[e];
        re[e] = r; im[e] = i;
    }
}
template <int NW>
DI void wait_flags(GAS u64* f, u64 tag, int idx) {
    for (unsigned spins = 0; spins < (1u << 20); spins++) {
        bool ok = true;
#pragma unroll
        for (int k = 0; k < NW; k++) ok &= (__hip_atomic_load(f + idx + 64 * k, RLX_AGENT) == tag);
        if (__all(ok)) break;
        __builtin_amdgcn_s_sleep(2);
    }
    __builtin_amdgcn_fence(__ATOMIC_ACQUIRE, "wavefront");
}

__global__ __launch_bounds__(512, 2) void k_fused(const uint16_t* __restrict__ x, const uint16_t* __restrict__ wr, const uint16_t* __restrict__ wi,
                                                  const uint16_t* __restrict__ w1, const uint16_t* __restrict__ b1, const uint16_t* __restrict__ w2,
                                                  const uint16_t* __restrict__ b2, uint16_t* __restrict__ out, char* __restrict__ ws
                                                  ) {
    extern __shared__ __attribute__((aligned(16))) char smem_g[];
    LAS char* smem = (LAS char*)smem_g;
    const int tid = threadIdx.x, lane = tid & 63;
    const int w = __builtin_amdgcn_readfirstlane(tid >> 6);
    const int jb = blockIdx.x, fs = jb & 7, t = jb >> 3;
    const unsigned lds0 = (unsigned)(uintptr_t)smem;
    const int rh = w & 1, ct = w >> 1;
    const __amdgpu_buffer_rsrc_t rs = __builtin_amdgcn_make_buffer_rsrc(ws, 0, WS_END, 0x00020000);
    STAMP(0);
    {
        const char* pc = (const char*)(__builtin_amdgcn_s_getpc() & ~127ull);
        unsigned o = tid * 128; if (o > CODE_BYTES) o = CODE_BYTES;
        glds4(pc, o, lds0 + LDS_JUNK);
    }
    const int hb = 2 * ((t >> 2) % 5), r4 = t & 3;
    int sbk[5];
#pragma unroll
    for (int i = 0; i < 4; i++) { int ii = (i + r4) & 3; sbk[i] = __builtin_amdgcn_readfirstlane(2 * ii + (2 * ii >= hb ? 1 : 0)); }
    sbk[4] = __builtin_amdgcn_readfirstlane(hb);
    const char* w1b = (const char*)w1 + fs * 1152;
    const char* w2b = (const char*)w2 + (size_t)fs * 576 * 1152;
    const unsigned ldsw = lds0 + w * 1024;
    auto mkvo = [&](bool full, unsigned pitch) {
        int ln = tid; PIN(ln); ln &= 63;
        unsigned r = full ? 4 * w + (ln >> 4) : 8 * w + (ln >> 3);
        unsigned c = full ? ((((ln >> 2) & 3) ^ (ln >> 4)) * 64) + (ln & 3) * 16 : ((((ln >> 2) & 1) ^ ((ln >> 4) & 1)) * 64) + (ln & 3) * 16;
        return r * pitch + c;
    };
    unsigned vo;
    auto gl = [&](auto U_, auto J_) {
        constexpr int U = decltype(U_)::value, J = decltype(J_)::value;
        constexpr int ph = U / 27, v = U % 27;
        constexpr int DST = (U % 6) * RING_SLOT + J * 8192;
        if constexpr (U < 54) {
            constexpr int R = v < 3 ? 192 * v + 64 * J : 96 * ((v - 3) % 6) + 32 * J;
            const int blk = sbk[v < 3 ? 4 : (v - 3) / 6];
            if constexpr (ph == 0) glds16<DST>(w1b + (unsigned)__builtin_amdgcn_readfirstlane(blk * 128 + R * 9216), vo, ldsw);
            else glds16<DST>(w2b + (unsigned)__builtin_amdgcn_readfirstlane(blk * 128 + (64 * sbk[R / 128] + R % 128) * 1152), vo, ldsw);
        }
    };
    auto setvo = [&](auto U_) {
        constexpr int U = decltype(U_)::value;
        if constexpr (U == 0) vo = mkvo(false, 9216);
        if constexpr (U == 3) vo = mkvo(true, 9216);
        if constexpr (U == 27) vo = mkvo(false, 1152);
        if constexpr (U == 30) vo = mkvo(true, 1152);
    };
    auto gl3 = [&](auto U_) { setvo(U_); static_for<0, 3>([&](auto J_) { gl(U_, J_); }); };

    {
        const int i = lane & 7, c = lane >> 3;
        const uint16_t* xr = x + (size_t)(8 * t + w + 256 * i) * 576 + c * 8;
        u32x4 q[9];
#pragma unroll
        for (int n = 0; n < 9; n++) q[n] = *(const u32x4*)(xr + n * 64);
        u64 ep = 0;
        if (tid == 0) ep = __hip_atomic_load((GAS u64*)(ws + WS_EPOCH), RLX_AGENT);
        u32x4 bb = {0, 0, 0, 0};
        const bool dob = tid >= 384 && tid < 384 + 72;
        if (dob) bb = *(const u32x4*)((const char*)b1 + fs * 1152 + (tid - 384) * 16);
        SB0();
        gl3(IC<0>{}); gl3(IC<1>{}); gl3(IC<2>{});
        SB0();
        f32x2 d[36];
        f32x2 s2 = {0.f, 0.f};
#pragma unroll
        for (int n = 0; n < 9; n++)
#pragma unroll
            for (int e = 0; e < 4; e++) { f32x2 v = {bf_lo(q[n][e]), bf_hi(q[n][e])}; d[4 * n + e] = v; s2 += v; }
        const float mu = red8(s2[0] + s2[1]) * (1.0f / 576.0f);
        const f32x2 mu2 = {mu, mu};
        f32x2 q2 = {0.f, 0.f};
#pragma unroll
        for (int e = 0; e < 36; e++) { d[e] -= mu2; q2 = __builtin_elementwise_fma(d[e], d[e], q2); }
        const float var = red8(q2[0] + q2[1]) * (1.0f / 576.0f);
        const float m1 = rbf(mu);
        const float is1 = 1.0f / rbf(__builtin_sqrtf(rbf(rbf(var) + 1e-5f)));
        const float mean2 = (mu - m1) * is1;
        const float var2 = var * is1 * is1;
        const float is2 = 1.0f / rbf(__builtin_sqrtf(rbf(rbf(var2) + 1e-5f)));
        const float ya = is1 * is2, yb = (mean2 - rbf(mean2)) * is2;
        const f32x2 ya2 = {ya, ya}, yb2 = {yb, yb};
        const int mloc = 8 * w + i;
        LAS char* yw = smem + LDS_Y + (mloc >> 5) * 36 * 1024 + (c >> 1) * 1024 + ((mloc & 31) + 32 * (c & 1)) * 16;
#pragma unroll
        for (int n = 0; n < 9; n++) {
            u32x4 o;
#pragma unroll
            for (int e = 0; e < 4; e++) { f32x2 v = __builtin_elementwise_fma(d[4 * n + e], ya2, yb2); o[e] = pack_bf16(v[0], v[1]); }
            *(LAS u32x4*)(yw + n * 4096) = o;
        }
        if (w == fs) {
            const f32x2 xa = {is1, is1}, xb = {mean2, mean2};
            const int pos = (i & 1) ? 4 + (i >> 1) : ((i >> 2) | (i & 2));
            LAS char* xw = smem + LDS_XS + pos * 1152 + c * 16;
#pragma unroll
            for (int n = 0; n < 9; n++) {
                u32x4 o;
#pragma unroll
                for (int e = 0; e < 4; e++) { f32x2 v = __builtin_elementwise_fma(d[4 * n + e], xa, xb); o[e] = pack_bf16(v[0], v[1]); }
                *(LAS u32x4*)(xw + n * 128) = o;
            }
        }
        if (dob) {
            f32x4 lo = {bf_lo(bb[0]), bf_hi(bb[0]), bf_lo(bb[1]), bf_hi(bb[1])}, hi = {bf_lo(bb[2]), bf_hi(bb[2]), bf_lo(bb[3]), bf_hi(bb[3])};
            *(LAS f32x4*)(smem + LDS_BIAS + (tid - 384) * 32) = lo;
            *(LAS f32x4*)(smem + LDS_BIAS + (tid - 384) * 32 + 16) = hi;
        }
        if (tid == 0) *(LAS u64*)(smem + LDS_TAG) = ep;
        waitv<0>();
        WAITL0();
        int li = w * 21 + (lane % 21); if (li > 161) li = 161;
        int rr = li / 9, cc = li % 9;
        glds4(w1b, (18 * t + rr) * 9216 + cc * 128, lds0 + LDS_JUNK);
        glds4(w2b, (18 * t + rr) * 1152 + cc * 128, lds0 + LDS_JUNK);
    }
    BAR();
    STAMP(1);
    u64 epoch;
    {
        u32x2 e2 = *(LAS u32x2*)(smem + LDS_TAG);
        epoch = ((u64)(unsigned)__builtin_amdgcn_readfirstlane(e2[1]) << 32) | (unsigned)__builtin_amdgcn_readfirstlane(e2[0]);
    }
    const u64 tag = (epoch + 1) * 0x9E3779B97F4A7C15ull + 0x632BE59BD9B4E019ull;

    auto put_flag = [&](bool me, unsigned off, u64 val) {
        u32x2 o = {(unsigned)val, (unsigned)(val >> 32)};
        __builtin_amdgcn_raw_buffer_store_b64(o, rs, me ? 0u : 0xF0000000u, off, 16);
    };
    LAS char* trc;
    auto settr = [&](auto U_) {
        constexpr int U = decltype(U_)::value, v = U % 27, ph = U / 27;
        if constexpr (v == 0 || v == 3) {
            int ln = tid; PIN(ln); ln &= 63;
            int G = ln >> 4, q = (ln >> 2) & 3, p = ln & 3;
            int row = (ph == 0 ? 8 : 4) * (G >> 1) + q;
            if constexpr (v == 3) trc = smem + row * 256 + ((ct ^ q) * 64) + (G & 1) * 32 + p * 8 + ((U % 6) / 2) * 2 * RING_SLOT;
            else trc = smem + row * 128 + (((ct & 1) ^ ((q >> 1) & 1)) * 64) + (G & 1) * 32 + p * 8 + ((U % 6) / 2) * 2 * RING_SLOT;
            PIN(trc);
        } else if constexpr (U % 2 == 0) {
            trc += (U % 6 == 0) ? -4 * RING_SLOT : 2 * RING_SLOT;
            PIN(trc);
        }
    };
    bf16x8 af[36];
    {
        LAS char* ab = smem + LDS_Y + rh * 36 * 1024 + lane * 16; PIN(ab);
#pragma unroll
        for (int j = 0; j < 36; j++) af[j] = *(LAS bf16x8*)(ab + j * 1024);
    }
    LAS char* biasp = smem + LDS_BIAS + (32 * ct + 4 * (lane >> 5)) * 4;
    const unsigned pbase = WS_PART + (unsigned)(((fs * 32 + t) * 36 + 2 * ct + rh) * 2048);
    unsigned plane = 0;

    f32x16 acc[2];
    s16x4 bl[6], bh[6];
    float gt[4];
    unsigned hq[5][8];
    auto ldbias = [&](auto P_, auto Q_, int blk) {
        constexpr int P = decltype(P_)::value, Q = decltype(Q_)::value;
        f32x4 b = *(LAS f32x4*)(biasp + blk * 256 + Q * 32);
        acc[P][4 * Q] = b[0]; acc[P][4 * Q + 1] = b[1]; acc[P][4 * Q + 2] = b[2]; acc[P][4 * Q + 3] = b[3];
    };
    auto gop = [&](auto C_, auto K_) {
        constexpr int C = decltype(C_)::value, K = decltype(K_)::value, P = C == 4 ? 1 : (C & 1);
        constexpr int NB = C == 4 ? 1 : C + 2;
        const float c0 = -2.0f * 0.7978845608028654f * 1.4426950408889634f;
        const float c1 = c0 * 0.044715f;
        if constexpr (K < 124) {
            constexpr int Q = K / 31, k = K % 31;
            if constexpr (k < 28) {
                constexpr int st = k / 4, e = k % 4;
                float xx = acc[P][4 * Q + e];
                if constexpr (st == 0) gt[e] = xx * xx;
                else if constexpr (st == 1) gt[e] = __builtin_fmaf(gt[e], c1, c0);
                else if constexpr (st == 2) gt[e] = xx * gt[e];
                else if constexpr (st == 3) gt[e] = __builtin_amdgcn_exp2f(gt[e]);
                else if constexpr (st == 4) gt[e] = 1.0f + gt[e];
                else if constexpr (st == 5) gt[e] = __builtin_amdgcn_rcpf(gt[e]);
                else gt[e] = xx * gt[e];
                PIN(gt[e]);
            } else if constexpr (k == 28) { hq[C][2 * Q] = pack_bf16(gt[0], gt[1]); PIN(hq[C][2 * Q]); }
            else if constexpr (k == 29) { hq[C][2 * Q + 1] = pack_bf16(gt[2], gt[3]); PIN(hq[C][2 * Q + 1]); }
            else { if constexpr (NB <= 3) ldbias(IC<P>{}, IC<Q>{}, sbk[NB]); }
        }
    };
    auto grange = [&](auto C_, auto A_, auto B_) {
        static_for<decltype(A_)::value, decltype(B_)::value>([&](auto K_) { gop(C_, K_); });
    };
    auto pstore = [&](auto P_, auto H_, int blk) {
        constexpr int P = decltype(P_)::value, H = decltype(H_)::value;
        u32x4 o;
        o[0] = pack_bf16(acc[P][8 * H + 0], acc[P][8 * H + 1]); o[1] = pack_bf16(acc[P][8 * H + 2], acc[P][8 * H + 3]);
        o[2] = pack_bf16(acc[P][8 * H + 4], acc[P][8 * H + 5]); o[3] = pack_bf16(acc[P][8 * H + 6], acc[P][8 * H + 7]);
        __builtin_amdgcn_raw_buffer_store_b128(o, rs, plane, pbase + blk * 8192 + H * 1024, 16);
    };
    const f32x16 zero16 = {0, 0, 0, 0, 0, 0, 0, 0, 0, 0, 0, 0, 0, 0, 0, 0};
    auto bfrag = [&](auto J_) {
        constexpr int J = decltype(J_)::value;
        return __builtin_bit_cast(bf16x8, __builtin_shufflevector(bl[J], bh[J], 0, 1, 2, 3, 4, 5, 6, 7));
    };
    auto fslot = [&](auto U_, auto LO_, auto GI_, auto&& fill) {
        constexpr int U = decltype(U_)::value, ph = U / 27, v = U % 27 - 3, c = v / 6, s = v % 6;
        constexpr bool LO = decltype(LO_)::value;
        constexpr int PB = c & 1;
        constexpr int PA = s == 0 ? PB ^ 1 : PB;
        constexpr int sp = (s + 5) % 6;
        constexpr int R2 = ph == 0 ? 1024 : 2048;
        settr(U_);
        LAS char* sb = trc + (U % 2) * RING_SLOT;
        setvo(GI_);
        static_for<0, 6>([&](auto G_) {
            constexpr int G = decltype(G_)::value;
            if constexpr (G < 3) {
                if constexpr (LO) acc[PA] = MFMA32(bfrag(IC<G + 3>{}), af[6 * sp + G + 3], acc[PA]);
            } else {
                acc[PB] = MFMA32(bfrag(IC<G - 3>{}), af[6 * s + G - 3], (ph == 1 && s == 0 && G == 3) ? zero16 : acc[PB]);
            }
            SB0();
            bl[G] = tr_rd(sb + G * 4096); bh[G] = tr_rd(sb + G * 4096 + R2);
            if constexpr (G < 3) gl(GI_, G_);
            fill(G_);
            SB0();
        });
    };
    auto hslot = [&](auto U_, auto GI_, auto GI2_) {
        constexpr int U = decltype(U_)::value, ph = U / 27, s = U % 27;
        constexpr int R2 = ph == 0 ? 512 : 1024;
        settr(U_);
        LAS char* sb = trc + (U % 2) * RING_SLOT;
        setvo(GI_);
        static_for<0, 12>([&](auto G_) {
            constexpr int G = decltype(G_)::value;
            if constexpr (G < 3) {
                if constexpr (s > 0) acc[1] = MFMA32(bfrag(IC<G + 3>{}), af[12 * s - 3 + G], acc[1]);
            } else {
                acc[1] = MFMA32(bfrag(IC<(G - 3) % 6>{}), af[12 * s + G - 3], (ph == 1 && s == 0 && G == 3) ? zero16 : acc[1]);
            }
            SB0();
            bl[G % 6] = tr_rd(sb + G * 2048); bh[G % 6] = tr_rd(sb + G * 2048 + R2);
            if constexpr (G < 6 && G % 2 == 0) gl(GI_, IC<G / 2>{});
            if constexpr (G >= 6 && G % 2 == 0) gl(GI2_, IC<G / 2 - 3>{});
            SB0();
        });
    };
    using T = std::true_type; using F = std::false_type;
    auto nofill = [&](auto) {};

    if (w < 4) {
        static_for<0, 4>([&](auto Q_) { ldbias(IC<1>{}, Q_, sbk[4]); ldbias(IC<0>{}, Q_, sbk[0]); });
        hslot(IC<0>{}, IC<99>{}, IC<99>{}); WAITL0(); BAR();
        hslot(IC<1>{}, IC<3>{}, IC<4>{}); BAR();
        hslot(IC<2>{}, IC<5>{}, IC<99>{}); waitv<6>(); BAR();
        STAMP(9);
        static_for<3, 9>([&](auto U_) {
            constexpr int U = decltype(U_)::value, s = U - 3;
            fslot(U_, T{}, IC<U + 3>{}, [&](auto G_) {
                constexpr int gi = 6 * s + decltype(G_)::value - 4;
                if constexpr (gi >= 0) grange(IC<4>{}, IC<(gi * 124) / 32>{}, IC<((gi + 1) * 124) / 32>{});
            });
            waitv<6>(); BAR();
        });
    } else {
        const int v = w - 4;
        Dft24h m; dft24h_setup(m, lane);
        WAITL0(); BAR();
        gl3(IC<3>{}); gl3(IC<4>{});
        const unsigned PP = 0x3F803F80u, PM = 0xBF803F80u  , MP = 0x3F80BF80u  , MM = 0xBF80BF80u;
        unsigned A0, A1, A2, A3, B0, B1, B2, B3; float cc;
        const float c8 = 0.70710678118654752f;
        if (v == 0) { A0 = PP; A1 = PP; A2 = PP; A3 = PP; B0 = PP; B1 = PP; B2 = MM; B3 = MM; cc = 1.f; }
        else if (v == 1) { A0 = PM; A1 = 0; A2 = PM; A3 = MP; B0 = 0; B1 = MP; B2 = MM; B3 = PP; cc = c8; }
        else if (v == 2) { A0 = PP; A1 = MM; A2 = 0; A3 = 0; B0 = 0; B1 = 0; B2 = MP; B3 = MP; cc = 1.f; }
        else { A0 = PM; A1 = 0; A2 = MP; A3 = PM; B0 = 0; B1 = PM; B2 = MM; B3 = PP; cc = c8; }
        float re[12], im[12];
        auto dot = [](unsigned a, unsigned b, float c) { return __builtin_amdgcn_fdot2_f32_bf16(__builtin_bit_cast(bf16x2, a), __builtin_bit_cast(bf16x2, b), c, false); };
        {
            const int G = lane >> 4, q = (lane >> 2) & 3, p = lane & 3;
            LAS char* xb = smem + LDS_XS + q * 1152 + (96 * (G >> 1) + 16 * (G & 1) + 4 * p) * 2;
#pragma unroll
            for (int a = 0; a < 12; a++) {
                u32x2 ev = __builtin_bit_cast(u32x2, tr_rd(xb + rho(a, 0) * 48));
                u32x2 od = __builtin_bit_cast(u32x2, tr_rd(xb + rho(a, 0) * 48 + 4 * 1152));
                re[a] = __builtin_fmaf(dot(od[0], A2, dot(od[1], A3, 0.f)), cc, dot(ev[0], A0, dot(ev[1], A1, 0.f)));
                im[a] = __builtin_fmaf(dot(od[0], B2, dot(od[1], B3, 0.f)), cc, dot(ev[0], B0, dot(ev[1], B1, 0.f)));
            }
        }
        dft24h_stage<true>(m, re, im);
        SB0(); BAR();
        gl3(IC<5>{});
        tw576(re, im, lane);
        dft24h_stage<false>(m, re, im);
        {
            const int hh = lane >> 5;
            const unsigned zo = (lane & 31) < 24 ? (unsigned)(((lane & 31) + 96 * hh) * 8) : 0xFFFFFF00u;
            const unsigned zb = WS_ZV + (unsigned)(jb * 4 + v) * ZV_PITCH;
#pragma unroll
            for (int a = 0; a < 12; a++) {
                u32x2 o = {__float_as_uint(re[a]), __float_as_uint(im[a])};
                __builtin_amdgcn_raw_buffer_store_b64(o, rs, zo, zb + rho(a, 0) * 192, 16);
            }
            u32x2 o = {__float_as_uint(re[0]), __float_as_uint(im[0])};
            __builtin_amdgcn_raw_buffer_store_b64(o, rs, lane == 0 ? 0u : 0xFFFFFF00u, zb + 576 * 8, 16);
        }
        static_for<0, 4>([&](auto Q_) { ldbias(IC<0>{}, Q_, sbk[0]); ldbias(IC<1>{}, Q_, sbk[1]); });
        waitv<19>(); BAR();
        static_for<3, 9>([&](auto U_) {
            constexpr int U = decltype(U_)::value;
            if constexpr (U == 3) fslot(U_, F{}, IC<U + 3>{}, nofill); else fslot(U_, T{}, IC<U + 3>{}, nofill);
            if constexpr (U == 3) waitv<19>(); else if constexpr (U == 4) waitv<19>(); else waitv<6>();
            BAR();
            if constexpr (U == 5) __builtin_amdgcn_raw_buffer_store_b32((unsigned)(tag >> 32), rs, tid == 256 ? 0u : 0xF0000000u, WS_ROWF + jb * 4, 16);
        });
    }
    STAMP(10);
    u32x4 rf;
    static_for<9, 27>([&](auto U_) {
        constexpr int U = decltype(U_)::value, c = (U - 3) / 6, s = (U - 3) % 6;
        fslot(U_, T{}, IC<U + 3>{}, [&](auto G_) {
            constexpr int gi = 6 * s + decltype(G_)::value - 4;
            if constexpr (gi >= 0) grange(IC<c - 1>{}, IC<(gi * 124) / 32>{}, IC<((gi + 1) * 124) / 32>{});
            if constexpr (U == 24 && decltype(G_)::value == 4) {
                int ln = tid; PIN(ln); ln &= 63;
                rf = __builtin_amdgcn_raw_buffer_load_b128(rs, ln * 16, WS_ROWF, 16);
            }
        });
        if constexpr (U < 24) { waitv<6>(); BAR(); }
        else if constexpr (U < 26) { waitv<7>(); BAR(); }
    });
    static_for<0, 3>([&](auto G_) { constexpr int G = decltype(G_)::value; acc[1] = MFMA32(bfrag(IC<G + 3>{}), af[33 + G], acc[1]); });
    STAMP(2);
    WAITL0();
    waitv<0>();
    BAR();
    u32x2 wqr[4], wqi[4];
    u32x4 za4[4], zb4[4]; u32x2 za2[4], zb2[4];
    float re[3][4], im[3][4];
    if (w >= 4) {
        int ln = tid; PIN(ln); ln &= 63;
        const int r = fs, n2 = ln & 15, g = ln >> 4;
        const int k0 = 9 * t + 3 * (w - 4);
        const unsigned tg = (unsigned)(tag >> 32);
        for (unsigned spins = 0; spins < (1u << 20); spins++) {
            if (__all(rf[0] == tg && rf[1] == tg && rf[2] == tg && rf[3] == tg)) break;
            __builtin_amdgcn_s_sleep(2);
            rf = __builtin_amdgcn_raw_buffer_load_b128(rs, ln * 16, WS_ROWF, 16);
        }
        __builtin_amdgcn_fence(__ATOMIC_ACQUIRE, "wavefront");
        put_flag(jb == 0 && tid == 256, WS_EPOCH, epoch + 1);
        const int vA = r <= 3 ? r : 0, vB = r >= 5 ? 8 - r : 0;
        const bool useA = r <= 4, useB = (r == 0 || r >= 4);
        const unsigned lo = (unsigned)(64 * g + n2) * 4 * ZV_PITCH;
        const unsigned loA = useA ? lo : 0xF0000000u, loB = useB ? lo : 0xF0000000u;
        const unsigned sA = WS_ZV + vA * ZV_PITCH + k0 * 8, sB = WS_ZV + vB * ZV_PITCH + (574 - k0) * 8;
#pragma unroll
        for (int e = 0; e < 4; e++) {
            za4[e] = __builtin_amdgcn_raw_buffer_load_b128(rs, loA, sA + e * 64 * ZV_PITCH, 16);
            za2[e] = __builtin_amdgcn_raw_buffer_load_b64(rs, loA, sA + e * 64 * ZV_PITCH + 16, 16);
            zb4[e] = __builtin_amdgcn_raw_buffer_load_b128(rs, loB, sB + e * 64 * ZV_PITCH, 16);
            zb2[e] = __builtin_amdgcn_raw_buffer_load_b64(rs, loB, sB + e * 64 * ZV_PITCH + 16, 16);
        }
        const int ks = k0 > 285 ? 285 : k0;
        const unsigned wo = (unsigned)((8 * (n2 + 64 * g) + r) * 289 + ks) * 2;
#pragma unroll
        for (int e = 0; e < 4; e++) {
            __builtin_memcpy(&wqr[e], (const char*)wr + wo + e * (128 * 289 * 2), 8);
            __builtin_memcpy(&wqi[e], (const char*)wi + wo + e * (128 * 289 * 2), 8);
        }
        SB0();
    }
    {
        int ln = tid; PIN(ln); ln &= 63;
        plane = ln * 16;
        LAS char* ab = smem + LDS_H + rh * 36 * 1024 + ln * 16;
        LAS char* hw = ab + 2 * ct * 1024;
        auto hst = [&](auto C_) {
            constexpr int C = decltype(C_)::value;
            u32x4 o0 = {hq[C][0], hq[C][1], hq[C][2], hq[C][3]}, o1 = {hq[C][4], hq[C][5], hq[C][6], hq[C][7]};
            *(LAS u32x4*)(hw + C * 8192) = o0; *(LAS u32x4*)(hw + C * 8192 + 1024) = o1;
        };
        hst(IC<0>{}); hst(IC<1>{}); hst(IC<2>{});
        if (w < 4) hst(IC<4>{});
        grange(IC<3>{}, IC<0>{}, IC<124>{});
        hst(IC<3>{});
        WAITL0();
        BAR();
        if (w >= 4) {
            const float sg = fs == 4 ? -1.f : 1.f;
#pragma unroll
            for (int e = 0; e < 4; e++) {
                re[0][e] = __builtin_fmaf(sg, __uint_as_float(zb2[e][0]), __uint_as_float(za4[e][0])); im[0][e] = __builtin_fmaf(-sg, __uint_as_float(zb2[e][1]), __uint_as_float(za4[e][1]));
                re[1][e] = __builtin_fmaf(sg, __uint_as_float(zb4[e][2]), __uint_as_float(za4[e][2])); im[1][e] = __builtin_fmaf(-sg, __uint_as_float(zb4[e][3]), __uint_as_float(za4[e][3]));
                re[2][e] = __builtin_fmaf(sg, __uint_as_float(zb4[e][0]), __uint_as_float(za2[e][0])); im[2][e] = __builtin_fmaf(-sg, __uint_as_float(zb4[e][1]), __uint_as_float(za2[e][1]));
#pragma unroll
                for (int c = 0; c < 3; c++) { PIN(re[c][e]); PIN(im[c][e]); }
            }
            SB0();
        }
#pragma unroll
        for (int j = 0; j < 36; j++) af[j] = *(LAS bf16x8*)(ab + j * 1024);
    }
    STAMP(3);
    if (w < 4) {
        hslot(IC<27>{}, IC<99>{}, IC<99>{}); WAITL0(); BAR();
        hslot(IC<28>{}, IC<30>{}, IC<31>{}); BAR();
        hslot(IC<29>{}, IC<32>{}, IC<99>{}); waitv<6>(); BAR();
        STAMP(11);
        fslot(IC<30>{}, T{}, IC<33>{}, [&](auto G_) { if constexpr (decltype(G_)::value == 5) pstore(IC<1>{}, IC<0>{}, sbk[4]); }); waitv<7>(); BAR();
        fslot(IC<31>{}, T{}, IC<34>{}, [&](auto G_) { if constexpr (decltype(G_)::value == 5) pstore(IC<1>{}, IC<1>{}, sbk[4]); }); waitv<8>(); BAR();
        fslot(IC<32>{}, T{}, IC<35>{}, nofill); waitv<8>(); BAR();
        fslot(IC<33>{}, T{}, IC<36>{}, nofill); waitv<7>(); BAR();
    } else {
        const int r = fs, n2 = lane & 15, g = lane >> 4;
        Dft16 m; dft16_setup(m, lane);
        float jc[4], js[4];
        {
            float ic[4], is[4];
#pragma unroll
            for (int e = 0; e < 4; e++) {
                float ang = (float)((64 * g + 16 * e + n2) * r) * (1.0f / 2048.0f);
                jc[e] = __builtin_amdgcn_cosf(ang); js[e] = __builtin_amdgcn_sinf(ang);
                ic[e] = r == 0 ? 0.5f * jc[e] : r == 4 ? -0.5f * js[e] : jc[e];
                is[e] = r == 0 ? 0.5f * js[e] : r == 4 ? 0.5f * jc[e] : js[e];
            }
            WAITL0(); SB0(); BAR();
            gl3(IC<30>{}); gl3(IC<31>{});
#pragma unroll
            for (int c = 0; c < 3; c++) cmul_neg(re[c], im[c], ic, is);
        }
#pragma unroll
        for (int c = 0; c < 3; c++) dft16_stage<true>(m, re[c], im[c]);
#pragma unroll
        for (int c = 0; c < 3; c++) cmul_neg(re[c], im[c], m.twc, m.tws);
#pragma unroll
        for (int c = 0; c < 3; c++) dft16_stage<false>(m, re[c], im[c]);
#pragma unroll
        for (int c = 0; c < 3; c++)
#pragma unroll
            for (int e = 0; e < 4; e++) {
                const float sc = 1.0f / (2048.0f * 576.0f);
                const bool last = (t == 31 && w == 7);
                const float wre = (c == 0 ? (last ? bf_hi(wqr[e][1]) : bf_lo(wqr[e][0])) : c == 1 ? bf_hi(wqr[e][0]) : bf_lo(wqr[e][1])) * sc;
                const float wim = (c == 0 ? (last ? bf_hi(wqi[e][1]) : bf_lo(wqi[e][0])) : c == 1 ? bf_hi(wqi[e][0]) : bf_lo(wqi[e][1])) * sc;
                float a = re[c][e] * wre - im[c][e] * wim;
                float b = -(re[c][e] * wim + im[c][e] * wre);
                re[c][e] = a; im[c][e] = b;
            }
        SB0(); BAR();
        gl3(IC<32>{});
#pragma unroll
        for (int c = 0; c < 3; c++) dft16_stage<true>(m, re[c], im[c]);
#pragma unroll
        for (int c = 0; c < 3; c++) cmul_neg(re[c], im[c], m.twc, m.tws);
#pragma unroll
        for (int c = 0; c < 3; c++) dft16_stage<false>(m, re[c], im[c]);
#pragma unroll
        for (int c = 0; c < 3; c++) cmul_neg(re[c], im[c], jc, js);
        {
            const unsigned vo_ = (unsigned)(64 * g + n2) * 96;
            const unsigned vb = WS_VP + (unsigned)(r * 32 + t) * (256 * 96) + (w - 4) * 24;
#pragma unroll
            for (int e = 0; e < 4; e++) {
                u32x4 o4 = {__float_as_uint(re[0][e]), __float_as_uint(im[0][e]), __float_as_uint(re[1][e]), __float_as_uint(im[1][e])};
                u32x2 o2 = {__float_as_uint(re[2][e]), __float_as_uint(im[2][e])};
                __builtin_amdgcn_raw_buffer_store_b128(o4, rs, vo_, vb + e * 16 * 96, 16);
                __builtin_amdgcn_raw_buffer_store_b64(o2, rs, vo_, vb + e * 16 * 96 + 16, 16);
            }
        }
        waitv<14>(); BAR();
        fslot(IC<30>{}, F{}, IC<33>{}, nofill); waitv<14>(); BAR();
        fslot(IC<31>{}, T{}, IC<34>{}, nofill); waitv<14>(); BAR();
        fslot(IC<32>{}, T{}, IC<35>{}, nofill); waitv<6>(); BAR();
        put_flag(tid == 256, WS_COLF + (fs * 32 + t) * 8, tag);
        fslot(IC<33>{}, T{}, IC<36>{}, nofill); waitv<6>(); BAR();
    }
    STAMP(12);
    static_for<34, 54>([&](auto U_) {
        constexpr int U = decltype(U_)::value, v = U - 30, c = v / 6, s = v % 6;
        auto fill = [&](auto G_) {
            constexpr int G = decltype(G_)::value;
            if constexpr (c >= 1 && s == 0 && G == 5) pstore(IC<(c + 1) & 1>{}, IC<0>{}, sbk[c >= 1 ? c - 1 : 0]);
            if constexpr (c >= 1 && s == 1 && G == 5) pstore(IC<(c + 1) & 1>{}, IC<1>{}, sbk[c >= 1 ? c - 1 : 0]);
        };
        constexpr int ns = (c >= 1) ? ((s == 0 ? 1 : 0) + (s == 1 ? 2 : 0) + (s == 2 ? 2 : 0) + (s == 3 ? 1 : 0)) : 0;
        fslot(U_, T{}, IC<U + 3>{}, fill);
        if constexpr (U <= 50) { waitv<6 + ns>(); BAR(); }
        else if constexpr (U == 51) { waitv<3 + ns>(); BAR(); }
        else if constexpr (U == 52) { waitv<0>(); BAR(); }
    });
    static_for<0, 3>([&](auto G_) { constexpr int G = decltype(G_)::value; acc[1] = MFMA32(bfrag(IC<G + 3>{}), af[33 + G], acc[1]); });
    pstore(IC<1>{}, IC<0>{}, sbk[3]);
    pstore(IC<1>{}, IC<1>{}, sbk[3]);
    STAMP(4);
    WAITL0();
    waitv<0>();
    BAR();
    STAMP(5);
    put_flag(tid == 0, WS_PARTF + (t * 16 + fs) * 8, tag);
    if (w >= 4) {
        const int v = w - 4, hh = lane >> 5;
        int n2 = lane & 31; if (n2 > 23) n2 = 23;
        const int rB = v == 0 ? 4 : 8 - v;
        wait_flags<1>((GAS u64*)(ws + WS_COLF), tag, (lane < 32 ? v : rB) * 32 + (lane & 31));
        float re[12], im[12];
        {
            u32x2 A[12], B[12];
#pragma unroll
            for (int a = 0; a < 12; a++) {
                const int k = 24 * rho(a, hh) + n2;
                const bool mir = k > 288; const int kk = mir ? 576 - k : k;
                int gq = (kk * 57) >> 9; if (gq > 31) gq = 31;
                const unsigned off = (unsigned)gq * (256 * 96) + (unsigned)(kk - 9 * gq) * 8;
                const unsigned rsel = (v != 0 && mir) ? rB : v;
                A[a] = __builtin_amdgcn_raw_buffer_load_b64(rs, off + rsel * (32 * 256 * 96), WS_VP + jb * 96, 16);
                const bool needB = v == 0 || k == 0 || k == 288;
                if (a == 0 || a == 4 || true) B[a] = __builtin_amdgcn_raw_buffer_load_b64(rs, needB ? off + rB * (32 * 256 * 96) : 0xF0000000u, WS_VP + jb * 96, 16);
            }
#pragma unroll
            for (int a = 0; a < 12; a++) {
                const int k = 24 * rho(a, hh) + n2;
                const bool mir = k > 288, spec = (k == 0 || k == 288);
                float ax = __uint_as_float(A[a][0]), ay = __uint_as_float(A[a][1]), bx = __uint_as_float(B[a][0]), by = __uint_as_float(B[a][1]);
                if (mir) { ay = -ay; by = -by; }
                if (v == 0) {
                    if (spec) { ay = 0.f; by = 0.f; }
                    re[a] = ax + by; im[a] = ay - bx;
                } else {
                    re[a] = spec ? 0.5f * (ax + bx) : ax; im[a] = spec ? 0.5f * (ay - by) : ay;
                }
            }
        }
        Dft24 m; dft24_setup(m, lane);
        dft24_stage<true>(m, re, im);
        tw576(re, im, lane);
        dft24_stage<false>(m, re, im);
        if ((lane & 31) < 24) {
#pragma unroll
            for (int a = 0; a < 12; a++) {
                f32x2 o = {re[a], -im[a]};
                *(LAS f32x2*)(smem + LDS_CB + (v * 576 + (lane & 31) + 24 * rho(a, hh)) * 8) = o;
            }
        }
        WAITL0();
    } else if (w == 0) {
        wait_flags<1>((GAS u64*)(ws + WS_PARTF) + t * 16, tag, lane & 7);
    }
    BAR();
    STAMP(6);
#pragma unroll
    for (int rep = 0; rep < 2; rep++) {
        if (rep == 1 && w != 0) break;
        const int pid = tid + 512 * rep;
        const int i = pid & 7, pc = pid >> 3, ehh = pc & 1, H = (pc >> 1) & 1, ctg = pc >> 2;
        const int mloc = 8 * fs + i;
        u32x4 pv[8];
        const unsigned po = (unsigned)((ctg * 2 + (mloc >> 5)) * 2 + H) * 1024 + ((mloc & 31) + 32 * ehh) * 16;
#pragma unroll
        for (int f = 0; f < 8; f++) pv[f] = __builtin_amdgcn_raw_buffer_load_b128(rs, po, WS_PART + (unsigned)(f * 32 + t) * 73728, 16);
        const int d0 = 32 * ctg + 16 * H + 4 * ehh;
        const size_t rowoff = (size_t)(jb + 256 * i) * 576;
        u32x2 xa = *(const u32x2*)(x + rowoff + d0), xb = *(const u32x2*)(x + rowoff + d0 + 8);
        u32x2 ba = *(const u32x2*)(b2 + d0), bb = *(const u32x2*)(b2 + d0 + 8);
        float cr[4], ci[4];
        cr[0] = 1.f; ci[0] = (i & 1) ? -1.f : 1.f;
#pragma unroll
        for (int r = 1; r < 4; r++) {
            float ang = (float)((i * r) & 7) * 0.125f;
            cr[r] = 2.f * __builtin_amdgcn_cosf(ang); ci[r] = -2.f * __builtin_amdgcn_sinf(ang);
        }
        float sv[8];
#pragma unroll
        for (int e = 0; e < 8; e++) {
            const int d = d0 + (e & 3) + 8 * (e >> 2);
            float s = 0.f;
#pragma unroll
            for (int r = 0; r < 4; r++) { f32x2 c = *(LAS f32x2*)(smem + LDS_CB + (r * 576 + d) * 8); s += cr[r] * c[0] + ci[r] * c[1]; }
            sv[e] = rbf(s);
        }
        float o[8];
#pragma unroll
        for (int e = 0; e < 4; e++) {
            float lo = 0.f, hi = 0.f;
#pragma unroll
            for (int f = 0; f < 8; f++) { lo += bf_lo(pv[f][e]); hi += bf_hi(pv[f][e]); }
            o[2 * e] = lo; o[2 * e + 1] = hi;
        }
        float xv[8] = {bf_lo(xa[0]), bf_hi(xa[0]), bf_lo(xa[1]), bf_hi(xa[1]), bf_lo(xb[0]), bf_hi(xb[0]), bf_lo(xb[1]), bf_hi(xb[1])};
        float bv[8] = {bf_lo(ba[0]), bf_hi(ba[0]), bf_lo(ba[1]), bf_hi(ba[1]), bf_lo(bb[0]), bf_hi(bb[0]), bf_lo(bb[1]), bf_hi(bb[1])};
        float res[8];
#pragma unroll
        for (int e = 0; e < 8; e++) {
            float ov = rbf(rbf(o[e]) + bv[e]);
            float gv = rbf(ov * __builtin_amdgcn_rcpf(1.0f + __builtin_amdgcn_exp2f(-1.4426950408889634f * ov)));
            res[e] = xv[e] + rbf(sv[e] * gv);
        }
        u32x2 oa = {pack_bf16(res[0], res[1]), pack_bf16(res[2], res[3])}, ob = {pack_bf16(res[4], res[5]), pack_bf16(res[6], res[7])};
        *(u32x2*)(out + rowoff + d0) = oa;
        *(u32x2*)(out + rowoff + d0 + 8) = ob;
    }
}

extern "C" void kernel_launch(void* const* d_in, const int* in_sizes, int n_in,
                              void* d_out, int out_size, void* d_ws, size_t ws_size,
                              hipStream_t stream) {
    (void)hipFuncSetAttribute((const void*)k_fused, hipFuncAttributeMaxDynamicSharedMemorySize, LDS_TOTAL);
    k_fused<<<256, 512, LDS_TOTAL, stream>>>((const uint16_t*)d_in[0], (const uint16_t*)d_in[1], (const uint16_t*)d_in[2],
                                             (const uint16_t*)d_in[4], (const uint16_t*)d_in[5], (const uint16_t*)d_in[6],
                                             (const uint16_t*)d_in[7], (uint16_t*)d_out, (char*)d_ws
                                             );
}
```

```cpp
#include <hip/hip_runtime.h>
#include <stdint.h>
#include <type_traits>

typedef __attribute__((ext_vector_type(8))) __bf16 bf16x8;
typedef __attribute__((ext_vector_type(2))) __bf16 bf16x2;
typedef __attribute__((ext_vector_type(8))) _Float16 f16x8;
typedef __attribute__((ext_vector_type(2))) _Float16 f16x2;
typedef __attribute__((ext_vector_type(4))) short s16x4;
typedef __attribute__((ext_vector_type(2))) float f32x2;
typedef __attribute__((ext_vector_type(4))) float f32x4;
typedef __attribute__((ext_vector_type(16))) float f32x16;
typedef __attribute__((ext_vector_type(4))) unsigned u32x4;
typedef __attribute__((ext_vector_type(2))) unsigned u32x2;
typedef __attribute__((ext_vector_type(2))) unsigned short u16x2;
typedef unsigned long long u64;
#define LAS __attribute__((address_space(3)))
#define GAS __attribute__((address_space(1)))
#define DI __device__ __forceinline__

constexpr int RING_SLOT = 24576;
constexpr int LDS_Y = 3 * RING_SLOT;
constexpr int LDS_H = 0;
constexpr int LDS_CB = 0;
constexpr int LDS_XB = 3 * RING_SLOT;
constexpr int LDS_TAG = 6 * RING_SLOT;
constexpr int LDS_T256 = LDS_TAG + 16;
constexpr int LDS_WMAX = LDS_T256 + 2048;
constexpr int LDS_BIAS = LDS_WMAX + 256;
constexpr int LDS_XS = LDS_BIAS + 2304;
constexpr int LDS_XA = LDS_BIAS;
constexpr int LDS_TOTAL = LDS_XS + 8 * 1152 + 64;
constexpr unsigned WS_EPOCH = 0;
constexpr unsigned WS_ROWF = 256;
constexpr unsigned WS_COLF = 4096;
constexpr unsigned WS_PARTF = 8192;
constexpr unsigned ZV_PITCH = 580 * 4;
constexpr unsigned WS_ZV = 16384;
constexpr unsigned WS_VP = WS_ZV + 256 * 4 * ZV_PITCH;
constexpr unsigned WS_PART = WS_VP + 8 * 32 * 256 * 80;
constexpr unsigned WS_END = WS_PART + 8u * 2048 * 576 * 2;
constexpr unsigned OOB = 0xF0000000u;

template <int IMM>
DI void glds16(const char* sbase, unsigned voff, unsigned ldsbase) {
    asm volatile("s_add_u32 m0, %2, %3\n\ts_nop 0\n\tglobal_load_lds_dwordx4 %0, %1" ::"v"(voff), "s"(sbase), "s"(ldsbase), "i"(IMM) : "memory", "scc");
}
DI void glds4(const char* sbase, unsigned voff, unsigned ldsaddr) {
    asm volatile("s_mov_b32 m0, %2\n\ts_nop 0\n\tglobal_load_lds_dword %0, %1" ::"v"(voff), "s"(sbase), "s"(ldsaddr) : "memory");
}
#define SB0() __builtin_amdgcn_sched_barrier(0)
#define PIN(x) asm volatile("" : "+v"(x))
DI s16x4 tr_rd(LAS char* p) { return __builtin_amdgcn_ds_read_tr16_b64_v4i16((LAS s16x4*)p); }
DI unsigned pack_bf16(float a, float b) { bf16x2 p; p[0] = (__bf16)a; p[1] = (__bf16)b; return __builtin_bit_cast(unsigned, p); }
DI unsigned pack_f16(float a, float b) { f16x2 p; p[0] = (_Float16)a; p[1] = (_Float16)b; return __builtin_bit_cast(unsigned, p); }
DI float bf_lo(unsigned u) { return __uint_as_float(u << 16); }
DI float bf_hi(unsigned u) { return __uint_as_float(u & 0xffff0000u); }
DI float rbf(float f) { return bf_lo(pack_bf16(f, 0.f)); }
#define STAMP(i)
#define MFMA32(a, b, c) __builtin_amdgcn_mfma_f32_32x32x16_bf16((a), (b), (c), 0, 0, 0)
#define MFMA32H(a, b, c) __builtin_amdgcn_mfma_f32_32x32x16_f16((a), (b), (c), 0, 0, 0)
#define MFMA16(a, b, c) __builtin_amdgcn_mfma_f32_16x16x32_bf16((a), (b), (c), 0, 0, 0)
template <int N> DI void waitv() { asm volatile("s_waitcnt vmcnt(%0)" ::"i"(N) : "memory"); }
#define WAITL0() asm volatile("s_waitcnt lgkmcnt(0)" ::: "memory")
#define BAR() do { __builtin_amdgcn_s_barrier(); SB0(); } while (0)
#define RLX_AGENT __ATOMIC_RELAXED, __HIP_MEMORY_SCOPE_AGENT
template <int V> using IC = std::integral_constant<int, V>;
template <int A, int B, typename F> DI void static_for(F&& f) {
    if constexpr (A < B) { f(IC<A>{}); static_for<A + 1, B>(f); }
}
DI void split2(float a, float b, unsigned& hi, unsigned& lo) {
    hi = pack_bf16(a, b);
    lo = pack_bf16(a - bf_lo(hi), b - bf_hi(hi));
}
DI bf16x8 mk8(unsigned a, unsigned b, unsigned c, unsigned d) { u32x4 v = {a, b, c, d}; return __builtin_bit_cast(bf16x8, v); }
DI f16x8 mk8h(unsigned a, unsigned b, unsigned c, unsigned d) { u32x4 v = {a, b, c, d}; return __builtin_bit_cast(f16x8, v); }
DI int rho(int a, int hh) { return (a & 3) + 8 * (a >> 2) + 4 * hh; }
DI float red8(float v) {
    v += __builtin_bit_cast(float, __builtin_amdgcn_update_dpp(0, __builtin_bit_cast(int, v), 0x128, 0xf, 0xf, false));
    { auto r = __builtin_amdgcn_permlane16_swap(__float_as_uint(v), __float_as_uint(v), false, false); v = __uint_as_float(r[0]) + __uint_as_float(r[1]); }
    { auto r = __builtin_amdgcn_permlane32_swap(__float_as_uint(v), __float_as_uint(v), false, false); v = __uint_as_float(r[0]) + __uint_as_float(r[1]); }
    return v;
}

struct Dft24 { bf16x8 Ph[3], Pl[3], Qh[3], Ql[3]; };
DI void dft24_setup(Dft24& m, int lane) {
    const int col = lane & 31, hh = lane >> 5;
    float c[12], s[12];
#pragma unroll
    for (int a = 0; a < 12; a++) {
        float ang = (float)((rho(a, hh) * col) % 24) * (1.0f / 24.0f);
        c[a] = __builtin_amdgcn_cosf(ang); s[a] = __builtin_amdgcn_sinf(ang);
    }
    unsigned ch[6], cl[6], sh[6], sl[6];
#pragma unroll
    for (int i = 0; i < 6; i++) { split2(c[2 * i], c[2 * i + 1], ch[i], cl[i]); split2(s[2 * i], s[2 * i + 1], sh[i], sl[i]); }
    const unsigned N = 0x80008000u;
    m.Ph[0] = mk8(ch[0], ch[1], ch[2], ch[3]); m.Ph[1] = mk8(ch[4], ch[5], sh[0], sh[1]); m.Ph[2] = mk8(sh[2], sh[3], sh[4], sh[5]);
    m.Pl[0] = mk8(cl[0], cl[1], cl[2], cl[3]); m.Pl[1] = mk8(cl[4], cl[5], sl[0], sl[1]); m.Pl[2] = mk8(sl[2], sl[3], sl[4], sl[5]);
    m.Qh[0] = mk8(sh[0] ^ N, sh[1] ^ N, sh[2] ^ N, sh[3] ^ N); m.Qh[1] = mk8(sh[4] ^ N, sh[5] ^ N, ch[0], ch[1]); m.Qh[2] = mk8(ch[2], ch[3], ch[4], ch[5]);
    m.Ql[0] = mk8(sl[0] ^ N, sl[1] ^ N, sl[2] ^ N, sl[3] ^ N); m.Ql[1] = mk8(sl[4] ^ N, sl[5] ^ N, cl[0], cl[1]); m.Ql[2] = mk8(cl[2], cl[3], cl[4], cl[5]);
}
template <bool ASA>
DI void dft24_stage(const Dft24& m, float (&re)[12], float (&im)[12]) {
    unsigned h[12], l[12];
#pragma unroll
    for (int i = 0; i < 6; i++) { split2(re[2 * i], re[2 * i + 1], h[i], l[i]); split2(im[2 * i], im[2 * i + 1], h[6 + i], l[6 + i]); }
    f32x16 dr = {0, 0, 0, 0, 0, 0, 0, 0, 0, 0, 0, 0, 0, 0, 0, 0}, di = dr;
#pragma unroll
    for (int s = 0; s < 3; s++) {
        bf16x8 xh = mk8(h[4 * s], h[4 * s + 1], h[4 * s + 2], h[4 * s + 3]);
        bf16x8 xl = mk8(l[4 * s], l[4 * s + 1], l[4 * s + 2], l[4 * s + 3]);
        if (ASA) {
            dr = MFMA32(xl, m.Ph[s], dr); di = MFMA32(xl, m.Qh[s], di);
            dr = MFMA32(xh, m.Pl[s], dr); di = MFMA32(xh, m.Ql[s], di);
            dr = MFMA32(xh, m.Ph[s], dr); di = MFMA32(xh, m.Qh[s], di);
        } else {
            dr = MFMA32(m.Ph[s], xl, dr); di = MFMA32(m.Qh[s], xl, di);
            dr = MFMA32(m.Pl[s], xh, dr); di = MFMA32(m.Ql[s], xh, di);
            dr = MFMA32(m.Ph[s], xh, dr); di = MFMA32(m.Qh[s], xh, di);
        }
    }
#pragma unroll
    for (int a = 0; a < 12; a++) { re[a] = dr[a]; im[a] = di[a]; }
}
DI void tw576(float (&re)[12], float (&im)[12], int lane) {
    const int col = lane & 31, hh = lane >> 5;
#pragma unroll
    for (int a = 0; a < 12; a++) {
        float an = (float)(rho(a, hh) * col) * (1.0f / 576.0f);
        float c = __builtin_amdgcn_cosf(an), s = __builtin_amdgcn_sinf(an);
        float r = re[a] * c + im[a] * s;
        float i = im[a] * c - re[a] * s;
        re[a] = r; im[a] = i;
    }
}
#define MFMA16H(a, b, c) __builtin_amdgcn_mfma_f32_16x16x32_f16((a), (b), (c), 0, 0, 0)
DI unsigned rotq(unsigned p) { return __builtin_amdgcn_alignbit(p, p, 16) ^ 0x00008000u; }
struct Dft24h { f16x8 P[3], Q[3]; };
DI void dft24h_setup(Dft24h& m, int lane) {
    const int col = lane & 31, hh = lane >> 5;
    unsigned p[12];
#pragma unroll
    for (int a = 0; a < 12; a++) {
        float an = (float)(rho(a, hh) * col) * (1.0f / 24.0f);
        an = __builtin_amdgcn_fractf(an + 0.01f) - 0.01f;
        p[a] = pack_f16(__builtin_amdgcn_cosf(an), __builtin_amdgcn_sinf(an));
    }
#pragma unroll
    for (int s = 0; s < 3; s++) {
        m.P[s] = mk8h(p[4 * s], p[4 * s + 1], p[4 * s + 2], p[4 * s + 3]);
        m.Q[s] = mk8h(rotq(p[4 * s]), rotq(p[4 * s + 1]), rotq(p[4 * s + 2]), rotq(p[4 * s + 3]));
    }
}
DI void dft24h_stage(const Dft24h& m, float (&re)[12], float (&im)[12]) {
    f32x16 dr = {0, 0, 0, 0, 0, 0, 0, 0, 0, 0, 0, 0, 0, 0, 0, 0}, di = dr;
#pragma unroll
    for (int s = 0; s < 3; s++) {
        f16x8 xh = mk8h(pack_f16(re[4 * s], im[4 * s]), pack_f16(re[4 * s + 1], im[4 * s + 1]), pack_f16(re[4 * s + 2], im[4 * s + 2]), pack_f16(re[4 * s + 3], im[4 * s + 3]));
        dr = MFMA32H(xh, m.P[s], dr); di = MFMA32H(xh, m.Q[s], di);
    }
#pragma unroll
    for (int a = 0; a < 12; a++) { re[a] = dr[a]; im[a] = di[a]; }
}
DI u32x4 mat16(const float (&ang)[4]) {
    u32x4 p;
#pragma unroll
    for (int e = 0; e < 4; e++) p[e] = pack_f16(0.25f * __builtin_amdgcn_cosf(ang[e]), 0.25f * __builtin_amdgcn_sinf(ang[e]));
    return p;
}
DI u32x4 rotq4(u32x4 p) { u32x4 q = {rotq(p[0]), rotq(p[1]), rotq(p[2]), rotq(p[3])}; return q; }
DI f16x8 ash(u32x4 v) { return __builtin_bit_cast(f16x8, v); }
template <int NW>
DI void wait_flags(GAS u64* f, u64 tag, int idx) {
    for (unsigned spins = 0; spins < (1u << 20); spins++) {
        bool ok = true;
#pragma unroll
        for (int k = 0; k < NW; k++) ok &= (__hip_atomic_load(f + idx + 64 * k, RLX_AGENT) == tag);
        if (__all(ok)) break;
        __builtin_amdgcn_s_sleep(2);
    }
    __builtin_amdgcn_fence(__ATOMIC_ACQUIRE, "wavefront");
}

__global__ __launch_bounds__(512, 2) void k_fused(const uint16_t* __restrict__ x, const uint16_t* __restrict__ wr, const uint16_t* __restrict__ wi,
                                                  const uint16_t* __restrict__ w1, const uint16_t* __restrict__ b1, const uint16_t* __restrict__ w2,
                                                  const uint16_t* __restrict__ b2, uint16_t* __restrict__ out, char* __restrict__ ws
                                                  ) {
    extern __shared__ __attribute__((aligned(16))) char smem_g[];
    LAS char* smem = (LAS char*)smem_g;
    const int tid = threadIdx.x, lane = tid & 63;
    const int w = __builtin_amdgcn_readfirstlane(tid >> 6);
    const int xg = blockIdx.x & 7, li = blockIdx.x >> 3;
    const int fs = (xg & 3) * 2 + (li & 1), t = (li >> 1) * 2 + (xg >> 2), tl = li >> 1;
    const int jb = 8 * t + fs;
    const unsigned lds0 = (unsigned)(uintptr_t)smem;
    const int rh = w & 1, ct = w >> 1;
    const __amdgpu_buffer_rsrc_t rs = __builtin_amdgcn_make_buffer_rsrc(ws, 0, WS_END, 0x00020000);
    STAMP(0);
    const int hb = 2 * (tl % 5), r4 = tl & 3;
    int sbk[5];
#pragma unroll
    for (int i = 0; i < 4; i++) { int ii = (i + r4) & 3; sbk[i] = __builtin_amdgcn_readfirstlane(2 * ii + (2 * ii >= hb ? 1 : 0)); }
    sbk[4] = __builtin_amdgcn_readfirstlane(hb);
    const char* w1b = (const char*)w1 + fs * 1152;
    const char* w2b = (const char*)w2 + (size_t)fs * 576 * 1152;
    const unsigned ldsw = lds0 + w * 1024;
    auto mkvo = [&](bool full, unsigned pitch) {
        int ln = tid; PIN(ln); ln &= 63;
        unsigned r = full ? 4 * w + (ln >> 4) : 8 * w + (ln >> 3);
        unsigned c = full ? ((((ln >> 2) & 3) ^ (ln >> 4)) * 64) + (ln & 3) * 16 : ((((ln >> 2) & 1) ^ ((ln >> 4) & 1)) * 64) + (ln & 3) * 16;
        return r * pitch + c;
    };
    unsigned vo;
    auto gl = [&](auto U_, auto J_) {
        constexpr int U = decltype(U_)::value, J = decltype(J_)::value % 3, X = decltype(J_)::value / 3;
        constexpr int ph = U / 27, v = U % 27;
        constexpr int DST = (U % 6) * RING_SLOT + J * 8192 + X * 4096;
        if constexpr (U < 54) {
            constexpr int R = v < 3 ? 192 * v + 64 * J : 96 * ((v - 3) % 6) + 32 * J + 16 * X;
            const int blk = sbk[v < 3 ? 4 : (v - 3) / 6];
            if constexpr (ph == 0) glds16<DST>(w1b + (unsigned)__builtin_amdgcn_readfirstlane(blk * 128 + R * 9216), vo, ldsw);
            else glds16<DST>(w2b + (unsigned)__builtin_amdgcn_readfirstlane(blk * 128 + (64 * sbk[R / 128] + R % 128) * 1152), vo, ldsw);
        }
    };
    auto setvo = [&](auto U_) {
        constexpr int U = decltype(U_)::value;
        if constexpr (U == 0) vo = mkvo(false, 9216);
        if constexpr (U == 3) vo = mkvo(true, 9216);
        if constexpr (U == 27) vo = mkvo(false, 1152);
        if constexpr (U == 30) vo = mkvo(true, 1152);
    };
    auto gl3 = [&](auto U_) { setvo(U_); static_for<0, 3>([&](auto J_) { gl(U_, J_); }); };

    Dft24h m24;
    {
        const int i = lane & 7, c = lane >> 3;
        const uint16_t* xr = x + (size_t)(8 * t + w + 256 * i) * 576 + c * 8;
        u32x4 q[9];
#pragma unroll
        for (int n = 0; n < 9; n++) q[n] = *(const u32x4*)(xr + n * 64);
        u64 ep = 0;
        if (tid == 0) ep = __hip_atomic_load((GAS u64*)(ws + WS_EPOCH), RLX_AGENT);
        u32x4 bb = {0, 0, 0, 0};
        const bool dob = tid >= 384 && tid < 384 + 72;
        if (dob) bb = *(const u32x4*)((const char*)b1 + fs * 1152 + (tid - 384) * 16);
        SB0();
        gl3(IC<0>{});
        SB0();
        f32x2 tw;
        {
            float an = (float)(((tid >> 4) & 15) * (8 * (tid & 15) + fs)) * (1.0f / 2048.0f);
            tw[0] = __builtin_amdgcn_cosf(an); tw[1] = __builtin_amdgcn_sinf(an);
        }
        if (w >= 4) dft24h_setup(m24, lane);
        SB0();
        f32x2 d[36];
        f32x2 s2 = {0.f, 0.f};
#pragma unroll
        for (int n = 0; n < 9; n++)
#pragma unroll
            for (int e = 0; e < 4; e++) { f32x2 v = {bf_lo(q[n][e]), bf_hi(q[n][e])}; d[4 * n + e] = v; s2 += v; }
        const float mu = red8(s2[0] + s2[1]) * (1.0f / 576.0f);
        const f32x2 mu2 = {mu, mu};
        f32x2 q2 = {0.f, 0.f};
#pragma unroll
        for (int e = 0; e < 36; e++) { d[e] -= mu2; q2 = __builtin_elementwise_fma(d[e], d[e], q2); }
        const float var = red8(q2[0] + q2[1]) * (1.0f / 576.0f);
        const float m1 = rbf(mu);
        const float is1 = 1.0f / rbf(__builtin_sqrtf(rbf(rbf(var) + 1e-5f)));
        const float mean2 = (mu - m1) * is1;
        const float var2 = var * is1 * is1;
        const float is2 = 1.0f / rbf(__builtin_sqrtf(rbf(rbf(var2) + 1e-5f)));
        const float ya = is1 * is2, yb = (mean2 - rbf(mean2)) * is2;
        const f32x2 ya2 = {ya, ya}, yb2 = {yb, yb};
        const int mloc = 8 * w + i;
        LAS char* yw = smem + LDS_Y + (mloc >> 5) * 36 * 1024 + (c >> 1) * 1024 + ((mloc & 31) + 32 * (c & 1)) * 16;
#pragma unroll
        for (int n = 0; n < 9; n++) {
            u32x4 o;
#pragma unroll
            for (int e = 0; e < 4; e++) { f32x2 v = __builtin_elementwise_fma(d[4 * n + e], ya2, yb2); o[e] = pack_bf16(v[0], v[1]); }
            *(LAS u32x4*)(yw + n * 4096) = o;
        }
        if (w == fs) {
            const f32x2 xa = {is1, is1}, xb = {mean2, mean2};
            const int pos = (i & 1) ? 4 + (i >> 1) : ((i >> 2) | (i & 2));
            LAS char* xw = smem + LDS_XS + pos * 1152 + c * 16;
#pragma unroll
            for (int n = 0; n < 9; n++) {
                u32x4 o;
#pragma unroll
                for (int e = 0; e < 4; e++) { f32x2 v = __builtin_elementwise_fma(d[4 * n + e], xa, xb); o[e] = pack_bf16(v[0], v[1]); }
                *(LAS u32x4*)(xw + n * 128) = o;
            }
        }
        if (dob) {
            f32x4 lo = {bf_lo(bb[0]), bf_hi(bb[0]), bf_lo(bb[1]), bf_hi(bb[1])}, hi = {bf_lo(bb[2]), bf_hi(bb[2]), bf_lo(bb[3]), bf_hi(bb[3])};
            *(LAS f32x4*)(smem + LDS_BIAS + (tid - 384) * 32) = lo;
            *(LAS f32x4*)(smem + LDS_BIAS + (tid - 384) * 32 + 16) = hi;
        }
        if (tid == 0) *(LAS u64*)(smem + LDS_TAG) = ep;
        if (tid < 256) *(LAS f32x2*)(smem + LDS_T256 + tid * 8) = tw;
        waitv<0>();
        WAITL0();
        gl3(IC<1>{}); gl3(IC<2>{});
    }
    BAR();
    STAMP(1);
    u64 epoch;
    {
        u32x2 e2 = *(LAS u32x2*)(smem + LDS_TAG);
        epoch = ((u64)(unsigned)__builtin_amdgcn_readfirstlane(e2[1]) << 32) | (unsigned)__builtin_amdgcn_readfirstlane(e2[0]);
    }
    const u64 tag = (epoch + 1) * 0x9E3779B97F4A7C15ull + 0x632BE59BD9B4E019ull;

    auto put_flag = [&](bool me, unsigned off, u64 val) {
        u32x2 o = {(unsigned)val, (unsigned)(val >> 32)};
        __builtin_amdgcn_raw_buffer_store_b64(o, rs, me ? 0u : OOB, off, 16);
    };
    LAS char* trc;
    auto settr = [&](auto U_) {
        constexpr int U = decltype(U_)::value, v = U % 27, ph = U / 27;
        if constexpr (v == 0 || v == 3) {
            int ln = tid; PIN(ln); ln &= 63;
            int G = ln >> 4, q = (ln >> 2) & 3, p = ln & 3;
            int row = (ph == 0 ? 8 : 4) * (G >> 1) + q;
            if constexpr (v == 3) trc = smem + row * 256 + ((ct ^ q) * 64) + (G & 1) * 32 + p * 8 + ((U % 6) / 2) * 2 * RING_SLOT;
            else trc = smem + row * 128 + (((ct & 1) ^ ((q >> 1) & 1)) * 64) + (G & 1) * 32 + p * 8 + ((U % 6) / 2) * 2 * RING_SLOT;
            PIN(trc);
        } else if constexpr (U % 2 == 0) {
            trc += (U % 6 == 0) ? -4 * RING_SLOT : 2 * RING_SLOT;
            PIN(trc);
        }
    };
    bf16x8 af[36];
    {
        LAS char* ab = smem + LDS_Y + rh * 36 * 1024 + lane * 16; PIN(ab);
#pragma unroll
        for (int j = 0; j < 36; j++) af[j] = *(LAS bf16x8*)(ab + j * 1024);
    }
    LAS char* biasp = smem + LDS_BIAS + (32 * ct + 4 * (lane >> 5)) * 4;
    const unsigned pbase = WS_PART + (unsigned)(((fs * 32 + t) * 36 + 2 * ct + rh) * 2048);
    unsigned plane = 0;

    f32x16 acc[2];
    s16x4 bl[6], bh[6];
    float gt[4];
    unsigned hq[5][8];
    auto ldbias = [&](auto P_, auto Q_, int blk) {
        constexpr int P = decltype(P_)::value, Q = decltype(Q_)::value;
        f32x4 b = *(LAS f32x4*)(biasp + blk * 256 + Q * 32);
        acc[P][4 * Q] = b[0]; acc[P][4 * Q + 1] = b[1]; acc[P][4 * Q + 2] = b[2]; acc[P][4 * Q + 3] = b[3];
    };
    auto gop = [&](auto C_, auto K_) {
        constexpr int C = decltype(C_)::value, K = decltype(K_)::value, P = C == 4 ? 1 : (C & 1);
        constexpr int NB = C == 4 ? 1 : C + 2;
        const float c0 = -2.0f * 0.7978845608028654f * 1.4426950408889634f;
        const float c1 = c0 * 0.044715f;
        if constexpr (K < 124) {
            constexpr int Q = K / 31, k = K % 31;
            if constexpr (k < 28) {
                constexpr int st = k / 4, e = k % 4;
                float xx = acc[P][4 * Q + e];
                if constexpr (st == 0) gt[e] = xx * xx;
                else if constexpr (st == 1) gt[e] = __builtin_fmaf(gt[e], c1, c0);
                else if constexpr (st == 2) gt[e] = xx * gt[e];
                else if constexpr (st == 3) gt[e] = __builtin_amdgcn_exp2f(gt[e]);
                else if constexpr (st == 4) gt[e] = 1.0f + gt[e];
                else if constexpr (st == 5) gt[e] = __builtin_amdgcn_rcpf(gt[e]);
                else gt[e] = xx * gt[e];
                PIN(gt[e]);
            } else if constexpr (k == 28) { hq[C][2 * Q] = pack_bf16(gt[0], gt[1]); PIN(hq[C][2 * Q]); }
            else if constexpr (k == 29) { hq[C][2 * Q + 1] = pack_bf16(gt[2], gt[3]); PIN(hq[C][2 * Q + 1]); }
            else { if constexpr (NB <= 3) ldbias(IC<P>{}, IC<Q>{}, sbk[NB]); }
        }
    };
    auto grange = [&](auto C_, auto A_, auto B_) {
        static_for<decltype(A_)::value, decltype(B_)::value>([&](auto K_) { gop(C_, K_); });
    };
    auto pstore = [&](auto P_, auto H_, int blk) {
        constexpr int P = decltype(P_)::value, H = decltype(H_)::value;
        u32x4 o;
        o[0] = pack_bf16(acc[P][8 * H + 0], acc[P][8 * H + 1]); o[1] = pack_bf16(acc[P][8 * H + 2], acc[P][8 * H + 3]);
        o[2] = pack_bf16(acc[P][8 * H + 4], acc[P][8 * H + 5]); o[3] = pack_bf16(acc[P][8 * H + 6], acc[P][8 * H + 7]);
        __builtin_amdgcn_raw_buffer_store_b128(o, rs, plane, pbase + blk * 8192 + H * 1024, 16);
    };
    const f32x16 zero16 = {0, 0, 0, 0, 0, 0, 0, 0, 0, 0, 0, 0, 0, 0, 0, 0};
    auto bfrag = [&](auto J_) {
        constexpr int J = decltype(J_)::value;
        return __builtin_bit_cast(bf16x8, __builtin_shufflevector(bl[J], bh[J], 0, 1, 2, 3, 4, 5, 6, 7));
    };
    auto fslot = [&](auto U_, auto LO_, auto GI_, auto&& fill) {
        constexpr int U = decltype(U_)::value, ph = U / 27, v = U % 27 - 3, c = v / 6, s = v % 6;
        constexpr bool LO = decltype(LO_)::value;
        constexpr int PB = c & 1;
        constexpr int PA = s == 0 ? PB ^ 1 : PB;
        constexpr int sp = (s + 5) % 6;
        constexpr int R2 = ph == 0 ? 1024 : 2048;
        settr(U_);
        LAS char* sb = trc + (U % 2) * RING_SLOT;
        setvo(GI_);
        static_for<0, 6>([&](auto G_) {
            constexpr int G = decltype(G_)::value;
            if constexpr (G < 3) {
                if constexpr (LO) acc[PA] = MFMA32(bfrag(IC<G + 3>{}), af[6 * sp + G + 3], acc[PA]);
            } else {
                acc[PB] = MFMA32(bfrag(IC<G - 3>{}), af[6 * s + G - 3], (ph == 1 && s == 0 && G == 3) ? zero16 : acc[PB]);
            }
            SB0();
            bl[G] = tr_rd(sb + G * 4096); bh[G] = tr_rd(sb + G * 4096 + R2);
            if constexpr (G < 3) gl(GI_, G_);
            fill(G_);
            SB0();
        });
    };
    auto hslot = [&](auto U_, auto&& issue) {
        constexpr int U = decltype(U_)::value, ph = U / 27, s = U % 27;
        constexpr int R2 = ph == 0 ? 512 : 1024;
        settr(U_);
        LAS char* sb = trc + (U % 2) * RING_SLOT;
        static_for<0, 12>([&](auto G_) {
            constexpr int G = decltype(G_)::value;
            if constexpr (G < 3) {
                if constexpr (s > 0) acc[1] = MFMA32(bfrag(IC<G + 3>{}), af[12 * s - 3 + G], acc[1]);
            } else {
                acc[1] = MFMA32(bfrag(IC<(G - 3) % 6>{}), af[12 * s + G - 3], (ph == 1 && s == 0 && G == 3) ? zero16 : acc[1]);
            }
            SB0();
            bl[G % 6] = tr_rd(sb + G * 2048); bh[G % 6] = tr_rd(sb + G * 2048 + R2);
            issue(G_);
            SB0();
        });
    };
    auto iss2 = [&](auto GI_, auto GI2_) {
        return [&, GI_, GI2_](auto G_) {
            constexpr int G = decltype(G_)::value;
            if constexpr (G == 0) setvo(GI_);
            if constexpr (G < 6 && G % 2 == 0) gl(GI_, IC<G / 2>{});
            if constexpr (G >= 6 && G % 2 == 0) gl(GI2_, IC<G / 2 - 3>{});
        };
    };
    using T = std::true_type; using F = std::false_type;
    auto nofill = [&](auto) {};

    if (w < 4) {
        static_for<0, 4>([&](auto Q_) { ldbias(IC<1>{}, Q_, sbk[4]); ldbias(IC<0>{}, Q_, sbk[0]); });
        hslot(IC<0>{}, nofill); WAITL0(); waitv<3>(); BAR();
        hslot(IC<1>{}, iss2(IC<3>{}, IC<4>{})); waitv<6>(); BAR();
        hslot(IC<2>{}, iss2(IC<5>{}, IC<99>{})); waitv<6>(); BAR();
        STAMP(9);
        static_for<3, 9>([&](auto U_) {
            constexpr int U = decltype(U_)::value, s = U - 3;
            fslot(U_, T{}, IC<U + 3>{}, [&](auto G_) {
                constexpr int gi = 6 * s + decltype(G_)::value - 4;
                if constexpr (gi >= 0) grange(IC<4>{}, IC<(gi * 124) / 32>{}, IC<((gi + 1) * 124) / 32>{});
            });
            waitv<6>(); BAR();
        });
    } else {
        const int v = w - 4;
        const unsigned PP = 0x3F803F80u, PM = 0xBF803F80u  , MP = 0x3F80BF80u  , MM = 0xBF80BF80u;
        unsigned A0, A1, A2, A3, B0, B1, B2, B3; float cc;
        const float c8 = 0.70710678118654752f;
        if (v == 0) { A0 = PP; A1 = PP; A2 = PP; A3 = PP; B0 = PP; B1 = PP; B2 = MM; B3 = MM; cc = 1.f; }
        else if (v == 1) { A0 = PM; A1 = 0; A2 = PM; A3 = MP; B0 = 0; B1 = MP; B2 = MM; B3 = PP; cc = c8; }
        else if (v == 2) { A0 = PP; A1 = MM; A2 = 0; A3 = 0; B0 = 0; B1 = 0; B2 = MP; B3 = MP; cc = 1.f; }
        else { A0 = PM; A1 = 0; A2 = MP; A3 = PM; B0 = 0; B1 = PM; B2 = MM; B3 = PP; cc = c8; }
        float re[12], im[12];
        auto dot = [](unsigned a, unsigned b, float c) { return __builtin_amdgcn_fdot2_f32_bf16(__builtin_bit_cast(bf16x2, a), __builtin_bit_cast(bf16x2, b), c, false); };
        const int G = lane >> 4, q = (lane >> 2) & 3, p = lane & 3;
        LAS char* xb = smem + LDS_XS + q * 1152 + (96 * (G >> 1) + 16 * (G & 1) + 4 * p) * 2;
        auto rad8 = [&](int a) {
            u32x2 ev = __builtin_bit_cast(u32x2, tr_rd(xb + rho(a, 0) * 48));
            u32x2 od = __builtin_bit_cast(u32x2, tr_rd(xb + rho(a, 0) * 48 + 4 * 1152));
            re[a] = __builtin_fmaf(dot(od[0], A2, dot(od[1], A3, 0.f)), cc, dot(ev[0], A0, dot(ev[1], A1, 0.f)));
            im[a] = __builtin_fmaf(dot(od[0], B2, dot(od[1], B3, 0.f)), cc, dot(ev[0], B0, dot(ev[1], B1, 0.f)));
            PIN(re[a]); PIN(im[a]);
        };
#pragma unroll
        for (int a = 0; a < 6; a++) rad8(a);
        WAITL0(); waitv<3>(); SB0(); BAR();
        gl3(IC<3>{}); gl3(IC<4>{});
#pragma unroll
        for (int a = 6; a < 12; a++) rad8(a);
        dft24h_stage(m24, re, im);
        waitv<6>(); SB0(); BAR();
        gl3(IC<5>{});
        tw576(re, im, lane);
        dft24h_stage(m24, re, im);
        {
            const int hh = lane >> 5;
            const unsigned zo = (lane & 31) < 24 ? (unsigned)(((lane & 31) * 24 + 4 * hh) * 4) : OOB;
            const unsigned zb = WS_ZV + (unsigned)(jb * 4 + v) * ZV_PITCH;
            unsigned z0 = 0;
#pragma unroll
            for (int qd = 0; qd < 3; qd++) {
                u32x4 o;
#pragma unroll
                for (int e = 0; e < 4; e++) o[e] = pack_f16(re[4 * qd + e] * (1.0f / 64.0f), im[4 * qd + e] * (1.0f / 64.0f));
                if (qd == 0) z0 = o[0];
                __builtin_amdgcn_raw_buffer_store_b128(o, rs, zo, zb + qd * 32, 16);
            }
            __builtin_amdgcn_raw_buffer_store_b32(z0, rs, lane == 0 ? 0u : OOB, zb + 576 * 4, 16);
        }
        static_for<0, 4>([&](auto Q_) { ldbias(IC<0>{}, Q_, sbk[0]); ldbias(IC<1>{}, Q_, sbk[1]); });
        waitv<10>(); BAR();
        static_for<3, 9>([&](auto U_) {
            constexpr int U = decltype(U_)::value;
            if constexpr (U == 3) fslot(U_, F{}, IC<U + 3>{}, nofill); else fslot(U_, T{}, IC<U + 3>{}, nofill);
            if constexpr (U <= 4) waitv<10>(); else waitv<6>();
            BAR();
            if constexpr (U == 5) __builtin_amdgcn_raw_buffer_store_b32((unsigned)(tag >> 32), rs, tid == 256 ? 0u : OOB, WS_ROWF + jb * 4, 16);
        });
    }
    STAMP(10);
    u32x4 rf;
    static_for<9, 27>([&](auto U_) {
        constexpr int U = decltype(U_)::value, c = (U - 3) / 6, s = (U - 3) % 6;
        fslot(U_, T{}, IC<U + 3>{}, [&](auto G_) {
            constexpr int gi = 6 * s + decltype(G_)::value - 4;
            if constexpr (gi >= 0) grange(IC<c - 1>{}, IC<(gi * 124) / 32>{}, IC<((gi + 1) * 124) / 32>{});
            if constexpr (U == 24 && decltype(G_)::value == 4) {
                int ln = tid; PIN(ln); ln &= 63;
                rf = __builtin_amdgcn_raw_buffer_load_b128(rs, ln * 16, WS_ROWF, 16);
            }
        });
        if constexpr (U < 24) { waitv<6>(); BAR(); }
        else if constexpr (U < 26) { waitv<7>(); BAR(); }
    });
    static_for<0, 3>([&](auto G_) { constexpr int G = decltype(G_)::value; acc[1] = MFMA32(bfrag(IC<G + 3>{}), af[33 + G], acc[1]); });
    STAMP(2);
    WAITL0();
    waitv<0>();
    BAR();
    u32x4 za[4], zb[4];
    unsigned short wa[4][4], wb[4][4];
    if (w >= 4) {
        int ln = tid; PIN(ln); ln &= 63;
        const int r = fs, col = ln & 15, g = ln >> 4, wq = w - 4;
        const unsigned tg = (unsigned)(tag >> 32);
        for (unsigned spins = 0; spins < (1u << 20); spins++) {
            if (__all(rf[0] == tg && rf[1] == tg && rf[2] == tg && rf[3] == tg)) break;
            __builtin_amdgcn_s_sleep(2);
            rf = __builtin_amdgcn_raw_buffer_load_b128(rs, ln * 16, WS_ROWF, 16);
        }
        __builtin_amdgcn_fence(__ATOMIC_ACQUIRE, "wavefront");
        put_flag(jb == 0 && tid == 256, WS_EPOCH, epoch + 1);
        const int vA = r <= 3 ? r : 0, vB = r >= 5 ? 8 - r : 0;
        const bool useA = r <= 4 && col < 10, useB = (r == 0 || r >= 4) && col < 10;
        const unsigned lo = (unsigned)(64 * g) * 4 * ZV_PITCH;
        const unsigned loA = useA ? lo + col * 4 : OOB, loB = useB ? lo + (15 - col) * 4 : OOB;
        const unsigned sA = WS_ZV + vA * ZV_PITCH + 9 * t * 4 + wq * 16 * ZV_PITCH, sB = WS_ZV + vB * ZV_PITCH + (561 - 9 * t) * 4 + wq * 16 * ZV_PITCH;
#pragma unroll
        for (int tl_ = 0; tl_ < 4; tl_++)
#pragma unroll
            for (int e = 0; e < 4; e++) {
                za[tl_][e] = __builtin_amdgcn_raw_buffer_load_b32(rs, loA, sA + (16 * e + tl_) * 4 * ZV_PITCH, 16);
                zb[tl_][e] = __builtin_amdgcn_raw_buffer_load_b32(rs, loB, sB + (16 * e + tl_) * 4 * ZV_PITCH, 16);
            }
        const unsigned wo = (unsigned)((8 * (4 * wq + 64 * g) + r) * 289 + 9 * t + (col < 10 ? col : 9));
#pragma unroll
        for (int tl_ = 0; tl_ < 4; tl_++)
#pragma unroll
            for (int e = 0; e < 4; e++) { wa[tl_][e] = wr[wo + (8 * tl_ + 128 * e) * 289]; wb[tl_][e] = wi[wo + (8 * tl_ + 128 * e) * 289]; }
        SB0();
    }
    LAS char* ab;
    {
        int ln = tid; PIN(ln); ln &= 63;
        plane = ln * 16;
        ab = smem + LDS_H + rh * 36 * 1024 + ln * 16;
        LAS char* hw = ab + 2 * ct * 1024;
        auto hst = [&](auto C_) {
            constexpr int C = decltype(C_)::value;
            u32x4 o0 = {hq[C][0], hq[C][1], hq[C][2], hq[C][3]}, o1 = {hq[C][4], hq[C][5], hq[C][6], hq[C][7]};
            *(LAS u32x4*)(hw + C * 8192) = o0; *(LAS u32x4*)(hw + C * 8192 + 1024) = o1;
        };
        hst(IC<0>{}); hst(IC<1>{}); hst(IC<2>{});
        if (w < 4) hst(IC<4>{});
        grange(IC<3>{}, IC<0>{}, IC<124>{});
        hst(IC<3>{});
        WAITL0();
        BAR();
    }
    STAMP(3);
    if (w < 4) {
#pragma unroll
        for (int j = 0; j < 36; j++) af[j] = *(LAS bf16x8*)(ab + j * 1024);
        hslot(IC<27>{}, nofill); WAITL0(); BAR();
        hslot(IC<28>{}, [&](auto G_) {
            constexpr int G = decltype(G_)::value;
            if constexpr (G == 0) setvo(IC<30>{});
            gl(IC<30 + G / 6>{}, IC<G % 6>{});
        }); BAR();
        hslot(IC<29>{}, iss2(IC<32>{}, IC<99>{})); waitv<9>(); BAR();
        STAMP(11);
        fslot(IC<30>{}, T{}, IC<33>{}, [&](auto G_) { if constexpr (decltype(G_)::value == 5) pstore(IC<1>{}, IC<0>{}, sbk[4]); }); waitv<7>(); BAR();
        fslot(IC<31>{}, T{}, IC<34>{}, [&](auto G_) { if constexpr (decltype(G_)::value == 5) pstore(IC<1>{}, IC<1>{}, sbk[4]); }); waitv<8>(); BAR();
        fslot(IC<32>{}, T{}, IC<35>{}, nofill); waitv<8>(); BAR();
        fslot(IC<33>{}, T{}, IC<36>{}, nofill); waitv<7>(); BAR();
    } else {
        int ln = tid; PIN(ln); ln &= 63;
        const int r = fs, col = ln & 15, g = ln >> 4, wq = w - 4;
        const f32x4 z4 = {0.f, 0.f, 0.f, 0.f};
        f32x4 dr[4], di[4];
        unsigned wp[4][4];
        LAS char* xw = smem + ((4 * g * 4 + wq) * 10 + (col < 10 ? col : 9)) * 16;
        LAS char* xr = smem + ((4 * wq * 4 + g) * 10 + (col < 10 ? col : 9)) * 16;
        {
            float an[4];
#pragma unroll
            for (int e = 0; e < 4; e++) an[e] = (float)(((4 * g + e) * (8 * col + r)) & 127) * (1.0f / 128.0f) + (r == 4 ? 0.25f : 0.f);
            const u32x4 P1 = mat16(an), Q1 = rotq4(P1);
            const unsigned fl = r == 4 ? 0x00008000u : 0x80000000u;
            const u32x4 P2 = P1 ^ fl, Q2 = Q1 ^ fl;
#pragma unroll
            for (int tl_ = 0; tl_ < 4; tl_++) {
                dr[tl_] = MFMA16H(ash(P1), ash(za[tl_]), z4); di[tl_] = MFMA16H(ash(Q1), ash(za[tl_]), z4);
                dr[tl_] = MFMA16H(ash(P2), ash(zb[tl_]), dr[tl_]); di[tl_] = MFMA16H(ash(Q2), ash(zb[tl_]), di[tl_]);
            }
            u32x4 o[4];
#pragma unroll
            for (int tl_ = 0; tl_ < 4; tl_++) {
                const f32x4 t0 = *(LAS f32x4*)(smem + LDS_T256 + ((4 * wq + tl_) * 16 + 4 * g) * 8), t1 = *(LAS f32x4*)(smem + LDS_T256 + ((4 * wq + tl_) * 16 + 4 * g) * 8 + 16);
#pragma unroll
                for (int e = 0; e < 4; e++) {
                    const float c = e < 2 ? t0[2 * e] : t1[2 * e - 4], sn = e < 2 ? t0[2 * e + 1] : t1[2 * e - 3];
                    o[e][tl_] = pack_f16(dr[tl_][e] * c + di[tl_][e] * sn, di[tl_][e] * c - dr[tl_][e] * sn);
                }
            }
            if (col < 10) {
#pragma unroll
                for (int e = 0; e < 4; e++) *(LAS u32x4*)(xw + LDS_XA + e * 640) = o[e];
            }
            u16x2 mx = {0, 0};
#pragma unroll
            for (int tl_ = 0; tl_ < 4; tl_++)
#pragma unroll
                for (int e = 0; e < 4; e++) {
                    wp[tl_][e] = (unsigned)wa[tl_][e] | ((unsigned)wb[tl_][e] << 16);
                    mx = __builtin_elementwise_max(mx, __builtin_bit_cast(u16x2, wp[tl_][e] & 0x7fff7fffu));
                    PIN(wp[tl_][e]);
                }
            unsigned m1 = mx[0] > mx[1] ? mx[0] : mx[1];
            { auto q = __builtin_amdgcn_permlane16_swap(m1, m1, false, false); m1 = q[0] > q[1] ? q[0] : q[1]; }
            { auto q = __builtin_amdgcn_permlane32_swap(m1, m1, false, false); m1 = q[0] > q[1] ? q[0] : q[1]; }
            *(LAS unsigned*)(smem + LDS_WMAX + col * 16 + wq * 4) = m1;
            SB0();
#pragma unroll
            for (int j = 0; j < 36; j++) af[j] = *(LAS bf16x8*)(ab + j * 1024);
            WAITL0(); SB0(); BAR();
        }
        float inv;
        {
            float an[4];
#pragma unroll
            for (int e = 0; e < 4; e++) an[e] = (float)(((4 * g + e) * col) & 15) * (1.0f / 16.0f);
            const u32x4 P3 = mat16(an), Q3 = rotq4(P3);
            const u32x4 m4 = *(LAS u32x4*)(smem + LDS_WMAX + col * 16);
            unsigned ex = max(max(m4[0], m4[1]), max(m4[2], m4[3])) >> 7;
            ex = min(max(ex, 1u), 252u);
            const float wsc = __uint_as_float((253u - ex) << 23);
            inv = __uint_as_float((ex + 1u) << 23);
#pragma unroll
            for (int tl_ = 0; tl_ < 4; tl_++) {
                const u32x4 xa = *(LAS u32x4*)(xr + LDS_XA + tl_ * 640);
                dr[tl_] = MFMA16H(ash(P3), ash(xa), z4); di[tl_] = MFMA16H(ash(Q3), ash(xa), z4);
            }
#pragma unroll
            for (int tl_ = 0; tl_ < 4; tl_++) {
                u32x4 y;
#pragma unroll
                for (int e = 0; e < 4; e++) {
                    const float wre = bf_lo(wp[tl_][e]) * wsc, wim = bf_hi(wp[tl_][e]) * wsc;
                    y[e] = pack_f16(dr[tl_][e] * wre - di[tl_][e] * wim, -(dr[tl_][e] * wim + di[tl_][e] * wre));
                }
                dr[tl_] = MFMA16H(ash(P3), ash(y), z4); di[tl_] = MFMA16H(ash(Q3), ash(y), z4);
            }
#pragma unroll
            for (int e = 0; e < 4; e++) {
                const f32x4 t0 = *(LAS f32x4*)(smem + LDS_T256 + ((4 * g + e) * 16 + 4 * wq) * 8), t1 = *(LAS f32x4*)(smem + LDS_T256 + ((4 * g + e) * 16 + 4 * wq) * 8 + 16);
                u32x4 o;
#pragma unroll
                for (int tl_ = 0; tl_ < 4; tl_++) {
                    const float c = tl_ < 2 ? t0[2 * tl_] : t1[2 * tl_ - 4], sn = tl_ < 2 ? t0[2 * tl_ + 1] : t1[2 * tl_ - 3];
                    o[tl_] = pack_f16(dr[tl_][e] * c + di[tl_][e] * sn, di[tl_][e] * c - dr[tl_][e] * sn);
                }
                if (col < 10) *(LAS u32x4*)(xw + LDS_XB + e * 640) = o;
            }
            WAITL0(); SB0(); BAR();
        }
        {
            setvo(IC<30>{});
            gl3(IC<32>{});
            float an[4];
#pragma unroll
            for (int e = 0; e < 4; e++) an[e] = (float)((col * (8 * (4 * g + e) + r)) & 127) * (1.0f / 128.0f);
            const u32x4 P4 = mat16(an), Q4 = rotq4(P4);
            const float fsc = (64.0f * 256.0f / (2048.0f * 576.0f)) * ((r & 3) == 0 ? 0.5f : 1.0f);
            const unsigned vo_ = col < 10 ? (unsigned)(64 * g * 80 + col * 8) : OOB;
            const unsigned vb = WS_VP + (unsigned)(r * 32 + t) * (256 * 80) + wq * 4 * 80;
#pragma unroll
            for (int tl_ = 0; tl_ < 4; tl_++) {
                const u32x4 xa = *(LAS u32x4*)(xr + LDS_XB + tl_ * 640);
                dr[tl_] = MFMA16H(ash(P4), ash(xa), z4); di[tl_] = MFMA16H(ash(Q4), ash(xa), z4);
            }
#pragma unroll
            for (int tl_ = 0; tl_ < 4; tl_++)
#pragma unroll
                for (int e = 0; e < 4; e++) {
                    u32x2 o = {__float_as_uint(dr[tl_][e] * fsc * inv), __float_as_uint(di[tl_][e] * fsc * inv)};
                    __builtin_amdgcn_raw_buffer_store_b64(o, rs, vo_, vb + (16 * e + tl_) * 80, 16);
                }
            SB0(); BAR();
        }
        fslot(IC<30>{}, F{}, IC<33>{}, nofill); BAR();
        fslot(IC<31>{}, T{}, IC<34>{}, nofill); waitv<22>(); BAR();
        fslot(IC<32>{}, T{}, IC<35>{}, nofill); waitv<6>(); BAR();
        put_flag(tid == 256, WS_COLF + (fs * 32 + t) * 8, tag);
        fslot(IC<33>{}, T{}, IC<36>{}, nofill); waitv<6>(); BAR();
    }
    STAMP(12);
    static_for<34, 54>([&](auto U_) {
        constexpr int U = decltype(U_)::value, v = U - 30, c = v / 6, s = v % 6;
        auto fill = [&](auto G_) {
            constexpr int G = decltype(G_)::value;
            if constexpr (c >= 1 && s == 0 && G == 5) pstore(IC<(c + 1) & 1>{}, IC<0>{}, sbk[c >= 1 ? c - 1 : 0]);
            if constexpr (c >= 1 && s == 1 && G == 5) pstore(IC<(c + 1) & 1>{}, IC<1>{}, sbk[c >= 1 ? c - 1 : 0]);
        };
        constexpr int ns = (c >= 1) ? ((s == 0 ? 1 : 0) + (s == 1 ? 2 : 0) + (s == 2 ? 2 : 0) + (s == 3 ? 1 : 0)) : 0;
        fslot(U_, T{}, IC<U + 3>{}, fill);
        if constexpr (U <= 50) { waitv<6 + ns>(); BAR(); }
        else if constexpr (U == 51) { waitv<3 + ns>(); BAR(); }
        else if constexpr (U == 52) { waitv<0>(); BAR(); }
    });
    static_for<0, 3>([&](auto G_) { constexpr int G = decltype(G_)::value; acc[1] = MFMA32(bfrag(IC<G + 3>{}), af[33 + G], acc[1]); });
    pstore(IC<1>{}, IC<0>{}, sbk[3]);
    pstore(IC<1>{}, IC<1>{}, sbk[3]);
    STAMP(4);
    WAITL0();
    waitv<0>();
    BAR();
    STAMP(5);
    put_flag(tid == 0, WS_PARTF + (t * 16 + fs) * 8, tag);
    if (w >= 4) {
        const int v = w - 4, hh = lane >> 5;
        int n2 = lane & 31; if (n2 > 23) n2 = 23;
        const int rB = v == 0 ? 4 : 8 - v;
        wait_flags<1>((GAS u64*)(ws + WS_COLF), tag, (lane < 32 ? v : rB) * 32 + (lane & 31));
        float re[12], im[12];
        {
            u32x2 A[12], B[12];
#pragma unroll
            for (int a = 0; a < 12; a++) {
                const int k = 24 * rho(a, hh) + n2;
                const bool mir = k > 288; const int kk = mir ? 576 - k : k;
                int gq = (kk * 57) >> 9; if (gq > 31) gq = 31;
                const unsigned off = (unsigned)gq * (256 * 80) + (unsigned)(kk - 9 * gq) * 8;
                const unsigned rsel = (v != 0 && mir) ? rB : v;
                A[a] = __builtin_amdgcn_raw_buffer_load_b64(rs, off + rsel * (32 * 256 * 80), WS_VP + jb * 80, 16);
                const bool needB = v == 0 || k == 0 || k == 288;
                B[a] = __builtin_amdgcn_raw_buffer_load_b64(rs, needB ? off + rB * (32 * 256 * 80) : OOB, WS_VP + jb * 80, 16);
            }
#pragma unroll
            for (int a = 0; a < 12; a++) {
                const int k = 24 * rho(a, hh) + n2;
                const bool mir = k > 288, spec = (k == 0 || k == 288);
                float ax = __uint_as_float(A[a][0]), ay = __uint_as_float(A[a][1]), bx = __uint_as_float(B[a][0]), by = __uint_as_float(B[a][1]);
                if (mir) { ay = -ay; by = -by; }
                if (v == 0) {
                    if (spec) { ay = 0.f; by = 0.f; }
                    re[a] = ax + by; im[a] = ay - bx;
                } else {
                    re[a] = spec ? 0.5f * (ax + bx) : ax; im[a] = spec ? 0.5f * (ay - by) : ay;
                }
            }
        }
        Dft24 m; dft24_setup(m, lane);
        dft24_stage<true>(m, re, im);
        tw576(re, im, lane);
        dft24_stage<false>(m, re, im);
        if ((lane & 31) < 24) {
#pragma unroll
            for (int a = 0; a < 12; a++) {
                f32x2 o = {re[a], -im[a]};
                *(LAS f32x2*)(smem + LDS_CB + (v * 576 + (lane & 31) + 24 * rho(a, hh)) * 8) = o;
            }
        }
        WAITL0();
    } else if (w == 0) {
        wait_flags<1>((GAS u64*)(ws + WS_PARTF) + t * 16, tag, lane & 7);
    }
    BAR();
    STAMP(6);
#pragma unroll
    for (int rep = 0; rep < 2; rep++) {
        if (rep == 1 && w != 0) break;
        const int pid = tid + 512 * rep;
        const int i = pid & 7, pc = pid >> 3, ehh = pc & 1, H = (pc >> 1) & 1, ctg = pc >> 2;
        const int mloc = 8 * fs + i;
        u32x4 pv[8];
        const unsigned po = (unsigned)((ctg * 2 + (mloc >> 5)) * 2 + H) * 1024 + ((mloc & 31) + 32 * ehh) * 16;
#pragma unroll
        for (int f = 0; f < 8; f++) pv[f] = __builtin_amdgcn_raw_buffer_load_b128(rs, po, WS_PART + (unsigned)(f * 32 + t) * 73728, 16);
        const int d0 = 32 * ctg + 16 * H + 4 * ehh;
        const size_t rowoff = (size_t)(jb + 256 * i) * 576;
        u32x2 xa = *(const u32x2*)(x + rowoff + d0), xb = *(const u32x2*)(x + rowoff + d0 + 8);
        u32x2 ba = *(const u32x2*)(b2 + d0), bb = *(const u32x2*)(b2 + d0 + 8);
        float cr[4], ci[4];
        cr[0] = 1.f; ci[0] = (i & 1) ? -1.f : 1.f;
#pragma unroll
        for (int r = 1; r < 4; r++) {
            float ang = (float)((i * r) & 7) * 0.125f;
            cr[r] = 2.f * __builtin_amdgcn_cosf(ang); ci[r] = -2.f * __builtin_amdgcn_sinf(ang);
        }
        float sv[8];
#pragma unroll
        for (int e = 0; e < 8; e++) {
            const int d = d0 + (e & 3) + 8 * (e >> 2);
            float s = 0.f;
#pragma unroll
            for (int r = 0; r < 4; r++) { f32x2 c = *(LAS f32x2*)(smem + LDS_CB + (r * 576 + d) * 8); s += cr[r] * c[0] + ci[r] * c[1]; }
            sv[e] = rbf(s);
        }
        float o[8];
#pragma unroll
        for (int e = 0; e < 4; e++) {
            float lo = 0.f, hi = 0.f;
#pragma unroll
            for (int f = 0; f < 8; f++) { lo += bf_lo(pv[f][e]); hi += bf_hi(pv[f][e]); }
            o[2 * e] = lo; o[2 * e + 1] = hi;
        }
        float xv[8] = {bf_lo(xa[0]), bf_hi(xa[0]), bf_lo(xa[1]), bf_hi(xa[1]), bf_lo(xb[0]), bf_hi(xb[0]), bf_lo(xb[1]), bf_hi(xb[1])};
        float bv[8] = {bf_lo(ba[0]), bf_hi(ba[0]), bf_lo(ba[1]), bf_hi(ba[1]), bf_lo(bb[0]), bf_hi(bb[0]), bf_lo(bb[1]), bf_hi(bb[1])};
        float res[8];
#pragma unroll
        for (int e = 0; e < 8; e++) {
            float ov = rbf(rbf(o[e]) + bv[e]);
            float gv = rbf(ov * __builtin_amdgcn_rcpf(1.0f + __builtin_amdgcn_exp2f(-1.4426950408889634f * ov)));
            res[e] = xv[e] + rbf(sv[e] * gv);
        }
        u32x2 oa = {pack_bf16(res[0], res[1]), pack_bf16(res[2], res[3])}, ob = {pack_bf16(res[4], res[5]), pack_bf16(res[6], res[7])};
        *(u32x2*)(out + rowoff + d0) = oa;
        *(u32x2*)(out + rowoff + d0 + 8) = ob;
    }
}

extern "C" void kernel_launch(void* const* d_in, const int* in_sizes, int n_in,
                              void* d_out, int out_size, void* d_ws, size_t ws_size,
                              hipStream_t stream) {
    (void)hipFuncSetAttribute((const void*)k_fused, hipFuncAttributeMaxDynamicSharedMemorySize, LDS_TOTAL);
    k_fused<<<256, 512, LDS_TOTAL, stream>>>((const uint16_t*)d_in[0], (const uint16_t*)d_in[1], (const uint16_t*)d_in[2],
                                             (const uint16_t*)d_in[4], (const uint16_t*)d_in[5], (const uint16_t*)d_in[6],
                                             (const uint16_t*)d_in[7], (uint16_t*)d_out, (char*)d_ws
                                             );
}
```

```cpp
#include <hip/hip_runtime.h>
#include <stdint.h>
#include <type_traits>
#define TS
#include <cstdio>

typedef __attribute__((ext_vector_type(8))) __bf16 bf16x8;
typedef __attribute__((ext_vector_type(2))) __bf16 bf16x2;
typedef __attribute__((ext_vector_type(8))) _Float16 f16x8;
typedef __attribute__((ext_vector_type(2))) _Float16 f16x2;
typedef __attribute__((ext_vector_type(4))) short s16x4;
typedef __attribute__((ext_vector_type(2))) float f32x2;
typedef __attribute__((ext_vector_type(4))) float f32x4;
typedef __attribute__((ext_vector_type(16))) float f32x16;
typedef __attribute__((ext_vector_type(4))) unsigned u32x4;
typedef __attribute__((ext_vector_type(2))) unsigned u32x2;
typedef __attribute__((ext_vector_type(2))) unsigned short u16x2;
typedef unsigned long long u64;
#define LAS __attribute__((address_space(3)))
#define GAS __attribute__((address_space(1)))
#define DI __device__ __forceinline__

constexpr int RING_SLOT = 24576;
constexpr int LDS_Y = 3 * RING_SLOT;
constexpr int LDS_H = 0;
constexpr int LDS_CB = 0;
constexpr int LDS_XB = 3 * RING_SLOT;
constexpr int LDS_TAG = 6 * RING_SLOT;
constexpr int LDS_T256 = LDS_TAG + 16;
constexpr int LDS_WMAX = LDS_T256 + 2048;
constexpr int LDS_BIAS = LDS_WMAX + 256;
constexpr int LDS_XS = LDS_BIAS + 2304;
constexpr int LDS_XA = LDS_BIAS;
constexpr int LDS_TOTAL = LDS_XS + 8 * 1152 + 64;
constexpr unsigned WS_EPOCH = 0;
constexpr unsigned WS_ROWF = 256;
constexpr unsigned WS_COLF = 4096;
constexpr unsigned WS_PARTF = 8192;
constexpr unsigned ZV_PITCH = 580 * 4;
constexpr unsigned WS_ZV = 16384;
constexpr unsigned WS_VP = WS_ZV + 256 * 4 * ZV_PITCH;
constexpr unsigned WS_PART = WS_VP + 8 * 32 * 256 * 80;
constexpr unsigned WS_END = WS_PART + 8u * 2048 * 576 * 2;
constexpr unsigned OOB = 0xF0000000u;

template <int IMM>
DI void glds16(const char* sbase, unsigned voff, unsigned ldsbase) {
    asm volatile("s_add_u32 m0, %2, %3\n\ts_nop 0\n\tglobal_load_lds_dwordx4 %0, %1" ::"v"(voff), "s"(sbase), "s"(ldsbase), "i"(IMM) : "memory", "scc");
}
DI void glds4(const char* sbase, unsigned voff, unsigned ldsaddr) {
    asm volatile("s_mov_b32 m0, %2\n\ts_nop 0\n\tglobal_load_lds_dword %0, %1" ::"v"(voff), "s"(sbase), "s"(ldsaddr) : "memory");
}
#define SB0() __builtin_amdgcn_sched_barrier(0)
#define PIN(x) asm volatile("" : "+v"(x))
DI s16x4 tr_rd(LAS char* p) { return __builtin_amdgcn_ds_read_tr16_b64_v4i16((LAS s16x4*)p); }
DI unsigned pack_bf16(float a, float b) { bf16x2 p; p[0] = (__bf16)a; p[1] = (__bf16)b; return __builtin_bit_cast(unsigned, p); }
DI unsigned pack_f16(float a, float b) { f16x2 p; p[0] = (_Float16)a; p[1] = (_Float16)b; return __builtin_bit_cast(unsigned, p); }
DI float bf_lo(unsigned u) { return __uint_as_float(u << 16); }
DI float bf_hi(unsigned u) { return __uint_as_float(u & 0xffff0000u); }
DI float rbf(float f) { return bf_lo(pack_bf16(f, 0.f)); }
#define STAMP(i) do { SB0(); u64 t_, c_; asm volatile("s_memrealtime %0\n\ts_memtime %1\n\ts_waitcnt lgkmcnt(0)" : "=s"(t_), "=s"(c_) :: "memory"); tsv[i] = t_; tsc[i] = c_; SB0(); } while (0)
#define MFMA32(a, b, c) __builtin_amdgcn_mfma_f32_32x32x16_bf16((a), (b), (c), 0, 0, 0)
#define MFMA32H(a, b, c) __builtin_amdgcn_mfma_f32_32x32x16_f16((a), (b), (c), 0, 0, 0)
#define MFMA16(a, b, c) __builtin_amdgcn_mfma_f32_16x16x32_bf16((a), (b), (c), 0, 0, 0)
template <int N> DI void waitv() { asm volatile("s_waitcnt vmcnt(%0)" ::"i"(N) : "memory"); }
#define WAITL0() asm volatile("s_waitcnt lgkmcnt(0)" ::: "memory")
#define BAR() do { __builtin_amdgcn_s_barrier(); SB0(); } while (0)
#define RLX_AGENT __ATOMIC_RELAXED, __HIP_MEMORY_SCOPE_AGENT
template <int V> using IC = std::integral_constant<int, V>;
template <int A, int B, typename F> DI void static_for(F&& f) {
    if constexpr (A < B) { f(IC<A>{}); static_for<A + 1, B>(f); }
}
DI void split2(float a, float b, unsigned& hi, unsigned& lo) {
    hi = pack_bf16(a, b);
    lo = pack_bf16(a - bf_lo(hi), b - bf_hi(hi));
}
DI bf16x8 mk8(unsigned a, unsigned b, unsigned c, unsigned d) { u32x4 v = {a, b, c, d}; return __builtin_bit_cast(bf16x8, v); }
DI f16x8 mk8h(unsigned a, unsigned b, unsigned c, unsigned d) { u32x4 v = {a, b, c, d}; return __builtin_bit_cast(f16x8, v); }
DI int rho(int a, int hh) { return (a & 3) + 8 * (a >> 2) + 4 * hh; }
DI float red8(float v) {
    v += __builtin_bit_cast(float, __builtin_amdgcn_update_dpp(0, __builtin_bit_cast(int, v), 0x128, 0xf, 0xf, false));
    { auto r = __builtin_amdgcn_permlane16_swap(__float_as_uint(v), __float_as_uint(v), false, false); v = __uint_as_float(r[0]) + __uint_as_float(r[1]); }
    { auto r = __builtin_amdgcn_permlane32_swap(__float_as_uint(v), __float_as_uint(v), false, false); v = __uint_as_float(r[0]) + __uint_as_float(r[1]); }
    return v;
}

struct Dft24 { bf16x8 Ph[3], Pl[3], Qh[3], Ql[3]; };
DI void dft24_setup(Dft24& m, int lane) {
    const int col = lane & 31, hh = lane >> 5;
    float c[12], s[12];
#pragma unroll
    for (int a = 0; a < 12; a++) {
        float ang = (float)((rho(a, hh) * col) % 24) * (1.0f / 24.0f);
        c[a] = __builtin_amdgcn_cosf(ang); s[a] = __builtin_amdgcn_sinf(ang);
    }
    unsigned ch[6], cl[6], sh[6], sl[6];
#pragma unroll
    for (int i = 0; i < 6; i++) { split2(c[2 * i], c[2 * i + 1], ch[i], cl[i]); split2(s[2 * i], s[2 * i + 1], sh[i], sl[i]); }
    const unsigned N = 0x80008000u;
    m.Ph[0] = mk8(ch[0], ch[1], ch[2], ch[3]); m.Ph[1] = mk8(ch[4], ch[5], sh[0], sh[1]); m.Ph[2] = mk8(sh[2], sh[3], sh[4], sh[5]);
    m.Pl[0] = mk8(cl[0], cl[1], cl[2], cl[3]); m.Pl[1] = mk8(cl[4], cl[5], sl[0], sl[1]); m.Pl[2] = mk8(sl[2], sl[3], sl[4], sl[5]);
    m.Qh[0] = mk8(sh[0] ^ N, sh[1] ^ N, sh[2] ^ N, sh[3] ^ N); m.Qh[1] = mk8(sh[4] ^ N, sh[5] ^ N, ch[0], ch[1]); m.Qh[2] = mk8(ch[2], ch[3], ch[4], ch[5]);
    m.Ql[0] = mk8(sl[0] ^ N, sl[1] ^ N, sl[2] ^ N, sl[3] ^ N); m.Ql[1] = mk8(sl[4] ^ N, sl[5] ^ N, cl[0], cl[1]); m.Ql[2] = mk8(cl[2], cl[3], cl[4], cl[5]);
}
template <bool ASA>
DI void dft24_stage(const Dft24& m, float (&re)[12], float (&im)[12]) {
    unsigned h[12], l[12];
#pragma unroll
    for (int i = 0; i < 6; i++) { split2(re[2 * i], re[2 * i + 1], h[i], l[i]); split2(im[2 * i], im[2 * i + 1], h[6 + i], l[6 + i]); }
    f32x16 dr = {0, 0, 0, 0, 0, 0, 0, 0, 0, 0, 0, 0, 0, 0, 0, 0}, di = dr;
#pragma unroll
    for (int s = 0; s < 3; s++) {
        bf16x8 xh = mk8(h[4 * s], h[4 * s + 1], h[4 * s + 2], h[4 * s + 3]);
        bf16x8 xl = mk8(l[4 * s], l[4 * s + 1], l[4 * s + 2], l[4 * s + 3]);
        if (ASA) {
            dr = MFMA32(xl, m.Ph[s], dr); di = MFMA32(xl, m.Qh[s], di);
            dr = MFMA32(xh, m.Pl[s], dr); di = MFMA32(xh, m.Ql[s], di);
            dr = MFMA32(xh, m.Ph[s], dr); di = MFMA32(xh, m.Qh[s], di);
        } else {
            dr = MFMA32(m.Ph[s], xl, dr); di = MFMA32(m.Qh[s], xl, di);
            dr = MFMA32(m.Pl[s], xh, dr); di = MFMA32(m.Ql[s], xh, di);
            dr = MFMA32(m.Ph[s], xh, dr); di = MFMA32(m.Qh[s], xh, di);
        }
    }
#pragma unroll
    for (int a = 0; a < 12; a++) { re[a] = dr[a]; im[a] = di[a]; }
}
DI void tw576(float (&re)[12], float (&im)[12], int lane) {
    const int col = lane & 31, hh = lane >> 5;
#pragma unroll
    for (int a = 0; a < 12; a++) {
        float an = (float)(rho(a, hh) * col) * (1.0f / 576.0f);
        float c = __builtin_amdgcn_cosf(an), s = __builtin_amdgcn_sinf(an);
        float r = re[a] * c + im[a] * s;
        float i = im[a] * c - re[a] * s;
        re[a] = r; im[a] = i;
    }
}
#define MFMA16H(a, b, c) __builtin_amdgcn_mfma_f32_16x16x32_f16((a), (b), (c), 0, 0, 0)
DI unsigned rotq(unsigned p) { return __builtin_amdgcn_alignbit(p, p, 16) ^ 0x00008000u; }
struct Dft24h { f16x8 P[3], Q[3]; };
DI void dft24h_setup(Dft24h& m, int lane) {
    const int col = lane & 31, hh = lane >> 5;
    unsigned p[12];
#pragma unroll
    for (int a = 0; a < 12; a++) {
        float an = (float)(rho(a, hh) * col) * (1.0f / 24.0f);
        an = __builtin_amdgcn_fractf(an + 0.01f) - 0.01f;
        p[a] = pack_f16(__builtin_amdgcn_cosf(an), __builtin_amdgcn_sinf(an));
    }
#pragma unroll
    for (int s = 0; s < 3; s++) {
        m.P[s] = mk8h(p[4 * s], p[4 * s + 1], p[4 * s + 2], p[4 * s + 3]);
        m.Q[s] = mk8h(rotq(p[4 * s]), rotq(p[4 * s + 1]), rotq(p[4 * s + 2]), rotq(p[4 * s + 3]));
    }
}
DI void dft24h_stage(const Dft24h& m, float (&re)[12], float (&im)[12]) {
    f32x16 dr = {0, 0, 0, 0, 0, 0, 0, 0, 0, 0, 0, 0, 0, 0, 0, 0}, di = dr;
#pragma unroll
    for (int s = 0; s < 3; s++) {
        f16x8 xh = mk8h(pack_f16(re[4 * s], im[4 * s]), pack_f16(re[4 * s + 1], im[4 * s + 1]), pack_f16(re[4 * s + 2], im[4 * s + 2]), pack_f16(re[4 * s + 3], im[4 * s + 3]));
        dr = MFMA32H(xh, m.P[s], dr); di = MFMA32H(xh, m.Q[s], di);
    }
#pragma unroll
    for (int a = 0; a < 12; a++) { re[a] = dr[a]; im[a] = di[a]; }
}
DI u32x4 mat16(const float (&ang)[4]) {
    u32x4 p;
#pragma unroll
    for (int e = 0; e < 4; e++) p[e] = pack_f16(0.25f * __builtin_amdgcn_cosf(ang[e]), 0.25f * __builtin_amdgcn_sinf(ang[e]));
    return p;
}
DI u32x4 rotq4(u32x4 p) { u32x4 q = {rotq(p[0]), rotq(p[1]), rotq(p[2]), rotq(p[3])}; return q; }
DI f16x8 ash(u32x4 v) { return __builtin_bit_cast(f16x8, v); }
template <int NW>
DI void wait_flags(GAS u64* f, u64 tag, int idx) {
    for (unsigned spins = 0; spins < (1u << 20); spins++) {
        bool ok = true;
#pragma unroll
        for (int k = 0; k < NW; k++) ok &= (__hip_atomic_load(f + idx + 64 * k, RLX_AGENT) == tag);
        if (__all(ok)) break;
        __builtin_amdgcn_s_sleep(2);
    }
    __builtin_amdgcn_fence(__ATOMIC_ACQUIRE, "wavefront");
}

__global__ __launch_bounds__(512, 2) void k_fused(const uint16_t* __restrict__ x, const uint16_t* __restrict__ wr, const uint16_t* __restrict__ wi,
                                                  const uint16_t* __restrict__ w1, const uint16_t* __restrict__ b1, const uint16_t* __restrict__ w2,
                                                  const uint16_t* __restrict__ b2, uint16_t* __restrict__ out, char* __restrict__ ws
                                                  , u64* __restrict__ ts
                                                  ) {
    extern __shared__ __attribute__((aligned(16))) char smem_g[];
    LAS char* smem = (LAS char*)smem_g;
    const int tid = threadIdx.x, lane = tid & 63;
    const int w = __builtin_amdgcn_readfirstlane(tid >> 6);
    const int xg = blockIdx.x & 7, li = blockIdx.x >> 3;
    const int fs = (xg & 3) * 2 + (li & 1), t = (li >> 1) * 2 + (xg >> 2), tl = li >> 1;
    const int jb = 8 * t + fs;
    const unsigned lds0 = (unsigned)(uintptr_t)smem;
    const int rh = w & 1, ct = w >> 1;
    const __amdgpu_buffer_rsrc_t rs = __builtin_amdgcn_make_buffer_rsrc(ws, 0, WS_END, 0x00020000);
    u64 tsv[14], tsc[14];
    STAMP(0);
    const int hb = 2 * (tl % 5), r4 = tl & 3;
    int sbk[5];
#pragma unroll
    for (int i = 0; i < 4; i++) { int ii = (i + r4) & 3; sbk[i] = __builtin_amdgcn_readfirstlane(2 * ii + (2 * ii >= hb ? 1 : 0)); }
    sbk[4] = __builtin_amdgcn_readfirstlane(hb);
    const char* w1b = (const char*)w1 + fs * 1152;
    const char* w2b = (const char*)w2 + (size_t)fs * 576 * 1152;
    const unsigned ldsw = lds0 + w * 1024;
    auto mkvo = [&](bool full, unsigned pitch) {
        int ln = tid; PIN(ln); ln &= 63;
        unsigned r = full ? 4 * w + (ln >> 4) : 8 * w + (ln >> 3);
        unsigned c = full ? ((((ln >> 2) & 3) ^ (ln >> 4)) * 64) + (ln & 3) * 16 : ((((ln >> 2) & 1) ^ ((ln >> 4) & 1)) * 64) + (ln & 3) * 16;
        return r * pitch + c;
    };
    unsigned vo;
    auto gl = [&](auto U_, auto J_) {
        constexpr int U = decltype(U_)::value, J = decltype(J_)::value % 3, X = decltype(J_)::value / 3;
        constexpr int ph = U / 27, v = U % 27;
        constexpr int DST = (U % 6) * RING_SLOT + J * 8192 + X * 4096;
        if constexpr (U < 54) {
            constexpr int R = v < 3 ? 192 * v + 64 * J : 96 * ((v - 3) % 6) + 32 * J + 16 * X;
            const int blk = sbk[v < 3 ? 4 : (v - 3) / 6];
            if constexpr (ph == 0) glds16<DST>(w1b + (unsigned)__builtin_amdgcn_readfirstlane(blk * 128 + R * 9216), vo, ldsw);
            else glds16<DST>(w2b + (unsigned)__builtin_amdgcn_readfirstlane(blk * 128 + (64 * sbk[R / 128] + R % 128) * 1152), vo, ldsw);
        }
    };
    auto setvo = [&](auto U_) {
        constexpr int U = decltype(U_)::value;
        if constexpr (U == 0) vo = mkvo(false, 9216);
        if constexpr (U == 3) vo = mkvo(true, 9216);
        if constexpr (U == 27) vo = mkvo(false, 1152);
        if constexpr (U == 30) vo = mkvo(true, 1152);
    };
    auto gl3 = [&](auto U_) { setvo(U_); static_for<0, 3>([&](auto J_) { gl(U_, J_); }); };

    Dft24h m24;
    {
        const int i = lane & 7, c = lane >> 3;
        const uint16_t* xr = x + (size_t)(8 * t + w + 256 * i) * 576 + c * 8;
        u32x4 q[9];
#pragma unroll
        for (int n = 0; n < 9; n++) q[n] = *(const u32x4*)(xr + n * 64);
        u64 ep = 0;
        if (tid == 0) ep = __hip_atomic_load((GAS u64*)(ws + WS_EPOCH), RLX_AGENT);
        u32x4 bb = {0, 0, 0, 0};
        const bool dob = tid >= 384 && tid < 384 + 72;
        if (dob) bb = *(const u32x4*)((const char*)b1 + fs * 1152 + (tid - 384) * 16);
        SB0();
        gl3(IC<0>{});
        SB0();
        f32x2 tw;
        {
            float an = (float)(((tid >> 4) & 15) * (8 * (tid & 15) + fs)) * (1.0f / 2048.0f);
            tw[0] = __builtin_amdgcn_cosf(an); tw[1] = __builtin_amdgcn_sinf(an);
        }
        if (w >= 4) dft24h_setup(m24, lane);
        SB0();
        f32x2 d[36];
        f32x2 s2 = {0.f, 0.f};
#pragma unroll
        for (int n = 0; n < 9; n++)
#pragma unroll
            for (int e = 0; e < 4; e++) { f32x2 v = {bf_lo(q[n][e]), bf_hi(q[n][e])}; d[4 * n + e] = v; s2 += v; }
        const float mu = red8(s2[0] + s2[1]) * (1.0f / 576.0f);
        const f32x2 mu2 = {mu, mu};
        f32x2 q2 = {0.f, 0.f};
#pragma unroll
        for (int e = 0; e < 36; e++) { d[e] -= mu2; q2 = __builtin_elementwise_fma(d[e], d[e], q2); }
        const float var = red8(q2[0] + q2[1]) * (1.0f / 576.0f);
        const float m1 = rbf(mu);
        const float is1 = 1.0f / rbf(__builtin_sqrtf(rbf(rbf(var) + 1e-5f)));
        const float mean2 = (mu - m1) * is1;
        const float var2 = var * is1 * is1;
        const float is2 = 1.0f / rbf(__builtin_sqrtf(rbf(rbf(var2) + 1e-5f)));
        const float ya = is1 * is2, yb = (mean2 - rbf(mean2)) * is2;
        const f32x2 ya2 = {ya, ya}, yb2 = {yb, yb};
        const int mloc = 8 * w + i;
        LAS char* yw = smem + LDS_Y + (mloc >> 5) * 36 * 1024 + (c >> 1) * 1024 + ((mloc & 31) + 32 * (c & 1)) * 16;
#pragma unroll
        for (int n = 0; n < 9; n++) {
            u32x4 o;
#pragma unroll
            for (int e = 0; e < 4; e++) { f32x2 v = __builtin_elementwise_fma(d[4 * n + e], ya2, yb2); o[e] = pack_bf16(v[0], v[1]); }
            *(LAS u32x4*)(yw + n * 4096) = o;
        }
        if (w == fs) {
            const f32x2 xa = {is1, is1}, xb = {mean2, mean2};
            const int pos = (i & 1) ? 4 + (i >> 1) : ((i >> 2) | (i & 2));
            LAS char* xw = smem + LDS_XS + pos * 1152 + c * 16;
#pragma unroll
            for (int n = 0; n < 9; n++) {
                u32x4 o;
#pragma unroll
                for (int e = 0; e < 4; e++) { f32x2 v = __builtin_elementwise_fma(d[4 * n + e], xa, xb); o[e] = pack_bf16(v[0], v[1]); }
                *(LAS u32x4*)(xw + n * 128) = o;
            }
        }
        if (dob) {
            f32x4 lo = {bf_lo(bb[0]), bf_hi(bb[0]), bf_lo(bb[1]), bf_hi(bb[1])}, hi = {bf_lo(bb[2]), bf_hi(bb[2]), bf_lo(bb[3]), bf_hi(bb[3])};
            *(LAS f32x4*)(smem + LDS_BIAS + (tid - 384) * 32) = lo;
            *(LAS f32x4*)(smem + LDS_BIAS + (tid - 384) * 32 + 16) = hi;
        }
        if (tid == 0) *(LAS u64*)(smem + LDS_TAG) = ep;
        if (tid < 256) *(LAS f32x2*)(smem + LDS_T256 + tid * 8) = tw;
        waitv<0>();
        WAITL0();
        gl3(IC<1>{}); gl3(IC<2>{});
    }
    BAR();
    STAMP(1);
    u64 epoch;
    {
        u32x2 e2 = *(LAS u32x2*)(smem + LDS_TAG);
        epoch = ((u64)(unsigned)__builtin_amdgcn_readfirstlane(e2[1]) << 32) | (unsigned)__builtin_amdgcn_readfirstlane(e2[0]);
    }
    const u64 tag = (epoch + 1) * 0x9E3779B97F4A7C15ull + 0x632BE59BD9B4E019ull;

    auto put_flag = [&](bool me, unsigned off, u64 val) {
        u32x2 o = {(unsigned)val, (unsigned)(val >> 32)};
        __builtin_amdgcn_raw_buffer_store_b64(o, rs, me ? 0u : OOB, off, 16);
    };
    LAS char* trc;
    auto settr = [&](auto U_) {
        constexpr int U = decltype(U_)::value, v = U % 27, ph = U / 27;
        if constexpr (v == 0 || v == 3) {
            int ln = tid; PIN(ln); ln &= 63;
            int G = ln >> 4, q = (ln >> 2) & 3, p = ln & 3;
            int row = (ph == 0 ? 8 : 4) * (G >> 1) + q;
            if constexpr (v == 3) trc = smem + row * 256 + ((ct ^ q) * 64) + (G & 1) * 32 + p * 8 + ((U % 6) / 2) * 2 * RING_SLOT;
            else trc = smem + row * 128 + (((ct & 1) ^ ((q >> 1) & 1)) * 64) + (G & 1) * 32 + p * 8 + ((U % 6) / 2) * 2 * RING_SLOT;
            PIN(trc);
        } else if constexpr (U % 2 == 0) {
            trc += (U % 6 == 0) ? -4 * RING_SLOT : 2 * RING_SLOT;
            PIN(trc);
        }
    };
    bf16x8 af[36];
    {
        LAS char* ab = smem + LDS_Y + rh * 36 * 1024 + lane * 16; PIN(ab);
#pragma unroll
        for (int j = 0; j < 36; j++) af[j] = *(LAS bf16x8*)(ab + j * 1024);
    }
    LAS char* biasp = smem + LDS_BIAS + (32 * ct + 4 * (lane >> 5)) * 4;
    const unsigned pbase = WS_PART + (unsigned)(((fs * 32 + t) * 36 + 2 * ct + rh) * 2048);
    unsigned plane = 0;

    f32x16 acc[2];
    s16x4 bl[6], bh[6];
    float gt[4];
    unsigned hq[5][8];
    auto ldbias = [&](auto P_, auto Q_, int blk) {
        constexpr int P = decltype(P_)::value, Q = decltype(Q_)::value;
        f32x4 b = *(LAS f32x4*)(biasp + blk * 256 + Q * 32);
        acc[P][4 * Q] = b[0]; acc[P][4 * Q + 1] = b[1]; acc[P][4 * Q + 2] = b[2]; acc[P][4 * Q + 3] = b[3];
    };
    auto gop = [&](auto C_, auto K_) {
        constexpr int C = decltype(C_)::value, K = decltype(K_)::value, P = C == 4 ? 1 : (C & 1);
        constexpr int NB = C == 4 ? 1 : C + 2;
        const float c0 = -2.0f * 0.7978845608028654f * 1.4426950408889634f;
        const float c1 = c0 * 0.044715f;
        if constexpr (K < 124) {
            constexpr int Q = K / 31, k = K % 31;
            if constexpr (k < 28) {
                constexpr int st = k / 4, e = k % 4;
                float xx = acc[P][4 * Q + e];
                if constexpr (st == 0) gt[e] = xx * xx;
                else if constexpr (st == 1) gt[e] = __builtin_fmaf(gt[e], c1, c0);
                else if constexpr (st == 2) gt[e] = xx * gt[e];
                else if constexpr (st == 3) gt[e] = __builtin_amdgcn_exp2f(gt[e]);
                else if constexpr (st == 4) gt[e] = 1.0f + gt[e];
                else if constexpr (st == 5) gt[e] = __builtin_amdgcn_rcpf(gt[e]);
                else gt[e] = xx * gt[e];
                PIN(gt[e]);
            } else if constexpr (k == 28) { hq[C][2 * Q] = pack_bf16(gt[0], gt[1]); PIN(hq[C][2 * Q]); }
            else if constexpr (k == 29) { hq[C][2 * Q + 1] = pack_bf16(gt[2], gt[3]); PIN(hq[C][2 * Q + 1]); }
            else { if constexpr (NB <= 3) ldbias(IC<P>{}, IC<Q>{}, sbk[NB]); }
        }
    };
    auto grange = [&](auto C_, auto A_, auto B_) {
        static_for<decltype(A_)::value, decltype(B_)::value>([&](auto K_) { gop(C_, K_); });
    };
    auto pstore = [&](auto P_, auto H_, int blk) {
        constexpr int P = decltype(P_)::value, H = decltype(H_)::value;
        u32x4 o;
        o[0] = pack_bf16(acc[P][8 * H + 0], acc[P][8 * H + 1]); o[1] = pack_bf16(acc[P][8 * H + 2], acc[P][8 * H + 3]);
        o[2] = pack_bf16(acc[P][8 * H + 4], acc[P][8 * H + 5]); o[3] = pack_bf16(acc[P][8 * H + 6], acc[P][8 * H + 7]);
        __builtin_amdgcn_raw_buffer_store_b128(o, rs, plane, pbase + blk * 8192 + H * 1024, 16);
    };
    const f32x16 zero16 = {0, 0, 0, 0, 0, 0, 0, 0, 0, 0, 0, 0, 0, 0, 0, 0};
    auto bfrag = [&](auto J_) {
        constexpr int J = decltype(J_)::value;
        return __builtin_bit_cast(bf16x8, __builtin_shufflevector(bl[J], bh[J], 0, 1, 2, 3, 4, 5, 6, 7));
    };
    auto fslot = [&](auto U_, auto LO_, auto GI_, auto&& fill) {
        constexpr int U = decltype(U_)::value, ph = U / 27, v = U % 27 - 3, c = v / 6, s = v % 6;
        constexpr bool LO = decltype(LO_)::value;
        constexpr int PB = c & 1;
        constexpr int PA = s == 0 ? PB ^ 1 : PB;
        constexpr int sp = (s + 5) % 6;
        constexpr int R2 = ph == 0 ? 1024 : 2048;
        settr(U_);
        LAS char* sb = trc + (U % 2) * RING_SLOT;
        setvo(GI_);
        static_for<0, 6>([&](auto G_) {
            constexpr int G = decltype(G_)::value;
            if constexpr (G < 3) {
                if constexpr (LO) acc[PA] = MFMA32(bfrag(IC<G + 3>{}), af[6 * sp + G + 3], acc[PA]);
            } else {
                acc[PB] = MFMA32(bfrag(IC<G - 3>{}), af[6 * s + G - 3], (ph == 1 && s == 0 && G == 3) ? zero16 : acc[PB]);
            }
            SB0();
            bl[G] = tr_rd(sb + G * 4096); bh[G] = tr_rd(sb + G * 4096 + R2);
            if constexpr (G < 3) gl(GI_, G_);
            fill(G_);
            SB0();
        });
    };
    auto hslot = [&](auto U_, auto&& issue) {
        constexpr int U = decltype(U_)::value, ph = U / 27, s = U % 27;
        constexpr int R2 = ph == 0 ? 512 : 1024;
        settr(U_);
        LAS char* sb = trc + (U % 2) * RING_SLOT;
        static_for<0, 12>([&](auto G_) {
            constexpr int G = decltype(G_)::value;
            if constexpr (G < 3) {
                if constexpr (s > 0) acc[1] = MFMA32(bfrag(IC<G + 3>{}), af[12 * s - 3 + G], acc[1]);
            } else {
                acc[1] = MFMA32(bfrag(IC<(G - 3) % 6>{}), af[12 * s + G - 3], (ph == 1 && s == 0 && G == 3) ? zero16 : acc[1]);
            }
            SB0();
            bl[G % 6] = tr_rd(sb + G * 2048); bh[G % 6] = tr_rd(sb + G * 2048 + R2);
            issue(G_);
            SB0();
        });
    };
    auto iss2 = [&](auto GI_, auto GI2_) {
        return [&, GI_, GI2_](auto G_) {
            constexpr int G = decltype(G_)::value;
            if constexpr (G == 0) setvo(GI_);
            if constexpr (G < 6 && G % 2 == 0) gl(GI_, IC<G / 2>{});
            if constexpr (G >= 6 && G % 2 == 0) gl(GI2_, IC<G / 2 - 3>{});
        };
    };
    using T = std::true_type; using F = std::false_type;
    auto nofill = [&](auto) {};

    if (w < 4) {
        static_for<0, 4>([&](auto Q_) { ldbias(IC<1>{}, Q_, sbk[4]); ldbias(IC<0>{}, Q_, sbk[0]); });
        hslot(IC<0>{}, nofill); WAITL0(); waitv<3>(); BAR();
        hslot(IC<1>{}, iss2(IC<3>{}, IC<4>{})); waitv<6>(); BAR();
        hslot(IC<2>{}, iss2(IC<5>{}, IC<99>{})); waitv<6>(); BAR();
        STAMP(9);
        static_for<3, 9>([&](auto U_) {
            constexpr int U = decltype(U_)::value, s = U - 3;
            fslot(U_, T{}, IC<U + 3>{}, [&](auto G_) {
                constexpr int gi = 6 * s + decltype(G_)::value - 4;
                if constexpr (gi >= 0) grange(IC<4>{}, IC<(gi * 124) / 32>{}, IC<((gi + 1) * 124) / 32>{});
            });
            waitv<6>(); BAR();
        });
    } else {
        const int v = w - 4;
        const unsigned PP = 0x3F803F80u, PM = 0xBF803F80u  , MP = 0x3F80BF80u  , MM = 0xBF80BF80u;
        unsigned A0, A1, A2, A3, B0, B1, B2, B3; float cc;
        const float c8 = 0.70710678118654752f;
        if (v == 0) { A0 = PP; A1 = PP; A2 = PP; A3 = PP; B0 = PP; B1 = PP; B2 = MM; B3 = MM; cc = 1.f; }
        else if (v == 1) { A0 = PM; A1 = 0; A2 = PM; A3 = MP; B0 = 0; B1 = MP; B2 = MM; B3 = PP; cc = c8; }
        else if (v == 2) { A0 = PP; A1 = MM; A2 = 0; A3 = 0; B0 = 0; B1 = 0; B2 = MP; B3 = MP; cc = 1.f; }
        else { A0 = PM; A1 = 0; A2 = MP; A3 = PM; B0 = 0; B1 = PM; B2 = MM; B3 = PP; cc = c8; }
        float re[12], im[12];
        auto dot = [](unsigned a, unsigned b, float c) { return __builtin_amdgcn_fdot2_f32_bf16(__builtin_bit_cast(bf16x2, a), __builtin_bit_cast(bf16x2, b), c, false); };
        const int G = lane >> 4, q = (lane >> 2) & 3, p = lane & 3;
        LAS char* xb = smem + LDS_XS + q * 1152 + (96 * (G >> 1) + 16 * (G & 1) + 4 * p) * 2;
        auto rad8 = [&](int a) {
            u32x2 ev = __builtin_bit_cast(u32x2, tr_rd(xb + rho(a, 0) * 48));
            u32x2 od = __builtin_bit_cast(u32x2, tr_rd(xb + rho(a, 0) * 48 + 4 * 1152));
            re[a] = __builtin_fmaf(dot(od[0], A2, dot(od[1], A3, 0.f)), cc, dot(ev[0], A0, dot(ev[1], A1, 0.f)));
            im[a] = __builtin_fmaf(dot(od[0], B2, dot(od[1], B3, 0.f)), cc, dot(ev[0], B0, dot(ev[1], B1, 0.f)));
            PIN(re[a]); PIN(im[a]);
        };
#pragma unroll
        for (int a = 0; a < 6; a++) rad8(a);
        WAITL0(); waitv<3>(); SB0(); BAR();
        gl3(IC<3>{}); gl3(IC<4>{});
#pragma unroll
        for (int a = 6; a < 12; a++) rad8(a);
        dft24h_stage(m24, re, im);
        waitv<6>(); SB0(); BAR();
        gl3(IC<5>{});
        tw576(re, im, lane);
        dft24h_stage(m24, re, im);
        {
            const int hh = lane >> 5;
            const unsigned zo = (lane & 31) < 24 ? (unsigned)(((lane & 31) * 24 + 4 * hh) * 4) : OOB;
            const unsigned zb = WS_ZV + (unsigned)(jb * 4 + v) * ZV_PITCH;
            unsigned z0 = 0;
#pragma unroll
            for (int qd = 0; qd < 3; qd++) {
                u32x4 o;
#pragma unroll
                for (int e = 0; e < 4; e++) o[e] = pack_f16(re[4 * qd + e] * (1.0f / 64.0f), im[4 * qd + e] * (1.0f / 64.0f));
                if (qd == 0) z0 = o[0];
                __builtin_amdgcn_raw_buffer_store_b128(o, rs, zo, zb + qd * 32, 16);
            }
            __builtin_amdgcn_raw_buffer_store_b32(z0, rs, lane == 0 ? 0u : OOB, zb + 576 * 4, 16);
        }
        static_for<0, 4>([&](auto Q_) { ldbias(IC<0>{}, Q_, sbk[0]); ldbias(IC<1>{}, Q_, sbk[1]); });
        waitv<10>(); BAR();
        static_for<3, 9>([&](auto U_) {
            constexpr int U = decltype(U_)::value;
            if constexpr (U == 3) fslot(U_, F{}, IC<U + 3>{}, nofill); else fslot(U_, T{}, IC<U + 3>{}, nofill);
            if constexpr (U <= 4) waitv<10>(); else waitv<6>();
            BAR();
            if constexpr (U == 5) __builtin_amdgcn_raw_buffer_store_b32((unsigned)(tag >> 32), rs, tid == 256 ? 0u : OOB, WS_ROWF + jb * 4, 16);
        });
    }
    STAMP(10);
    u32x4 rf;
    static_for<9, 27>([&](auto U_) {
        constexpr int U = decltype(U_)::value, c = (U - 3) / 6, s = (U - 3) % 6;
        fslot(U_, T{}, IC<U + 3>{}, [&](auto G_) {
            constexpr int gi = 6 * s + decltype(G_)::value - 4;
            if constexpr (gi >= 0) grange(IC<c - 1>{}, IC<(gi * 124) / 32>{}, IC<((gi + 1) * 124) / 32>{});
            if constexpr (U == 24 && decltype(G_)::value == 4) {
                int ln = tid; PIN(ln); ln &= 63;
                rf = __builtin_amdgcn_raw_buffer_load_b128(rs, ln * 16, WS_ROWF, 16);
            }
        });
        if constexpr (U < 24) { waitv<6>(); BAR(); }
        else if constexpr (U < 26) { waitv<7>(); BAR(); }
    });
    static_for<0, 3>([&](auto G_) { constexpr int G = decltype(G_)::value; acc[1] = MFMA32(bfrag(IC<G + 3>{}), af[33 + G], acc[1]); });
    STAMP(2);
    WAITL0();
    waitv<0>();
    BAR();
    u32x4 za[4], zb[4];
    unsigned short wa[4][4], wb[4][4];
    if (w >= 4) {
        int ln = tid; PIN(ln); ln &= 63;
        const int r = fs, col = ln & 15, g = ln >> 4, wq = w - 4;
        const unsigned tg = (unsigned)(tag >> 32);
        for (unsigned spins = 0; spins < (1u << 20); spins++) {
            if (__all(rf[0] == tg && rf[1] == tg && rf[2] == tg && rf[3] == tg)) break;
            __builtin_amdgcn_s_sleep(2);
            rf = __builtin_amdgcn_raw_buffer_load_b128(rs, ln * 16, WS_ROWF, 16);
        }
        __builtin_amdgcn_fence(__ATOMIC_ACQUIRE, "wavefront");
        put_flag(jb == 0 && tid == 256, WS_EPOCH, epoch + 1);
        const int vA = r <= 3 ? r : 0, vB = r >= 5 ? 8 - r : 0;
        const bool useA = r <= 4 && col < 10, useB = (r == 0 || r >= 4) && col < 10;
        const unsigned lo = (unsigned)(64 * g) * 4 * ZV_PITCH;
        const unsigned loA = useA ? lo + col * 4 : OOB, loB = useB ? lo + (15 - col) * 4 : OOB;
        const unsigned sA = WS_ZV + vA * ZV_PITCH + 9 * t * 4 + wq * 16 * ZV_PITCH, sB = WS_ZV + vB * ZV_PITCH + (561 - 9 * t) * 4 + wq * 16 * ZV_PITCH;
#pragma unroll
        for (int tl_ = 0; tl_ < 4; tl_++)
#pragma unroll
            for (int e = 0; e < 4; e++) {
                za[tl_][e] = __builtin_amdgcn_raw_buffer_load_b32(rs, loA, sA + (16 * e + tl_) * 4 * ZV_PITCH, 16);
                zb[tl_][e] = __builtin_amdgcn_raw_buffer_load_b32(rs, loB, sB + (16 * e + tl_) * 4 * ZV_PITCH, 16);
            }
        const unsigned wo = (unsigned)((8 * (4 * wq + 64 * g) + r) * 289 + 9 * t + (col < 10 ? col : 9));
#pragma unroll
        for (int tl_ = 0; tl_ < 4; tl_++)
#pragma unroll
            for (int e = 0; e < 4; e++) { wa[tl_][e] = wr[wo + (8 * tl_ + 128 * e) * 289]; wb[tl_][e] = wi[wo + (8 * tl_ + 128 * e) * 289]; }
        SB0();
    }
    LAS char* ab;
    {
        int ln = tid; PIN(ln); ln &= 63;
        plane = ln * 16;
        ab = smem + LDS_H + rh * 36 * 1024 + ln * 16;
        LAS char* hw = ab + 2 * ct * 1024;
        auto hst = [&](auto C_) {
            constexpr int C = decltype(C_)::value;
            u32x4 o0 = {hq[C][0], hq[C][1], hq[C][2], hq[C][3]}, o1 = {hq[C][4], hq[C][5], hq[C][6], hq[C][7]};
            *(LAS u32x4*)(hw + C * 8192) = o0; *(LAS u32x4*)(hw + C * 8192 + 1024) = o1;
        };
        hst(IC<0>{}); hst(IC<1>{}); hst(IC<2>{});
        if (w < 4) hst(IC<4>{});
        grange(IC<3>{}, IC<0>{}, IC<124>{});
        hst(IC<3>{});
        WAITL0();
        BAR();
    }
    STAMP(3);
    if (w < 4) {
#pragma unroll
        for (int j = 0; j < 36; j++) af[j] = *(LAS bf16x8*)(ab + j * 1024);
        hslot(IC<27>{}, nofill); WAITL0(); BAR();
        hslot(IC<28>{}, [&](auto G_) {
            constexpr int G = decltype(G_)::value;
            if constexpr (G == 0) setvo(IC<30>{});
            gl(IC<30 + G / 6>{}, IC<G % 6>{});
        }); BAR();
        hslot(IC<29>{}, iss2(IC<32>{}, IC<99>{})); waitv<9>(); BAR();
        STAMP(11);
        fslot(IC<30>{}, T{}, IC<33>{}, [&](auto G_) { if constexpr (decltype(G_)::value == 5) pstore(IC<1>{}, IC<0>{}, sbk[4]); }); waitv<7>(); BAR();
        fslot(IC<31>{}, T{}, IC<34>{}, [&](auto G_) { if constexpr (decltype(G_)::value == 5) pstore(IC<1>{}, IC<1>{}, sbk[4]); }); waitv<8>(); BAR();
        fslot(IC<32>{}, T{}, IC<35>{}, nofill); waitv<8>(); BAR();
        fslot(IC<33>{}, T{}, IC<36>{}, nofill); waitv<7>(); BAR();
    } else {
        int ln = tid; PIN(ln); ln &= 63;
        const int r = fs, col = ln & 15, g = ln >> 4, wq = w - 4;
        const f32x4 z4 = {0.f, 0.f, 0.f, 0.f};
        f32x4 dr[4], di[4];
        unsigned wp[4][4];
        LAS char* xw = smem + ((4 * g * 4 + wq) * 10 + (col < 10 ? col : 9)) * 16;
        LAS char* xr = smem + ((4 * wq * 4 + g) * 10 + (col < 10 ? col : 9)) * 16;
        {
            float an[4];
#pragma unroll
            for (int e = 0; e < 4; e++) an[e] = (float)(((4 * g + e) * (8 * col + r)) & 127) * (1.0f / 128.0f) + (r == 4 ? 0.25f : 0.f);
            const u32x4 P1 = mat16(an), Q1 = rotq4(P1);
            const unsigned fl = r == 4 ? 0x00008000u : 0x80000000u;
            const u32x4 P2 = P1 ^ fl, Q2 = Q1 ^ fl;
#pragma unroll
            for (int tl_ = 0; tl_ < 4; tl_++) {
                dr[tl_] = MFMA16H(ash(P1), ash(za[tl_]), z4); di[tl_] = MFMA16H(ash(Q1), ash(za[tl_]), z4);
                dr[tl_] = MFMA16H(ash(P2), ash(zb[tl_]), dr[tl_]); di[tl_] = MFMA16H(ash(Q2), ash(zb[tl_]), di[tl_]);
            }
            u32x4 o[4];
#pragma unroll
            for (int tl_ = 0; tl_ < 4; tl_++) {
                const f32x4 t0 = *(LAS f32x4*)(smem + LDS_T256 + ((4 * wq + tl_) * 16 + 4 * g) * 8), t1 = *(LAS f32x4*)(smem + LDS_T256 + ((4 * wq + tl_) * 16 + 4 * g) * 8 + 16);
#pragma unroll
                for (int e = 0; e < 4; e++) {
                    const float c = e < 2 ? t0[2 * e] : t1[2 * e - 4], sn = e < 2 ? t0[2 * e + 1] : t1[2 * e - 3];
                    o[e][tl_] = pack_f16(dr[tl_][e] * c + di[tl_][e] * sn, di[tl_][e] * c - dr[tl_][e] * sn);
                }
            }
            if (col < 10) {
#pragma unroll
                for (int e = 0; e < 4; e++) *(LAS u32x4*)(xw + LDS_XA + e * 640) = o[e];
            }
            u16x2 mx = {0, 0};
#pragma unroll
            for (int tl_ = 0; tl_ < 4; tl_++)
#pragma unroll
                for (int e = 0; e < 4; e++) {
                    wp[tl_][e] = (unsigned)wa[tl_][e] | ((unsigned)wb[tl_][e] << 16);
                    mx = __builtin_elementwise_max(mx, __builtin_bit_cast(u16x2, wp[tl_][e] & 0x7fff7fffu));
                    PIN(wp[tl_][e]);
                }
            unsigned m1 = mx[0] > mx[1] ? mx[0] : mx[1];
            { auto q = __builtin_amdgcn_permlane16_swap(m1, m1, false, false); m1 = q[0] > q[1] ? q[0] : q[1]; }
            { auto q = __builtin_amdgcn_permlane32_swap(m1, m1, false, false); m1 = q[0] > q[1] ? q[0] : q[1]; }
            *(LAS unsigned*)(smem + LDS_WMAX + col * 16 + wq * 4) = m1;
            SB0();
#pragma unroll
            for (int j = 0; j < 36; j++) af[j] = *(LAS bf16x8*)(ab + j * 1024);
            WAITL0(); SB0(); BAR();
        }
        float inv;
        {
            float an[4];
#pragma unroll
            for (int e = 0; e < 4; e++) an[e] = (float)(((4 * g + e) * col) & 15) * (1.0f / 16.0f);
            const u32x4 P3 = mat16(an), Q3 = rotq4(P3);
            const u32x4 m4 = *(LAS u32x4*)(smem + LDS_WMAX + col * 16);
            unsigned ex = max(max(m4[0], m4[1]), max(m4[2], m4[3])) >> 7;
            ex = min(max(ex, 1u), 252u);
            const float wsc = __uint_as_float((253u - ex) << 23);
            inv = __uint_as_float((ex + 1u) << 23);
#pragma unroll
            for (int tl_ = 0; tl_ < 4; tl_++) {
                const u32x4 xa = *(LAS u32x4*)(xr + LDS_XA + tl_ * 640);
                dr[tl_] = MFMA16H(ash(P3), ash(xa), z4); di[tl_] = MFMA16H(ash(Q3), ash(xa), z4);
            }
#pragma unroll
            for (int tl_ = 0; tl_ < 4; tl_++) {
                u32x4 y;
#pragma unroll
                for (int e = 0; e < 4; e++) {
                    const float wre = bf_lo(wp[tl_][e]) * wsc, wim = bf_hi(wp[tl_][e]) * wsc;
                    y[e] = pack_f16(dr[tl_][e] * wre - di[tl_][e] * wim, -(dr[tl_][e] * wim + di[tl_][e] * wre));
                }
                dr[tl_] = MFMA16H(ash(P3), ash(y), z4); di[tl_] = MFMA16H(ash(Q3), ash(y), z4);
            }
#pragma unroll
            for (int e = 0; e < 4; e++) {
                const f32x4 t0 = *(LAS f32x4*)(smem + LDS_T256 + ((4 * g + e) * 16 + 4 * wq) * 8), t1 = *(LAS f32x4*)(smem + LDS_T256 + ((4 * g + e) * 16 + 4 * wq) * 8 + 16);
                u32x4 o;
#pragma unroll
                for (int tl_ = 0; tl_ < 4; tl_++) {
                    const float c = tl_ < 2 ? t0[2 * tl_] : t1[2 * tl_ - 4], sn = tl_ < 2 ? t0[2 * tl_ + 1] : t1[2 * tl_ - 3];
                    o[tl_] = pack_f16(dr[tl_][e] * c + di[tl_][e] * sn, di[tl_][e] * c - dr[tl_][e] * sn);
                }
                if (col < 10) *(LAS u32x4*)(xw + LDS_XB + e * 640) = o;
            }
            WAITL0(); SB0(); BAR();
        }
        {
            setvo(IC<30>{});
            gl3(IC<32>{});
            float an[4];
#pragma unroll
            for (int e = 0; e < 4; e++) an[e] = (float)((col * (8 * (4 * g + e) + r)) & 127) * (1.0f / 128.0f);
            const u32x4 P4 = mat16(an), Q4 = rotq4(P4);
            const float fsc = (64.0f * 256.0f / (2048.0f * 576.0f)) * ((r & 3) == 0 ? 0.5f : 1.0f);
            const unsigned vo_ = col < 10 ? (unsigned)(64 * g * 80 + col * 8) : OOB;
            const unsigned vb = WS_VP + (unsigned)(r * 32 + t) * (256 * 80) + wq * 4 * 80;
#pragma unroll
            for (int tl_ = 0; tl_ < 4; tl_++) {
                const u32x4 xa = *(LAS u32x4*)(xr + LDS_XB + tl_ * 640);
                dr[tl_] = MFMA16H(ash(P4), ash(xa), z4); di[tl_] = MFMA16H(ash(Q4), ash(xa), z4);
            }
#pragma unroll
            for (int tl_ = 0; tl_ < 4; tl_++)
#pragma unroll
                for (int e = 0; e < 4; e++) {
                    u32x2 o = {__float_as_uint(dr[tl_][e] * fsc * inv), __float_as_uint(di[tl_][e] * fsc * inv)};
                    __builtin_amdgcn_raw_buffer_store_b64(o, rs, vo_, vb + (16 * e + tl_) * 80, 16);
                }
            SB0(); BAR();
        }
        fslot(IC<30>{}, F{}, IC<33>{}, nofill); BAR();
        fslot(IC<31>{}, T{}, IC<34>{}, nofill); waitv<22>(); BAR();
        fslot(IC<32>{}, T{}, IC<35>{}, nofill); waitv<6>(); BAR();
        put_flag(tid == 256, WS_COLF + (fs * 32 + t) * 8, tag);
        fslot(IC<33>{}, T{}, IC<36>{}, nofill); waitv<6>(); BAR();
    }
    STAMP(12);
    static_for<34, 54>([&](auto U_) {
        constexpr int U = decltype(U_)::value, v = U - 30, c = v / 6, s = v % 6;
        auto fill = [&](auto G_) {
            constexpr int G = decltype(G_)::value;
            if constexpr (c >= 1 && s == 0 && G == 5) pstore(IC<(c + 1) & 1>{}, IC<0>{}, sbk[c >= 1 ? c - 1 : 0]);
            if constexpr (c >= 1 && s == 1 && G == 5) pstore(IC<(c + 1) & 1>{}, IC<1>{}, sbk[c >= 1 ? c - 1 : 0]);
        };
        constexpr int ns = (c >= 1) ? ((s == 0 ? 1 : 0) + (s == 1 ? 2 : 0) + (s == 2 ? 2 : 0) + (s == 3 ? 1 : 0)) : 0;
        fslot(U_, T{}, IC<U + 3>{}, fill);
        if constexpr (U <= 50) { waitv<6 + ns>(); BAR(); }
        else if constexpr (U == 51) { waitv<3 + ns>(); BAR(); }
        else if constexpr (U == 52) { waitv<0>(); BAR(); }
    });
    static_for<0, 3>([&](auto G_) { constexpr int G = decltype(G_)::value; acc[1] = MFMA32(bfrag(IC<G + 3>{}), af[33 + G], acc[1]); });
    pstore(IC<1>{}, IC<0>{}, sbk[3]);
    pstore(IC<1>{}, IC<1>{}, sbk[3]);
    STAMP(4);
    WAITL0();
    waitv<0>();
    BAR();
    STAMP(5);
    put_flag(tid == 0, WS_PARTF + (t * 16 + fs) * 8, tag);
    if (w >= 4) {
        const int v = w - 4, hh = lane >> 5;
        int n2 = lane & 31; if (n2 > 23) n2 = 23;
        const int rB = v == 0 ? 4 : 8 - v;
        wait_flags<1>((GAS u64*)(ws + WS_COLF), tag, (lane < 32 ? v : rB) * 32 + (lane & 31));
        float re[12], im[12];
        {
            u32x2 A[12], B[12];
#pragma unroll
            for (int a = 0; a < 12; a++) {
                const int k = 24 * rho(a, hh) + n2;
                const bool mir = k > 288; const int kk = mir ? 576 - k : k;
                int gq = (kk * 57) >> 9; if (gq > 31) gq = 31;
                const unsigned off = (unsigned)gq * (256 * 80) + (unsigned)(kk - 9 * gq) * 8;
                const unsigned rsel = (v != 0 && mir) ? rB : v;
                A[a] = __builtin_amdgcn_raw_buffer_load_b64(rs, off + rsel * (32 * 256 * 80), WS_VP + jb * 80, 16);
                const bool needB = v == 0 || k == 0 || k == 288;
                B[a] = __builtin_amdgcn_raw_buffer_load_b64(rs, needB ? off + rB * (32 * 256 * 80) : OOB, WS_VP + jb * 80, 16);
            }
#pragma unroll
            for (int a = 0; a < 12; a++) {
                const int k = 24 * rho(a, hh) + n2;
                const bool mir = k > 288, spec = (k == 0 || k == 288);
                float ax = __uint_as_float(A[a][0]), ay = __uint_as_float(A[a][1]), bx = __uint_as_float(B[a][0]), by = __uint_as_float(B[a][1]);
                if (mir) { ay = -ay; by = -by; }
                if (v == 0) {
                    if (spec) { ay = 0.f; by = 0.f; }
                    re[a] = ax + by; im[a] = ay - bx;
                } else {
                    re[a] = spec ? 0.5f * (ax + bx) : ax; im[a] = spec ? 0.5f * (ay - by) : ay;
                }
            }
        }
        Dft24 m; dft24_setup(m, lane);
        dft24_stage<true>(m, re, im);
        tw576(re, im, lane);
        dft24_stage<false>(m, re, im);
        if ((lane & 31) < 24) {
#pragma unroll
            for (int a = 0; a < 12; a++) {
                f32x2 o = {re[a], -im[a]};
                *(LAS f32x2*)(smem + LDS_CB + (v * 576 + (lane & 31) + 24 * rho(a, hh)) * 8) = o;
            }
        }
        WAITL0();
    } else if (w == 0) {
        wait_flags<1>((GAS u64*)(ws + WS_PARTF) + t * 16, tag, lane & 7);
    }
    BAR();
    STAMP(6);
#pragma unroll
    for (int rep = 0; rep < 2; rep++) {
        if (rep == 1 && w != 0) break;
        const int pid = tid + 512 * rep;
        const int i = pid & 7, pc = pid >> 3, ehh = pc & 1, H = (pc >> 1) & 1, ctg = pc >> 2;
        const int mloc = 8 * fs + i;
        u32x4 pv[8];
        const unsigned po = (unsigned)((ctg * 2 + (mloc >> 5)) * 2 + H) * 1024 + ((mloc & 31) + 32 * ehh) * 16;
#pragma unroll
        for (int f = 0; f < 8; f++) pv[f] = __builtin_amdgcn_raw_buffer_load_b128(rs, po, WS_PART + (unsigned)(f * 32 + t) * 73728, 16);
        const int d0 = 32 * ctg + 16 * H + 4 * ehh;
        const size_t rowoff = (size_t)(jb + 256 * i) * 576;
        u32x2 xa = *(const u32x2*)(x + rowoff + d0), xb = *(const u32x2*)(x + rowoff + d0 + 8);
        u32x2 ba = *(const u32x2*)(b2 + d0), bb = *(const u32x2*)(b2 + d0 + 8);
        float cr[4], ci[4];
        cr[0] = 1.f; ci[0] = (i & 1) ? -1.f : 1.f;
#pragma unroll
        for (int r = 1; r < 4; r++) {
            float ang = (float)((i * r) & 7) * 0.125f;
            cr[r] = 2.f * __builtin_amdgcn_cosf(ang); ci[r] = -2.f * __builtin_amdgcn_sinf(ang);
        }
        float sv[8];
#pragma unroll
        for (int e = 0; e < 8; e++) {
            const int d = d0 + (e & 3) + 8 * (e >> 2);
            float s = 0.f;
#pragma unroll
            for (int r = 0; r < 4; r++) { f32x2 c = *(LAS f32x2*)(smem + LDS_CB + (r * 576 + d) * 8); s += cr[r] * c[0] + ci[r] * c[1]; }
            sv[e] = rbf(s);
        }
        float o[8];
#pragma unroll
        for (int e = 0; e < 4; e++) {
            float lo = 0.f, hi = 0.f;
#pragma unroll
            for (int f = 0; f < 8; f++) { lo += bf_lo(pv[f][e]); hi += bf_hi(pv[f][e]); }
            o[2 * e] = lo; o[2 * e + 1] = hi;
        }
        float xv[8] = {bf_lo(xa[0]), bf_hi(xa[0]), bf_lo(xa[1]), bf_hi(xa[1]), bf_lo(xb[0]), bf_hi(xb[0]), bf_lo(xb[1]), bf_hi(xb[1])};
        float bv[8] = {bf_lo(ba[0]), bf_hi(ba[0]), bf_lo(ba[1]), bf_hi(ba[1]), bf_lo(bb[0]), bf_hi(bb[0]), bf_lo(bb[1]), bf_hi(bb[1])};
        float res[8];
#pragma unroll
        for (int e = 0; e < 8; e++) {
            float ov = rbf(rbf(o[e]) + bv[e]);
            float gv = rbf(ov * __builtin_amdgcn_rcpf(1.0f + __builtin_amdgcn_exp2f(-1.4426950408889634f * ov)));
            res[e] = xv[e] + rbf(sv[e] * gv);
        }
        u32x2 oa = {pack_bf16(res[0], res[1]), pack_bf16(res[2], res[3])}, ob = {pack_bf16(res[4], res[5]), pack_bf16(res[6], res[7])};
        *(u32x2*)(out + rowoff + d0) = oa;
        *(u32x2*)(out + rowoff + d0 + 8) = ob;
    }
    STAMP(7);
    waitv<0>();
    STAMP(8);
    if (tid == 0 && (blockIdx.x % 37) == 0) printf("blk %d start %llu: p1 %d s2 %d s8 %d P1end %d tr %d s29 %d s33 %d P2end %d drain %d wait %d epi %d end %d (x10ns)\n", blockIdx.x, tsv[0] % 100000,
        (int)(tsv[1] - tsv[0]), (int)(tsv[9] - tsv[0]), (int)(tsv[10] - tsv[0]), (int)(tsv[2] - tsv[0]), (int)(tsv[3] - tsv[0]), (int)(tsv[11] - tsv[0]), (int)(tsv[12] - tsv[0]),
        (int)(tsv[4] - tsv[0]), (int)(tsv[5] - tsv[0]), (int)(tsv[6] - tsv[0]), (int)(tsv[7] - tsv[0]), (int)(tsv[8] - tsv[0]));
}

extern "C" void kernel_launch(void* const* d_in, const int* in_sizes, int n_in,
                              void* d_out, int out_size, void* d_ws, size_t ws_size,
                              hipStream_t stream) {
    (void)hipFuncSetAttribute((const void*)k_fused, hipFuncAttributeMaxDynamicSharedMemorySize, LDS_TOTAL);
    k_fused<<<256, 512, LDS_TOTAL, stream>>>((const uint16_t*)d_in[0], (const uint16_t*)d_in[1], (const uint16_t*)d_in[2],
                                             (const uint16_t*)d_in[4], (const uint16_t*)d_in[5], (const uint16_t*)d_in[6],
                                             (const uint16_t*)d_in[7], (uint16_t*)d_out, (char*)d_ws
                                             , (u64*)((char*)d_ws + (64u << 20))
                                             );
}
```

```cpp
#include <hip/hip_runtime.h>
#include <stdint.h>
#include <type_traits>

typedef __attribute__((ext_vector_type(8))) __bf16 bf16x8;
typedef __attribute__((ext_vector_type(2))) __bf16 bf16x2;
typedef __attribute__((ext_vector_type(8))) _Float16 f16x8;
typedef __attribute__((ext_vector_type(2))) _Float16 f16x2;
typedef __attribute__((ext_vector_type(4))) short s16x4;
typedef __attribute__((ext_vector_type(2))) float f32x2;
typedef __attribute__((ext_vector_type(4))) float f32x4;
typedef __attribute__((ext_vector_type(16))) float f32x16;
typedef __attribute__((ext_vector_type(4))) unsigned u32x4;
typedef __attribute__((ext_vector_type(2))) unsigned u32x2;
typedef __attribute__((ext_vector_type(2))) unsigned short u16x2;
typedef unsigned long long u64;
#define LAS __attribute__((address_space(3)))
#define GAS __attribute__((address_space(1)))
#define DI __device__ __forceinline__

constexpr int RING_SLOT = 24576;
constexpr int LDS_Y = 3 * RING_SLOT;
constexpr int LDS_H = 0;
constexpr int LDS_CB = 0;
constexpr int LDS_XR = RING_SLOT;
constexpr int LDS_OUT = RING_SLOT + 12288;
constexpr int LDS_TAG = 6 * RING_SLOT;
constexpr int LDS_T256 = LDS_TAG + 16;
constexpr int LDS_WMAX = LDS_T256 + 2048;
constexpr int LDS_BIAS = LDS_WMAX + 256;
constexpr int LDS_XS = LDS_BIAS + 2304;
constexpr int LDS_XA = LDS_BIAS;
constexpr int LDS_TOTAL = LDS_XS + 8 * 1152 + 64;
constexpr unsigned WS_EPOCH = 0;
constexpr unsigned WS_ROWF = 256;
constexpr unsigned WS_COLF = 4096;
constexpr unsigned WS_PARTF = 8192;
constexpr unsigned ZV_PITCH = 580 * 4;
constexpr unsigned WS_ZV = 16384;
constexpr unsigned WS_VP = WS_ZV + 256 * 4 * ZV_PITCH;
constexpr unsigned WS_PART = WS_VP + 8 * 32 * 256 * 80;
constexpr unsigned WS_END = WS_PART + 8u * 2048 * 576 * 2;
constexpr unsigned OOB = 0xF0000000u;

template <int IMM>
DI void glds16(const char* sbase, unsigned voff, unsigned ldsbase) {
    asm volatile("s_add_u32 m0, %2, %3\n\ts_nop 0\n\tglobal_load_lds_dwordx4 %0, %1" ::"v"(voff), "s"(sbase), "s"(ldsbase), "i"(IMM) : "memory", "scc");
}
DI void glds16r(const char* sbase, unsigned voff, unsigned ldsaddr) {
    asm volatile("s_mov_b32 m0, %2\n\ts_nop 0\n\tglobal_load_lds_dwordx4 %0, %1" ::"v"(voff), "s"(sbase), "s"(ldsaddr) : "memory");
}
#define GL1(n) "s_add_u32 m0, m0, 0x400\n\ts_nop 0\n\tglobal_load_lds_dwordx4 %" #n ", %12\n\t"
DI void glds16x12(const char* sbase, const unsigned (&vo)[12], unsigned ldsaddr) {
    asm volatile("s_mov_b32 m0, %13\n\ts_nop 0\n\tglobal_load_lds_dwordx4 %0, %12\n\t"
                 GL1(1) GL1(2) GL1(3) GL1(4) GL1(5) GL1(6) GL1(7) GL1(8) GL1(9) GL1(10) GL1(11)
                 ::"v"(vo[0]), "v"(vo[1]), "v"(vo[2]), "v"(vo[3]), "v"(vo[4]), "v"(vo[5]), "v"(vo[6]), "v"(vo[7]), "v"(vo[8]), "v"(vo[9]), "v"(vo[10]), "v"(vo[11]),
                 "s"(sbase), "s"(ldsaddr) : "memory", "scc");
}
template <int OFF>
DI u32x4 gload16(const char* sbase, unsigned voff) {
    u32x4 r;
    asm volatile("global_load_dwordx4 %0, %1, %2 offset:%3" : "=v"(r) : "v"(voff), "s"(sbase), "i"(OFF) : "memory");
    return r;
}
#define SB0() __builtin_amdgcn_sched_barrier(0)
#define PIN(x) asm volatile("" : "+v"(x))
DI s16x4 tr_rd(LAS char* p) { return __builtin_amdgcn_ds_read_tr16_b64_v4i16((LAS s16x4*)p); }
DI unsigned pack_bf16(float a, float b) { bf16x2 p; p[0] = (__bf16)a; p[1] = (__bf16)b; return __builtin_bit_cast(unsigned, p); }
DI unsigned pack_f16(float a, float b) { f16x2 p; p[0] = (_Float16)a; p[1] = (_Float16)b; return __builtin_bit_cast(unsigned, p); }
DI float bf_lo(unsigned u) { return __uint_as_float(u << 16); }
DI float bf_hi(unsigned u) { return __uint_as_float(u & 0xffff0000u); }
DI float rbf(float f) { return bf_lo(pack_bf16(f, 0.f)); }
#define STAMP(i)
#define STAMPB(i)
#define MFMA32(a, b, c) __builtin_amdgcn_mfma_f32_32x32x16_bf16((a), (b), (c), 0, 0, 0)
#define MFMA32H(a, b, c) __builtin_amdgcn_mfma_f32_32x32x16_f16((a), (b), (c), 0, 0, 0)
#define MFMA16(a, b, c) __builtin_amdgcn_mfma_f32_16x16x32_bf16((a), (b), (c), 0, 0, 0)
template <int N> DI void waitv() { asm volatile("s_waitcnt vmcnt(%0)" ::"i"(N) : "memory"); }
#define WAITL0() asm volatile("s_waitcnt lgkmcnt(0)" ::: "memory")
#define BAR() do { __builtin_amdgcn_s_barrier(); SB0(); } while (0)
#define RLX_AGENT __ATOMIC_RELAXED, __HIP_MEMORY_SCOPE_AGENT
template <int V> using IC = std::integral_constant<int, V>;
template <int A, int B, typename F> DI void static_for(F&& f) {
    if constexpr (A < B) { f(IC<A>{}); static_for<A + 1, B>(f); }
}
DI f16x8 mk8h(unsigned a, unsigned b, unsigned c, unsigned d) { u32x4 v = {a, b, c, d}; return __builtin_bit_cast(f16x8, v); }
DI int rho(int a, int hh) { return (a & 3) + 8 * (a >> 2) + 4 * hh; }
DI float red8(float v) {
    v += __builtin_bit_cast(float, __builtin_amdgcn_update_dpp(0, __builtin_bit_cast(int, v), 0xB1, 0xf, 0xf, false));
    v += __builtin_bit_cast(float, __builtin_amdgcn_update_dpp(0, __builtin_bit_cast(int, v), 0x4E, 0xf, 0xf, false));
    v += __builtin_bit_cast(float, __builtin_amdgcn_update_dpp(0, __builtin_bit_cast(int, v), 0x141, 0xf, 0xf, false));
    return v;
}

DI void tw576(float (&re)[12], float (&im)[12], int lane) {
    const float b0 = (float)(lane & 31) * (1.0f / 576.0f), b1_ = (float)(4 * (lane >> 5)) * b0;
#pragma unroll
    for (int a = 0; a < 12; a++) {
        float an = (float)rho(a, 0) * b0 + b1_;
        float c = __builtin_amdgcn_cosf(an), s = __builtin_amdgcn_sinf(an);
        float r = re[a] * c + im[a] * s;
        float i = im[a] * c - re[a] * s;
        re[a] = r; im[a] = i;
    }
}
#define MFMA16H(a, b, c) __builtin_amdgcn_mfma_f32_16x16x32_f16((a), (b), (c), 0, 0, 0)
DI unsigned rotq(unsigned p) { return __builtin_amdgcn_alignbit(p, p, 16) ^ 0x00008000u; }
struct Dft24h { f16x8 P[3], Q[3]; };
DI void dft24h_setup(Dft24h& m, int lane) {
    const int col = lane & 31, hh = lane >> 5;
    unsigned p[12];
#pragma unroll
    for (int a = 0; a < 12; a++) {
        float an = (float)(rho(a, hh) * col) * (1.0f / 24.0f);
        an = __builtin_amdgcn_fractf(an + 0.01f) - 0.01f;
        p[a] = pack_f16(__builtin_amdgcn_cosf(an), __builtin_amdgcn_sinf(an));
    }
#pragma unroll
    for (int s = 0; s < 3; s++) {
        m.P[s] = mk8h(p[4 * s], p[4 * s + 1], p[4 * s + 2], p[4 * s + 3]);
        m.Q[s] = mk8h(rotq(p[4 * s]), rotq(p[4 * s + 1]), rotq(p[4 * s + 2]), rotq(p[4 * s + 3]));
    }
}
DI void dft24h_stage(const Dft24h& m, const unsigned (&h)[12], float (&re)[12], float (&im)[12]) {
    f32x16 dr = {0, 0, 0, 0, 0, 0, 0, 0, 0, 0, 0, 0, 0, 0, 0, 0}, di = dr;
#pragma unroll
    for (int s = 0; s < 3; s++) {
        f16x8 xh = mk8h(h[4 * s], h[4 * s + 1], h[4 * s + 2], h[4 * s + 3]);
        dr = MFMA32H(xh, m.P[s], dr); di = MFMA32H(xh, m.Q[s], di);
    }
#pragma unroll
    for (int a = 0; a < 12; a++) { re[a] = dr[a]; im[a] = di[a]; }
}
DI u32x4 mat16(const float (&ang)[4]) {
    u32x4 p;
#pragma unroll
    for (int e = 0; e < 4; e++) p[e] = pack_f16(0.25f * __builtin_amdgcn_cosf(ang[e]), 0.25f * __builtin_amdgcn_sinf(ang[e]));
    return p;
}
DI u32x4 rotq4(u32x4 p) { u32x4 q = {rotq(p[0]), rotq(p[1]), rotq(p[2]), rotq(p[3])}; return q; }
DI f16x8 ash(u32x4 v) { return __builtin_bit_cast(f16x8, v); }
template <int NW>
DI void wait_flags(GAS u64* f, u64 tag, int idx) {
    for (unsigned spins = 0; spins < (1u << 20); spins++) {
        bool ok = true;
#pragma unroll
        for (int k = 0; k < NW; k++) ok &= (__hip_atomic_load(f + idx + 64 * k, RLX_AGENT) == tag);
        if (__all(ok)) break;
        __builtin_amdgcn_s_sleep(2);
    }
    __builtin_amdgcn_fence(__ATOMIC_ACQUIRE, "wavefront");
}

__global__ __launch_bounds__(512, 2) void k_fused(const uint16_t* __restrict__ x, const uint16_t* __restrict__ wr, const uint16_t* __restrict__ wi,
                                                  const uint16_t* __restrict__ w1, const uint16_t* __restrict__ b1, const uint16_t* __restrict__ w2,
                                                  const uint16_t* __restrict__ b2, uint16_t* __restrict__ out, char* __restrict__ ws
                                                  ) {
    extern __shared__ __attribute__((aligned(16))) char smem_g[];
    LAS char* smem = (LAS char*)smem_g;
    const int tid = threadIdx.x, lane = tid & 63;
    const int w = __builtin_amdgcn_readfirstlane(tid >> 6);
    const int xg = blockIdx.x & 7, li = blockIdx.x >> 3;
    const int fs = (xg & 3) * 2 + (li & 1), t = (li >> 1) * 2 + (xg >> 2), tl = li >> 1;
    const int jb = 8 * t + fs;
    const unsigned lds0 = (unsigned)(uintptr_t)smem;
    const int rh = w & 1, ct = w >> 1;
    const __amdgpu_buffer_rsrc_t rs = __builtin_amdgcn_make_buffer_rsrc(ws, 0, WS_END, 0x00020000);
    STAMP(0);
    int sbk[5];
#pragma unroll
    for (int i = 0; i < 4; i++) sbk[i] = 2 * ((i + tl) & 3);
    sbk[4] = 8;
    const int rot9 = tl % 9;
    const char* w1b = (const char*)w1 + fs * 1152;
    const char* w2b = (const char*)w2 + (size_t)fs * 576 * 1152;
    const unsigned ldsw = lds0 + w * 1024;
    auto mkvo = [&](bool full, unsigned pitch) {
        int ln = tid; PIN(ln); ln &= 63;
        unsigned r = full ? 4 * w + (ln >> 4) : 8 * w + (ln >> 3);
        unsigned c = full ? ((((ln >> 2) & 3) ^ (ln >> 4)) * 64) + (ln & 3) * 16 : ((((ln >> 2) & 1) ^ ((ln >> 4) & 1)) * 64) + (ln & 3) * 16;
        return r * pitch + c;
    };
    unsigned vo;
    auto gl = [&](auto U_, auto J_) {
        constexpr int U = decltype(U_)::value, J = decltype(J_)::value;
        constexpr int DST = (U % 6) * RING_SLOT + J * 8192;
        if constexpr (U < 3) glds16<DST>(w1b + (unsigned)(8 * 128 + (192 * U + 64 * J) * 9216), vo, ldsw);
        else if constexpr (U < 27) glds16<DST>(w1b + (unsigned)__builtin_amdgcn_readfirstlane(sbk[(U - 3) / 6] * 128 + (96 * ((U - 3) % 6) + 32 * J) * 9216), vo, ldsw);
        else if constexpr (U < 30) glds16<DST>(w2b + (unsigned)__builtin_amdgcn_readfirstlane(rot9 * 128 + (192 * (U - 27) + 64 * J) * 1152), vo, ldsw);
    };
    auto setvo = [&](auto U_) {
        constexpr int U = decltype(U_)::value;
        if constexpr (U == 0) vo = mkvo(false, 9216);
        if constexpr (U == 3) vo = mkvo(true, 9216);
        if constexpr (U == 27) vo = mkvo(false, 1152);
    };
    auto gl3 = [&](auto U_) { setvo(U_); static_for<0, 3>([&](auto J_) { gl(U_, J_); }); };

    Dft24h m24;
    {
        const int i = lane >> 3, c = lane & 7;
        const char* xr = (const char*)x + (size_t)(8 * t + w) * 1152;
        const unsigned xo = (unsigned)i * (256 * 1152) + c * 16;
        u32x4 q[9];
        static_for<0, 9>([&](auto N_) {
            constexpr int n = decltype(N_)::value;
            q[n] = gload16<n * 128>(xr, xo);
        });
        const u32x2 ep = __builtin_amdgcn_raw_buffer_load_b64(rs, tid == 0 ? 0u : OOB, WS_EPOCH, 16);
        const bool dob = tid >= 384 && tid < 384 + 72;
        const u32x4 bb = *(const u32x4*)((const char*)b1 + fs * 1152 + (dob ? tid - 384 : 0) * 16);
        SB0();
        f32x2 tw;
        {
            float an = (float)(((tid >> 4) & 15) * (8 * (tid & 15) + fs)) * (1.0f / 2048.0f);
            tw[0] = __builtin_amdgcn_cosf(an); tw[1] = __builtin_amdgcn_sinf(an);
        }
        if (w >= 4) dft24h_setup(m24, lane); else __builtin_amdgcn_s_sleep(6);
        SB0();
        gl3(IC<0>{}); gl3(IC<1>{}); gl3(IC<2>{});
        SB0();
        asm volatile("s_waitcnt vmcnt(11)" : "+v"(q[0]), "+v"(q[1]), "+v"(q[2]), "+v"(q[3]), "+v"(q[4]), "+v"(q[5]), "+v"(q[6]), "+v"(q[7]), "+v"(q[8]) :: "memory");
        STAMP(13);
        f32x2 d[36];
        f32x2 s2 = {0.f, 0.f};
#pragma unroll
        for (int n = 0; n < 9; n++)
#pragma unroll
            for (int e = 0; e < 4; e++) { f32x2 v = {bf_lo(q[n][e]), bf_hi(q[n][e])}; d[4 * n + e] = v; s2 += v; }
        const float mu = red8(s2[0] + s2[1]) * (1.0f / 576.0f);
        const f32x2 mu2 = {mu, mu};
        f32x2 q2 = {0.f, 0.f};
#pragma unroll
        for (int e = 0; e < 36; e++) { d[e] -= mu2; q2 = __builtin_elementwise_fma(d[e], d[e], q2); }
        const float var = red8(q2[0] + q2[1]) * (1.0f / 576.0f);
        const float m1 = rbf(mu);
        const float is1 = 1.0f / rbf(__builtin_sqrtf(rbf(rbf(var) + 1e-5f)));
        const float mean2 = (mu - m1) * is1;
        const float var2 = var * is1 * is1;
        const float is2 = 1.0f / rbf(__builtin_sqrtf(rbf(rbf(var2) + 1e-5f)));
        const float ya = is1 * is2, yb = (mean2 - rbf(mean2)) * is2;
        const f32x2 ya2 = {ya, ya}, yb2 = {yb, yb};
        const int mloc = 8 * w + i;
        LAS char* yw = smem + LDS_Y + (mloc >> 5) * 36 * 1024 + (c >> 1) * 1024 + (((mloc & 31) ^ c) + 32 * (c & 1)) * 16;
#pragma unroll
        for (int n = 0; n < 9; n++) {
            u32x4 o;
#pragma unroll
            for (int e = 0; e < 4; e++) { f32x2 v = __builtin_elementwise_fma(d[4 * n + e], ya2, yb2); o[e] = pack_bf16(v[0], v[1]); }
            *(LAS u32x4*)(yw + n * 4096) = o;
        }
        if (w == fs) {
            const f32x2 xa = {is1, is1}, xb = {mean2, mean2};
            const int pos = (i & 1) ? 4 + (i >> 1) : ((i >> 2) | (i & 2));
            LAS char* xw = smem + LDS_XS + pos * 1152 + c * 16;
#pragma unroll
            for (int n = 0; n < 9; n++) {
                u32x4 o;
#pragma unroll
                for (int e = 0; e < 4; e++) { f32x2 v = __builtin_elementwise_fma(d[4 * n + e], xa, xb); o[e] = pack_bf16(v[0], v[1]); }
                *(LAS u32x4*)(xw + n * 128) = o;
            }
        }
        if (dob) {
            f32x4 lo = {bf_lo(bb[0]), bf_hi(bb[0]), bf_lo(bb[1]), bf_hi(bb[1])}, hi = {bf_lo(bb[2]), bf_hi(bb[2]), bf_lo(bb[3]), bf_hi(bb[3])};
            *(LAS f32x4*)(smem + LDS_BIAS + (tid - 384) * 32) = lo;
            *(LAS f32x4*)(smem + LDS_BIAS + (tid - 384) * 32 + 16) = hi;
        }
        if (tid == 0) *(LAS u32x2*)(smem + LDS_TAG) = ep;
        if (tid < 256) *(LAS f32x2*)(smem + LDS_T256 + tid * 8) = tw;
        STAMP(14);
        waitv<0>();
        WAITL0();
    }
    BAR();
    STAMP(1);
    u64 epoch;
    {
        u32x2 e2 = *(LAS u32x2*)(smem + LDS_TAG);
        epoch = ((u64)(unsigned)__builtin_amdgcn_readfirstlane(e2[1]) << 32) | (unsigned)__builtin_amdgcn_readfirstlane(e2[0]);
    }
    const u64 tag = (epoch + 1) * 0x9E3779B97F4A7C15ull + 0x632BE59BD9B4E019ull;

    auto put_flag = [&](bool me, unsigned off, u64 val) {
        u32x2 o = {(unsigned)val, (unsigned)(val >> 32)};
        __builtin_amdgcn_raw_buffer_store_b64(o, rs, me ? 0u : OOB, off, 16);
    };
    LAS char* trc;
    auto settr = [&](auto U_) {
        constexpr int U = decltype(U_)::value;
        if constexpr (U == 0 || U == 3 || U == 27) {
            int ln = tid; PIN(ln); ln &= 63;
            int G = ln >> 4, q = (ln >> 2) & 3, p = ln & 3;
            int row = (U < 27 ? 8 : 4) * (G >> 1) + q;
            if constexpr (U == 3) trc = smem + row * 256 + ((ct ^ q) * 64) + (G & 1) * 32 + p * 8 + ((U % 6) / 2) * 2 * RING_SLOT;
            else trc = smem + row * 128 + (((ct & 1) ^ ((q >> 1) & 1)) * 64) + (G & 1) * 32 + p * 8 + ((U % 6) / 2) * 2 * RING_SLOT;
            PIN(trc);
        } else if constexpr (U % 2 == 0) {
            trc += (U % 6 == 0) ? -4 * RING_SLOT : 2 * RING_SLOT;
            PIN(trc);
        }
    };
    bf16x8 af[36];
    auto load_y = [&]() {
        LAS char* ab = smem + LDS_Y + rh * 36 * 1024;
#pragma unroll
        for (int j = 0; j < 36; j++) af[j] = *(LAS bf16x8*)(ab + j * 1024 + (lane ^ (2 * (j & 3) + (lane >> 5))) * 16);
    };
    LAS char* biasp = smem + LDS_BIAS + (32 * ct + 4 * (lane >> 5)) * 4;
    const unsigned pbase = WS_PART + (unsigned)(((fs * 32 + t) * 36 + 2 * ct + rh) * 2048);
    unsigned plane = 0;

    f32x16 acc[2];
    s16x4 bl[6], bh[6];
    float gt[4];
    unsigned hq[5][8];
    auto ldbias = [&](auto P_, auto Q_, int blk) {
        constexpr int P = decltype(P_)::value, Q = decltype(Q_)::value;
        f32x4 b = *(LAS f32x4*)(biasp + blk * 256 + Q * 32);
        acc[P][4 * Q] = b[0]; acc[P][4 * Q + 1] = b[1]; acc[P][4 * Q + 2] = b[2]; acc[P][4 * Q + 3] = b[3];
    };
    auto gop = [&](auto C_, auto K_) {
        constexpr int C = decltype(C_)::value, K = decltype(K_)::value, P = C == 4 ? 1 : (C & 1);
        constexpr int NB = C == 4 ? 1 : C + 2;
        const float c0 = -2.0f * 0.7978845608028654f * 1.4426950408889634f;
        const float c1 = c0 * 0.044715f;
        if constexpr (K < 124) {
            constexpr int Q = K / 31, k = K % 31;
            if constexpr (k < 28) {
                constexpr int st = k / 4, e = k % 4;
                float xx = acc[P][4 * Q + e];
                if constexpr (st == 0) gt[e] = xx * xx;
                else if constexpr (st == 1) gt[e] = __builtin_fmaf(gt[e], c1, c0);
                else if constexpr (st == 2) gt[e] = xx * gt[e];
                else if constexpr (st == 3) gt[e] = __builtin_amdgcn_exp2f(gt[e]);
                else if constexpr (st == 4) gt[e] = 1.0f + gt[e];
                else if constexpr (st == 5) gt[e] = __builtin_amdgcn_rcpf(gt[e]);
                else gt[e] = xx * gt[e];
                PIN(gt[e]);
            } else if constexpr (k == 28) { hq[C][2 * Q] = pack_bf16(gt[0], gt[1]); PIN(hq[C][2 * Q]); }
            else if constexpr (k == 29) { hq[C][2 * Q + 1] = pack_bf16(gt[2], gt[3]); PIN(hq[C][2 * Q + 1]); }
            else { if constexpr (NB <= 3) ldbias(IC<P>{}, IC<Q>{}, sbk[NB]); }
        }
    };
    auto grange = [&](auto C_, auto A_, auto B_) {
        static_for<decltype(A_)::value, decltype(B_)::value>([&](auto K_) { gop(C_, K_); });
    };
    auto pstore = [&](auto P_, auto H_, int blk) {
        constexpr int P = decltype(P_)::value, H = decltype(H_)::value;
        u32x4 o;
        o[0] = pack_bf16(acc[P][8 * H + 0], acc[P][8 * H + 1]); o[1] = pack_bf16(acc[P][8 * H + 2], acc[P][8 * H + 3]);
        o[2] = pack_bf16(acc[P][8 * H + 4], acc[P][8 * H + 5]); o[3] = pack_bf16(acc[P][8 * H + 6], acc[P][8 * H + 7]);
        __builtin_amdgcn_raw_buffer_store_b128(o, rs, plane, pbase + blk * 8192 + H * 1024, 16);
    };
    const f32x16 zero16 = {0, 0, 0, 0, 0, 0, 0, 0, 0, 0, 0, 0, 0, 0, 0, 0};
    auto bfrag = [&](auto J_) {
        constexpr int J = decltype(J_)::value;
        return __builtin_bit_cast(bf16x8, __builtin_shufflevector(bl[J], bh[J], 0, 1, 2, 3, 4, 5, 6, 7));
    };
    auto fslot = [&](auto U_, auto LO_, auto GI_, auto&& fill) {
        constexpr int U = decltype(U_)::value, v = U - 3, c = v / 6, s = v % 6;
        constexpr bool LO = decltype(LO_)::value;
        constexpr int PB = c & 1;
        constexpr int PA = s == 0 ? PB ^ 1 : PB;
        constexpr int sp = (s + 5) % 6;
        settr(U_);
        LAS char* sb = trc + (U % 2) * RING_SLOT;
        setvo(GI_);
        static_for<0, 6>([&](auto G_) {
            constexpr int G = decltype(G_)::value;
            if constexpr (G < 3) {
                if constexpr (LO) acc[PA] = MFMA32(bfrag(IC<G + 3>{}), af[6 * sp + G + 3], acc[PA]);
            } else {
                acc[PB] = MFMA32(bfrag(IC<G - 3>{}), af[6 * s + G - 3], acc[PB]);
            }
            SB0();
            bl[G] = tr_rd(sb + G * 4096); bh[G] = tr_rd(sb + G * 4096 + 1024);
            if constexpr (G < 3) gl(GI_, G_);
            fill(G_);
            SB0();
        });
    };
    auto hslot = [&](auto U_, auto PB_, auto LO_, auto&& fill) {
        constexpr int U = decltype(U_)::value, ph = U / 27, s = U % 3, PB = decltype(PB_)::value;
        constexpr int PA = s == 0 ? PB ^ 1 : PB;
        constexpr int R2 = ph == 0 ? 512 : 1024;
        settr(U_);
        LAS char* sb = trc + (U % 2) * RING_SLOT;
        static_for<0, 12>([&](auto G_) {
            constexpr int G = decltype(G_)::value;
            if constexpr (G < 3) {
                if constexpr (decltype(LO_)::value) acc[PA] = MFMA32(bfrag(IC<G + 3>{}), af[(12 * s + 33) % 36 + G], acc[PA]);
            } else {
                acc[PB] = MFMA32(bfrag(IC<(G - 3) % 6>{}), af[12 * s + G - 3], (ph == 1 && s == 0 && G == 3) ? zero16 : acc[PB]);
            }
            SB0();
            bl[G % 6] = tr_rd(sb + G * 2048); bh[G % 6] = tr_rd(sb + G * 2048 + R2);
            fill(G_);
            SB0();
        });
    };
    auto iss2 = [&](auto GI_, auto GI2_) {
        return [&, GI_, GI2_](auto G_) {
            constexpr int G = decltype(G_)::value;
            if constexpr (G == 0) setvo(GI_);
            if constexpr (G < 6 && G % 2 == 0) gl(GI_, IC<G / 2>{});
            if constexpr (G >= 6 && G % 2 == 0) gl(GI2_, IC<G / 2 - 3>{});
        };
    };
    using T = std::true_type; using F = std::false_type;
    auto nofill = [&](auto) {};

    if (w < 4) {
        load_y();
        static_for<0, 4>([&](auto Q_) { ldbias(IC<1>{}, Q_, sbk[4]); ldbias(IC<0>{}, Q_, sbk[0]); });
        hslot(IC<0>{}, IC<1>{}, F{}, nofill); WAITL0(); BAR();
        hslot(IC<1>{}, IC<1>{}, T{}, iss2(IC<3>{}, IC<4>{})); BAR();
        hslot(IC<2>{}, IC<1>{}, T{}, iss2(IC<5>{}, IC<99>{})); waitv<6>(); BAR();
        STAMP(9);
        static_for<3, 9>([&](auto U_) {
            constexpr int U = decltype(U_)::value, s = U - 3;
            fslot(U_, T{}, IC<U + 3>{}, [&](auto G_) {
                constexpr int gi = 6 * s + decltype(G_)::value - 4;
                if constexpr (gi >= 0) grange(IC<4>{}, IC<(gi * 124) / 32>{}, IC<((gi + 1) * 124) / 32>{});
            });
            waitv<6>(); BAR();
        });
    } else {
        const int v = w - 4;
        const unsigned PP = 0x3F803F80u, PM = 0xBF803F80u  , MP = 0x3F80BF80u  , MM = 0xBF80BF80u;
        unsigned A0, A1, A2, A3, B0, B1, B2, B3; float cc;
        const float c8 = 0.70710678118654752f;
        if (v == 0) { A0 = PP; A1 = PP; A2 = PP; A3 = PP; B0 = PP; B1 = PP; B2 = MM; B3 = MM; cc = 1.f; }
        else if (v == 1) { A0 = PM; A1 = 0; A2 = PM; A3 = MP; B0 = 0; B1 = MP; B2 = MM; B3 = PP; cc = c8; }
        else if (v == 2) { A0 = PP; A1 = MM; A2 = 0; A3 = 0; B0 = 0; B1 = 0; B2 = MP; B3 = MP; cc = 1.f; }
        else { A0 = PM; A1 = 0; A2 = MP; A3 = PM; B0 = 0; B1 = PM; B2 = MM; B3 = PP; cc = c8; }
        float re[12], im[12];
        unsigned hw_[12];
        auto dot = [](unsigned a, unsigned b, float c) { return __builtin_amdgcn_fdot2_f32_bf16(__builtin_bit_cast(bf16x2, a), __builtin_bit_cast(bf16x2, b), c, false); };
        {
            const int G = lane >> 4, q = (lane >> 2) & 3, p = lane & 3;
            LAS char* xb = smem + LDS_XS + q * 1152 + (96 * (G >> 1) + 16 * (G & 1) + 4 * p) * 2;
            u32x2 ev[12], od[12];
#pragma unroll
            for (int a = 0; a < 12; a++) {
                ev[a] = __builtin_bit_cast(u32x2, tr_rd(xb + rho(a, 0) * 48));
                od[a] = __builtin_bit_cast(u32x2, tr_rd(xb + rho(a, 0) * 48 + 4 * 1152));
            }
            SB0();
#pragma unroll
            for (int a = 0; a < 12; a++) {
                float r_ = dot(ev[a][0], A0, dot(ev[a][1], A1, cc * dot(od[a][0], A2, dot(od[a][1], A3, 0.f))));
                float i_ = dot(ev[a][0], B0, dot(ev[a][1], B1, cc * dot(od[a][0], B2, dot(od[a][1], B3, 0.f))));
                hw_[a] = pack_f16(r_, i_);
            }
#pragma unroll
            for (int a = 0; a < 12; a++) PIN(hw_[a]);
            SB0();
        }
        load_y();
        WAITL0(); SB0(); STAMPB(0); BAR();
        dft24h_stage(m24, hw_, re, im);
        gl3(IC<3>{}); gl3(IC<4>{});
        tw576(re, im, lane);
#pragma unroll
        for (int a = 0; a < 12; a++) { hw_[a] = pack_f16(re[a], im[a]); PIN(hw_[a]); }
        SB0(); STAMPB(1); BAR();
        dft24h_stage(m24, hw_, re, im);
        gl3(IC<5>{});
        {
            const int hh = lane >> 5;
            const unsigned zo = (lane & 31) < 24 ? (unsigned)(((lane & 31) * 24 + 4 * hh) * 4) : OOB;
            const unsigned zb = WS_ZV + (unsigned)(jb * 4 + v) * ZV_PITCH;
            unsigned z0 = 0;
#pragma unroll
            for (int qd = 0; qd < 3; qd++) {
                u32x4 o;
#pragma unroll
                for (int e = 0; e < 4; e++) o[e] = pack_f16(re[4 * qd + e] * (1.0f / 64.0f), im[4 * qd + e] * (1.0f / 64.0f));
                if (qd == 0) z0 = o[0];
                __builtin_amdgcn_raw_buffer_store_b128(o, rs, zo, zb + qd * 32, 16);
            }
            __builtin_amdgcn_raw_buffer_store_b32(z0, rs, lane == 0 ? 0u : OOB, zb + 576 * 4, 16);
        }
        static_for<0, 4>([&](auto Q_) { ldbias(IC<0>{}, Q_, sbk[0]); ldbias(IC<1>{}, Q_, sbk[1]); });
        STAMPB(2); waitv<10>(); STAMPB(3); BAR();
        static_for<3, 9>([&](auto U_) {
            constexpr int U = decltype(U_)::value;
            if constexpr (U == 3) fslot(U_, F{}, IC<U + 3>{}, nofill); else fslot(U_, T{}, IC<U + 3>{}, nofill);
            if constexpr (U <= 4) waitv<10>(); else waitv<6>();
            BAR();
            if constexpr (U == 5) __builtin_amdgcn_raw_buffer_store_b32((unsigned)(tag >> 32), rs, tid == 256 ? 0u : OOB, WS_ROWF + jb * 4, 16);
        });
    }
    STAMP(10);
    u32x4 rf;
    static_for<9, 27>([&](auto U_) {
        constexpr int U = decltype(U_)::value, c = (U - 3) / 6, s = (U - 3) % 6;
        fslot(U_, T{}, IC<U + 3>{}, [&](auto G_) {
            constexpr int gi = 6 * s + decltype(G_)::value - 4;
            if constexpr (gi >= 0) grange(IC<c - 1>{}, IC<(gi * 124) / 32>{}, IC<((gi + 1) * 124) / 32>{});
            if constexpr (U == 24 && decltype(G_)::value == 4) {
                int ln = tid; PIN(ln); ln &= 63;
                rf = __builtin_amdgcn_raw_buffer_load_b128(rs, ln * 16, WS_ROWF, 16);
            }
        });
        if constexpr (U < 24) { waitv<6>(); BAR(); }
        else if constexpr (U < 26) { waitv<7>(); BAR(); }
    });
    static_for<0, 3>([&](auto G_) { constexpr int G = decltype(G_)::value; acc[1] = MFMA32(bfrag(IC<G + 3>{}), af[33 + G], acc[1]); });
    STAMP(2);
    WAITL0();
    waitv<0>();
    BAR();
    LAS char* ab;
    {
        int ln = tid; PIN(ln); ln &= 63;
        plane = ln * 16;
        ab = smem + LDS_H + rh * 36 * 1024 + ln * 16;
        LAS char* hw = ab + 2 * ct * 1024;
        auto hst = [&](auto C_) {
            constexpr int C = decltype(C_)::value;
            u32x4 o0 = {hq[C][0], hq[C][1], hq[C][2], hq[C][3]}, o1 = {hq[C][4], hq[C][5], hq[C][6], hq[C][7]};
            *(LAS u32x4*)(hw + sbk[C] * 4096) = o0; *(LAS u32x4*)(hw + sbk[C] * 4096 + 1024) = o1;
        };
        hst(IC<0>{}); hst(IC<1>{}); hst(IC<2>{});
        if (w < 4) hst(IC<4>{});
        grange(IC<3>{}, IC<0>{}, IC<124>{});
        hst(IC<3>{});
        WAITL0();
        STAMPB(5);
        BAR();
    }
    STAMP(3);
    if (w < 4) {
#pragma unroll
        for (int j = 0; j < 36; j++) af[j] = *(LAS bf16x8*)(ab + j * 1024);
        hslot(IC<27>{}, IC<1>{}, F{}, nofill); WAITL0(); BAR();
        hslot(IC<28>{}, IC<1>{}, T{}, nofill); BAR();
        hslot(IC<29>{}, IC<1>{}, T{}, nofill); BAR();
        STAMP(11);
        int cb = rot9;
        for (int it = 0; it < 4; it++) {
            hslot(IC<30>{}, IC<0>{}, T{}, [&](auto G_) {
                if constexpr (decltype(G_)::value == 6) pstore(IC<1>{}, IC<0>{}, cb);
                if constexpr (decltype(G_)::value == 9) pstore(IC<1>{}, IC<1>{}, cb);
            }); BAR();
            hslot(IC<31>{}, IC<0>{}, T{}, nofill); BAR();
            hslot(IC<32>{}, IC<0>{}, T{}, nofill); BAR();
            cb = cb == 8 ? 0 : cb + 1;
            hslot(IC<33>{}, IC<1>{}, T{}, [&](auto G_) {
                if constexpr (decltype(G_)::value == 6) pstore(IC<0>{}, IC<0>{}, cb);
                if constexpr (decltype(G_)::value == 9) pstore(IC<0>{}, IC<1>{}, cb);
            }); BAR();
            hslot(IC<34>{}, IC<1>{}, T{}, nofill); BAR();
            hslot(IC<35>{}, IC<1>{}, T{}, nofill);
            cb = cb == 8 ? 0 : cb + 1;
            if (it < 3) BAR();
        }
        static_for<0, 3>([&](auto G_) { constexpr int G = decltype(G_)::value; acc[1] = MFMA32(bfrag(IC<G + 3>{}), af[33 + G], acc[1]); });
        pstore(IC<1>{}, IC<0>{}, cb);
        pstore(IC<1>{}, IC<1>{}, cb);
        STAMP(4);
        waitv<0>();
        put_flag(lane == 0, WS_PARTF + (t * 32 + fs * 4 + w) * 8, tag);
        STAMP(5);
    } else if (w < 6) {
        int ln = tid; PIN(ln); ln &= 63;
        const unsigned lv = (unsigned)(ln >> 3) * 1152 + ((((ln >> 2) & 1) ^ ((ln >> 4) & 1)) * 64) + (ln & 3) * 16;
        unsigned vv[12];
#pragma unroll
        for (int i = 0; i < 12; i++) vv[i] = lv + i * (8 * 1152);
        const char* src0 = w2b + (w - 4) * (96 * 1152);
        const unsigned dst0 = lds0 + (w - 4) * 12288;
        BAR();
        int cb = rot9 == 8 ? 0 : rot9 + 1, sub = 0, rsl = 0;
        for (int u = 28; u < 52; u++) {
            glds16x12(src0 + (unsigned)(cb * 128 + sub * (192 * 1152)), vv, dst0 + rsl * RING_SLOT);
            sub++; if (sub == 3) { sub = 0; cb = cb == 8 ? 0 : cb + 1; }
            rsl = rsl == 5 ? 0 : rsl + 1;
            waitv<12>();
            BAR();
        }
        waitv<0>();
        BAR();
        {
            const unsigned xo = (unsigned)ln * 16;
#pragma unroll
            for (int n = 0; n < 5; n++) {
                const int pcs = (w - 4) * 5 + n;
                const int un = pcs * 64 + ln;
                if (pcs < 9) {
                    const int row = un / 72, cc = un % 72;
                    glds16r((const char*)x, (unsigned)(jb + 256 * row) * 1152 + cc * 16, lds0 + LDS_XR + pcs * 1024);
                } else {
                    glds16r((const char*)b2, xo, lds0 + LDS_XR + 9216);
                    glds16r((const char*)b2, 128 + xo, lds0 + LDS_XR + 9216 + 128);
                }
            }
            waitv<0>();
        }
    } else {
        int ticks = 0;
        auto tick = [&]() { SB0(); if (ticks < 26) { __builtin_amdgcn_s_barrier(); ticks++; } SB0(); };
        int ln = tid; PIN(ln); ln &= 63;
        const int r = fs, col = ln & 15, g = ln >> 4, w2_ = w - 6;
        const f32x4 z4 = {0.f, 0.f, 0.f, 0.f};
        const unsigned tg = (unsigned)(tag >> 32);
        for (unsigned spins = 0; spins < (1u << 20); spins++) {
            if (__all(rf[0] == tg && rf[1] == tg && rf[2] == tg && rf[3] == tg)) break;
            __builtin_amdgcn_s_sleep(2);
            rf = __builtin_amdgcn_raw_buffer_load_b128(rs, ln * 16, WS_ROWF, 16);
        }
        __builtin_amdgcn_fence(__ATOMIC_ACQUIRE, "wavefront");
        put_flag(jb == 0 && tid == 384, WS_EPOCH, epoch + 1);
        {
            u32x4 za[8], zb[8];
            unsigned short wa[8][4], wb[8][4];
            const int vA = r <= 3 ? r : 0, vB = r >= 5 ? 8 - r : 0;
            const bool useA = r <= 4 && col < 10, useB = (r == 0 || r >= 4) && col < 10;
            const unsigned lo = (unsigned)(64 * g) * 4 * ZV_PITCH;
            const unsigned loA = useA ? lo + col * 4 : OOB, loB = useB ? lo + (15 - col) * 4 : OOB;
            const unsigned sA = WS_ZV + vA * ZV_PITCH + 9 * t * 4 + w2_ * 32 * ZV_PITCH, sB = WS_ZV + vB * ZV_PITCH + (561 - 9 * t) * 4 + w2_ * 32 * ZV_PITCH;
#pragma unroll
            for (int tl_ = 0; tl_ < 8; tl_++) {
#pragma unroll
                for (int e = 0; e < 4; e++) {
                    za[tl_][e] = __builtin_amdgcn_raw_buffer_load_b32(rs, loA, sA + (16 * e + tl_) * 4 * ZV_PITCH, 16);
                    zb[tl_][e] = __builtin_amdgcn_raw_buffer_load_b32(rs, loB, sB + (16 * e + tl_) * 4 * ZV_PITCH, 16);
                }
                if (tl_ == 3 || tl_ == 7) tick();
            }
            const unsigned wo = (unsigned)((8 * (8 * w2_ + 64 * g) + r) * 289 + 9 * t + (col < 10 ? col : 9));
#pragma unroll
            for (int tl_ = 0; tl_ < 8; tl_++) {
#pragma unroll
                for (int e = 0; e < 4; e++) { wa[tl_][e] = wr[wo + (8 * tl_ + 128 * e) * 289]; wb[tl_][e] = wi[wo + (8 * tl_ + 128 * e) * 289]; }
                if (tl_ == 3 || tl_ == 7) tick();
            }
            STAMPB(4);
            LAS char* xw = smem + LDS_XA + ((4 * g * 4 + 2 * w2_) * 10 + (col < 10 ? col : 9)) * 16;
            LAS char* xr = smem + LDS_XA + ((8 * w2_ * 4 + g) * 10 + (col < 10 ? col : 9)) * 16;
            f32x4 dr[8], di[8];
            {
                float an[4];
#pragma unroll
                for (int e = 0; e < 4; e++) an[e] = (float)(((4 * g + e) * (8 * col + r)) & 127) * (1.0f / 128.0f) + (r == 4 ? 0.25f : 0.f);
                const u32x4 P1 = mat16(an), Q1 = rotq4(P1);
                const unsigned fl = r == 4 ? 0x00008000u : 0x80000000u;
                const u32x4 P2 = P1 ^ fl, Q2 = Q1 ^ fl;
                tick();
#pragma unroll
                for (int tl_ = 0; tl_ < 8; tl_++) {
                    dr[tl_] = MFMA16H(ash(P1), ash(za[tl_]), z4); di[tl_] = MFMA16H(ash(Q1), ash(za[tl_]), z4);
                    dr[tl_] = MFMA16H(ash(P2), ash(zb[tl_]), dr[tl_]); di[tl_] = MFMA16H(ash(Q2), ash(zb[tl_]), di[tl_]);
                    if (tl_ == 3 || tl_ == 7) tick();
                }
#pragma unroll
                for (int hf = 0; hf < 2; hf++) {
                    u32x4 o[4];
#pragma unroll
                    for (int tq = 0; tq < 4; tq++) {
                        const int tl_ = 4 * hf + tq;
                        LAS char* tp = smem + LDS_T256 + ((8 * w2_ + tl_) * 16 + 4 * g) * 8;
                        const f32x4 t0 = *(LAS f32x4*)tp, t1 = *(LAS f32x4*)(tp + 16);
#pragma unroll
                        for (int e = 0; e < 4; e++) {
                            const float c = e < 2 ? t0[2 * e] : t1[2 * e - 4], sn = e < 2 ? t0[2 * e + 1] : t1[2 * e - 3];
                            o[e][tq] = pack_f16(dr[tl_][e] * c + di[tl_][e] * sn, di[tl_][e] * c - dr[tl_][e] * sn);
                        }
                    }
                    if (col < 10) {
#pragma unroll
                        for (int e = 0; e < 4; e++) *(LAS u32x4*)(xw + e * 640 + hf * 160) = o[e];
                    }
                    tick();
                }
            }
            STAMPB(6);
            unsigned wp[8][4];
            {
                u16x2 mx = {0, 0};
#pragma unroll
                for (int tl_ = 0; tl_ < 8; tl_++)
#pragma unroll
                    for (int e = 0; e < 4; e++) {
                        wp[tl_][e] = (unsigned)wa[tl_][e] | ((unsigned)wb[tl_][e] << 16);
                        mx = __builtin_elementwise_max(mx, __builtin_bit_cast(u16x2, wp[tl_][e] & 0x7fff7fffu));
                    }
                unsigned m1 = mx[0] > mx[1] ? mx[0] : mx[1];
                { auto q = __builtin_amdgcn_permlane16_swap(m1, m1, false, false); m1 = q[0] > q[1] ? q[0] : q[1]; }
                { auto q = __builtin_amdgcn_permlane32_swap(m1, m1, false, false); m1 = q[0] > q[1] ? q[0] : q[1]; }
                *(LAS unsigned*)(smem + LDS_WMAX + col * 8 + w2_ * 4) = m1;
                WAITL0();
                tick();
            }
            float inv;
            {
                float an[4];
#pragma unroll
                for (int e = 0; e < 4; e++) an[e] = (float)(((4 * g + e) * col) & 15) * (1.0f / 16.0f);
                const u32x4 P3 = mat16(an), Q3 = rotq4(P3);
                const u32x2 m2 = *(LAS u32x2*)(smem + LDS_WMAX + col * 8);
                unsigned ex = max(m2[0], m2[1]) >> 7;
                ex = min(max(ex, 1u), 252u);
                const float wsc = __uint_as_float((253u - ex) << 23);
                inv = __uint_as_float((ex + 1u) << 23);
#pragma unroll
                for (int tl_ = 0; tl_ < 8; tl_++) {
                    const u32x4 xa = *(LAS u32x4*)(xr + tl_ * 640);
                    dr[tl_] = MFMA16H(ash(P3), ash(xa), z4); di[tl_] = MFMA16H(ash(Q3), ash(xa), z4);
                }
                WAITL0();
                tick();
#pragma unroll
                for (int tl_ = 0; tl_ < 8; tl_++) {
                    u32x4 y;
#pragma unroll
                    for (int e = 0; e < 4; e++) {
                        const float wre = bf_lo(wp[tl_][e]) * wsc, wim = bf_hi(wp[tl_][e]) * wsc;
                        y[e] = pack_f16(dr[tl_][e] * wre - di[tl_][e] * wim, -(dr[tl_][e] * wim + di[tl_][e] * wre));
                    }
                    dr[tl_] = MFMA16H(ash(P3), ash(y), z4); di[tl_] = MFMA16H(ash(Q3), ash(y), z4);
                    if (tl_ & 1) tick();
                }
#pragma unroll
                for (int e = 0; e < 4; e++) {
#pragma unroll
                    for (int hf = 0; hf < 2; hf++) {
                        LAS char* tp = smem + LDS_T256 + ((4 * g + e) * 16 + 8 * w2_ + 4 * hf) * 8;
                        const f32x4 t0 = *(LAS f32x4*)tp, t1 = *(LAS f32x4*)(tp + 16);
                        u32x4 o;
#pragma unroll
                        for (int tq = 0; tq < 4; tq++) {
                            const int tl_ = 4 * hf + tq;
                            const float c = tq < 2 ? t0[2 * tq] : t1[2 * tq - 4], sn = tq < 2 ? t0[2 * tq + 1] : t1[2 * tq - 3];
                            o[tq] = pack_f16(dr[tl_][e] * c + di[tl_][e] * sn, di[tl_][e] * c - dr[tl_][e] * sn);
                        }
                        if (col < 10) *(LAS u32x4*)(xw + e * 640 + hf * 160) = o;
                    }
                    if (e == 1) tick();
                }
                WAITL0();
                tick();
            }
            STAMPB(9);
            {
                float an[4];
#pragma unroll
                for (int e = 0; e < 4; e++) an[e] = (float)((col * (8 * (4 * g + e) + r)) & 127) * (1.0f / 128.0f);
                const u32x4 P4 = mat16(an), Q4 = rotq4(P4);
                const float fsc = (64.0f * 256.0f / (2048.0f * 576.0f)) * ((r & 3) == 0 ? 0.5f : 1.0f);
                const unsigned vo_ = col < 10 ? (unsigned)(64 * g * 80 + col * 8) : OOB;
                const unsigned vb = WS_VP + (unsigned)(r * 32 + t) * (256 * 80) + w2_ * 8 * 80;
#pragma unroll
                for (int tl_ = 0; tl_ < 8; tl_++) {
                    const u32x4 xa = *(LAS u32x4*)(xr + tl_ * 640);
                    dr[tl_] = MFMA16H(ash(P4), ash(xa), z4); di[tl_] = MFMA16H(ash(Q4), ash(xa), z4);
                }
                tick();
#pragma unroll
                for (int tl_ = 0; tl_ < 8; tl_++) {
#pragma unroll
                    for (int e = 0; e < 4; e++) {
                        u32x2 o = {__float_as_uint(dr[tl_][e] * fsc * inv), __float_as_uint(di[tl_][e] * fsc * inv)};
                        __builtin_amdgcn_raw_buffer_store_b64(o, rs, vo_, vb + (16 * e + tl_) * 80, 16);
                    }
                    if (tl_ == 3 || tl_ == 7) tick();
                }
            }
        }
        STAMPB(10);
        Dft24h mi; dft24h_setup(mi, ln);
        tick();
        waitv<0>();
        put_flag(ln == 0, WS_COLF + ((fs * 32 + t) * 2 + w2_) * 8, tag);
        tick();
        const int hh = ln >> 5;
        int n2 = ln & 31; if (n2 > 23) n2 = 23;
        for (unsigned spins = 0; spins < (1u << 20); spins++) {
            bool ok = true;
#pragma unroll
            for (int k = 0; k < 4; k++) {
                const u32x4 f = __builtin_amdgcn_raw_buffer_load_b128(rs, ln * 16, WS_COLF + k * 1024, 16);
                ok &= (f[0] == (unsigned)tag && f[1] == tg && f[2] == (unsigned)tag && f[3] == tg);
            }
            if (__all(ok)) break;
            tick();
        }
        __builtin_amdgcn_fence(__ATOMIC_ACQUIRE, "wavefront");
        STAMPB(11);
        u32x2 A[2][12], B[2][12];
#pragma unroll
        for (int it = 0; it < 2; it++) {
            const int v = w2_ + 2 * it, rB = v == 0 ? 4 : 8 - v;
#pragma unroll
            for (int a = 0; a < 12; a++) {
                const int k = 24 * rho(a, hh) + n2;
                const bool mir = k > 288; const int kk = mir ? 576 - k : k;
                int gq = (kk * 57) >> 9; if (gq > 31) gq = 31;
                const unsigned off = (unsigned)gq * (256 * 80) + (unsigned)(kk - 9 * gq) * 8;
                const unsigned rsel = (v != 0 && mir) ? rB : v;
                A[it][a] = __builtin_amdgcn_raw_buffer_load_b64(rs, off + rsel * (32 * 256 * 80), WS_VP + jb * 80, 16);
                const bool needB = v == 0 || k == 0 || k == 288;
                if (it == 0 || a == 0 || a == 4) B[it][a] = __builtin_amdgcn_raw_buffer_load_b64(rs, needB ? off + rB * (32 * 256 * 80) : OOB, WS_VP + jb * 80, 16);
                else B[it][a] = u32x2{0u, 0u};
            }
            tick();
        }
        tick(); tick();
        f32x4 co[2][6];
#pragma unroll
        for (int it = 0; it < 2; it++) {
            const int v = w2_ + 2 * it;
            float re[12], im[12];
            float mx = 0.f;
#pragma unroll
            for (int a = 0; a < 12; a++) {
                const int k = 24 * rho(a, hh) + n2;
                const bool mir = k > 288, spec = (k == 0 || k == 288);
                float ax = __uint_as_float(A[it][a][0]), ay = __uint_as_float(A[it][a][1]), bx = __uint_as_float(B[it][a][0]), by = __uint_as_float(B[it][a][1]);
                if (mir) { ay = -ay; by = -by; }
                if (v == 0) {
                    if (spec) { ay = 0.f; by = 0.f; }
                    re[a] = ax + by; im[a] = ay - bx;
                } else {
                    re[a] = spec ? 0.5f * (ax + bx) : ax; im[a] = spec ? 0.5f * (ay - by) : ay;
                }
                mx = fmaxf(mx, fmaxf(fabsf(re[a]), fabsf(im[a])));
            }
            unsigned mu_ = __float_as_uint(mx);
            mu_ = max(mu_, (unsigned)__builtin_amdgcn_update_dpp(0, (int)mu_, 0xB1, 0xf, 0xf, false));
            mu_ = max(mu_, (unsigned)__builtin_amdgcn_update_dpp(0, (int)mu_, 0x4E, 0xf, 0xf, false));
            mu_ = max(mu_, (unsigned)__builtin_amdgcn_update_dpp(0, (int)mu_, 0x141, 0xf, 0xf, false));
            mu_ = max(mu_, (unsigned)__builtin_amdgcn_update_dpp(0, (int)mu_, 0x128, 0xf, 0xf, false));
            { auto q = __builtin_amdgcn_permlane16_swap(mu_, mu_, false, false); mu_ = max(q[0], q[1]); }
            { auto q = __builtin_amdgcn_permlane32_swap(mu_, mu_, false, false); mu_ = max(q[0], q[1]); }
            unsigned ex = min(max(mu_ >> 23, 10u), 250u);
            const float sc = __uint_as_float((262u - ex) << 23), isc = __uint_as_float((ex - 8u) << 23);
            unsigned hw_[12];
#pragma unroll
            for (int a = 0; a < 12; a++) hw_[a] = pack_f16(re[a] * sc, im[a] * sc);
            tick();
            dft24h_stage(mi, hw_, re, im);
            tw576(re, im, ln);
#pragma unroll
            for (int a = 0; a < 12; a++) hw_[a] = pack_f16(re[a], im[a]);
            tick();
            dft24h_stage(mi, hw_, re, im);
#pragma unroll
            for (int a = 0; a < 12; a += 2) { f32x4 o = {re[a] * isc, -im[a] * isc, re[a + 1] * isc, -im[a + 1] * isc}; co[it][a / 2] = o; }
            tick();
        }
        STAMPB(12);
        while (ticks < 26) tick();
        if ((ln & 31) < 24) {
#pragma unroll
            for (int it = 0; it < 2; it++)
#pragma unroll
                for (int a = 0; a < 12; a += 2) *(LAS f32x4*)(smem + LDS_CB + ((w2_ + 2 * it) * 576 + 24 * (ln & 31) + rho(a, hh)) * 8) = co[it][a / 2];
        }
        WAITL0();
        STAMPB(13);
    }
    BAR();
    STAMP(6);
    {
    int ln = tid; PIN(ln);
    float sv[2][8], xv[2][8], bv[2][8];
#pragma unroll
    for (int rep = 0; rep < 2; rep++) {
        if (rep == 1 && w != 5) break;
        const int pid = rep == 0 ? ln : 512 + (ln & 63);
        const int i = pid & 7, pc = pid >> 3, ehh = pc & 1, H = (pc >> 1) & 1, ctg = pc >> 2;
        const int d0 = 32 * ctg + 16 * H + 4 * ehh;
        float cr[4], ci[4];
        cr[0] = 1.f; ci[0] = (i & 1) ? -1.f : 1.f;
#pragma unroll
        for (int r = 1; r < 4; r++) {
            float ang = (float)((i * r) & 7) * 0.125f;
            cr[r] = 2.f * __builtin_amdgcn_cosf(ang); ci[r] = -2.f * __builtin_amdgcn_sinf(ang);
        }
#pragma unroll
        for (int h2 = 0; h2 < 2; h2++) {
            float s4[4] = {0.f, 0.f, 0.f, 0.f};
#pragma unroll
            for (int r = 0; r < 4; r++) {
                const f32x4 c0 = *(LAS f32x4*)(smem + LDS_CB + (r * 576 + d0 + 8 * h2) * 8), c1 = *(LAS f32x4*)(smem + LDS_CB + (r * 576 + d0 + 8 * h2) * 8 + 16);
                s4[0] += cr[r] * c0[0] + ci[r] * c0[1]; s4[1] += cr[r] * c0[2] + ci[r] * c0[3];
                s4[2] += cr[r] * c1[0] + ci[r] * c1[1]; s4[3] += cr[r] * c1[2] + ci[r] * c1[3];
            }
            const u32x2 xq = *(LAS u32x2*)(smem + LDS_XR + i * 1152 + (d0 + 8 * h2) * 2), bq = *(LAS u32x2*)(smem + LDS_XR + 9216 + (d0 + 8 * h2) * 2);
#pragma unroll
            for (int e = 0; e < 4; e++) {
                sv[rep][4 * h2 + e] = rbf(s4[e]);
                xv[rep][4 * h2 + e] = (e & 1) ? bf_hi(xq[e >> 1]) : bf_lo(xq[e >> 1]);
                bv[rep][4 * h2 + e] = (e & 1) ? bf_hi(bq[e >> 1]) : bf_lo(bq[e >> 1]);
                PIN(sv[rep][4 * h2 + e]);
            }
        }
    }
    if (w == 4) {
        wait_flags<1>((GAS u64*)(ws + WS_PARTF) + t * 32, tag, ln & 31);
    }
    SB0(); BAR();
    STAMP(15);
#pragma unroll
    for (int rep = 0; rep < 2; rep++) {
        if (rep == 1 && w != 5) break;
        const int pid = rep == 0 ? ln : 512 + (ln & 63);
        const int i = pid & 7, pc = pid >> 3, ehh = pc & 1, H = (pc >> 1) & 1, ctg = pc >> 2;
        const int mloc = 8 * fs + i;
        u32x4 pv[8];
        const unsigned po = (unsigned)((ctg * 2 + (mloc >> 5)) * 2 + H) * 1024 + ((mloc & 31) + 32 * ehh) * 16;
#pragma unroll
        for (int f = 0; f < 8; f++) pv[f] = __builtin_amdgcn_raw_buffer_load_b128(rs, po, WS_PART + (unsigned)(f * 32 + t) * 73728, 16);
        const int d0 = 32 * ctg + 16 * H + 4 * ehh;
        float res[8];
#pragma unroll
        for (int e = 0; e < 8; e++) {
            float o = 0.f;
#pragma unroll
            for (int f = 0; f < 8; f++) o += (e & 1) ? bf_hi(pv[f][e >> 1]) : bf_lo(pv[f][e >> 1]);
            float ov = rbf(rbf(o) + bv[rep][e]);
            float gv = rbf(ov * __builtin_amdgcn_rcpf(1.0f + __builtin_amdgcn_exp2f(-1.4426950408889634f * ov)));
            res[e] = xv[rep][e] + rbf(sv[rep][e] * gv);
        }
        u32x2 oa = {pack_bf16(res[0], res[1]), pack_bf16(res[2], res[3])}, ob = {pack_bf16(res[4], res[5]), pack_bf16(res[6], res[7])};
        *(LAS u32x2*)(smem + LDS_OUT + i * 1152 + d0 * 2) = oa;
        *(LAS u32x2*)(smem + LDS_OUT + i * 1152 + d0 * 2 + 16) = ob;
    }
    WAITL0();
    BAR();
#pragma unroll
    for (int rep = 0; rep < 2; rep++) {
        if (rep == 1 && w != 0) break;
        const int un = ln + 512 * rep;
        const int row = un / 72, cc = un % 72;
        *(u32x4*)((char*)out + (size_t)(jb + 256 * row) * 1152 + cc * 16) = *(LAS u32x4*)(smem + LDS_OUT + un * 16);
    }
    }
}

extern "C" void kernel_launch(void* const* d_in, const int* in_sizes, int n_in,
                              void* d_out, int out_size, void* d_ws, size_t ws_size,
                              hipStream_t stream) {
    (void)hipFuncSetAttribute((const void*)k_fused, hipFuncAttributeMaxDynamicSharedMemorySize, LDS_TOTAL);
    k_fused<<<256, 512, LDS_TOTAL, stream>>>((const uint16_t*)d_in[0], (const uint16_t*)d_in[1], (const uint16_t*)d_in[2],
                                             (const uint16_t*)d_in[4], (const uint16_t*)d_in[5], (const uint16_t*)d_in[6],
                                             (const uint16_t*)d_in[7], (uint16_t*)d_out, (char*)d_ws
                                             );
}
```

```cpp
#include <hip/hip_runtime.h>
#include <stdint.h>
#include <type_traits>

typedef __attribute__((ext_vector_type(8))) __bf16 bf16x8;
typedef __attribute__((ext_vector_type(2))) __bf16 bf16x2;
typedef __attribute__((ext_vector_type(8))) _Float16 f16x8;
typedef __attribute__((ext_vector_type(2))) _Float16 f16x2;
typedef __attribute__((ext_vector_type(4))) short s16x4;
typedef __attribute__((ext_vector_type(2))) float f32x2;
typedef __attribute__((ext_vector_type(4))) float f32x4;
typedef __attribute__((ext_vector_type(16))) float f32x16;
typedef __attribute__((ext_vector_type(4))) unsigned u32x4;
typedef __attribute__((ext_vector_type(2))) unsigned u32x2;
typedef __attribute__((ext_vector_type(2))) unsigned short u16x2;
typedef unsigned long long u64;
#define LAS __attribute__((address_space(3)))
#define GAS __attribute__((address_space(1)))
#define DI __device__ __forceinline__

constexpr int RING_SLOT = 24576;
constexpr int LDS_Y = 3 * RING_SLOT;
constexpr int LDS_H = 0;
constexpr int LDS_CB = 0;
constexpr int LDS_XR = RING_SLOT;
constexpr int LDS_OUT = RING_SLOT + 12288;
constexpr int LDS_ZS = 4 * RING_SLOT;
constexpr int LDS_WS = 5 * RING_SLOT;
constexpr int LDS_VS = 2 * RING_SLOT;
constexpr int LDS_TAG = 6 * RING_SLOT;
constexpr int LDS_T256 = LDS_TAG + 16;
constexpr int LDS_WMAX = LDS_T256 + 1024;
constexpr int LDS_BIAS = LDS_WMAX + 512;
constexpr int LDS_XS = LDS_BIAS + 2304;
constexpr int LDS_XA = LDS_BIAS;
constexpr int LDS_TOTAL = LDS_XS + 8 * 1152 + 64;
constexpr unsigned WS_EPOCH = 0;
constexpr unsigned WS_ROWF = 256;
constexpr unsigned WS_COLF = 2048;
constexpr unsigned WS_PARTF = 4096;
constexpr unsigned ZV_PITCH = 580 * 4;
constexpr unsigned WS_ZV = 4096 + 16384;
constexpr unsigned WS_VP = WS_ZV + 256 * 4 * ZV_PITCH;
constexpr unsigned WS_PART = WS_VP + 8 * 32 * 256 * 80;
constexpr unsigned WS_END = WS_PART + 8u * 2048 * 576 * 2;
constexpr unsigned OOB = 0xF0000000u;

template <int IMM>
DI void glds16(const char* sbase, unsigned voff, unsigned ldsbase) {
    asm volatile("s_add_u32 m0, %2, %3\n\ts_nop 0\n\tglobal_load_lds_dwordx4 %0, %1" ::"v"(voff), "s"(sbase), "s"(ldsbase), "i"(IMM) : "memory", "scc");
}
DI void glds16r(const char* sbase, unsigned voff, unsigned ldsaddr) {
    asm volatile("s_mov_b32 m0, %2\n\ts_nop 0\n\tglobal_load_lds_dwordx4 %0, %1" ::"v"(voff), "s"(sbase), "s"(ldsaddr) : "memory");
}
DI void glds16s(const char* sbase, unsigned voff, unsigned ldsaddr) {
    asm volatile("s_mov_b32 m0, %2\n\ts_nop 0\n\tglobal_load_lds_dwordx4 %0, %1 sc1" ::"v"(voff), "s"(sbase), "s"(ldsaddr) : "memory");
}
template <int OFF>
DI u32x4 gload16(const char* sbase, unsigned voff) {
    u32x4 r;
    asm volatile("global_load_dwordx4 %0, %1, %2 offset:%3" : "=v"(r) : "v"(voff), "s"(sbase), "i"(OFF) : "memory");
    return r;
}
#define SB0() __builtin_amdgcn_sched_barrier(0)
#define PIN(x) asm volatile("" : "+v"(x))
DI s16x4 tr_rd(LAS char* p) { return __builtin_amdgcn_ds_read_tr16_b64_v4i16((LAS s16x4*)p); }
DI unsigned pack_bf16(float a, float b) { bf16x2 p; p[0] = (__bf16)a; p[1] = (__bf16)b; return __builtin_bit_cast(unsigned, p); }
DI unsigned pack_f16(float a, float b) { f16x2 p; p[0] = (_Float16)a; p[1] = (_Float16)b; return __builtin_bit_cast(unsigned, p); }
DI float bf_lo(unsigned u) { return __uint_as_float(u << 16); }
DI float bf_hi(unsigned u) { return __uint_as_float(u & 0xffff0000u); }
DI float rbf(float f) { return bf_lo(pack_bf16(f, 0.f)); }
#define STAMP(i)
#define STAMPB(i)
#define MFMA32(a, b, c) __builtin_amdgcn_mfma_f32_32x32x16_bf16((a), (b), (c), 0, 0, 0)
#define MFMA32H(a, b, c) __builtin_amdgcn_mfma_f32_32x32x16_f16((a), (b), (c), 0, 0, 0)
#define MFMA16(a, b, c) __builtin_amdgcn_mfma_f32_16x16x32_bf16((a), (b), (c), 0, 0, 0)
template <int N> DI void waitv() { asm volatile("s_waitcnt vmcnt(%0)" ::"i"(N) : "memory"); }
#define WAITL0() asm volatile("s_waitcnt lgkmcnt(0)" ::: "memory")
#define BAR() do { __builtin_amdgcn_s_barrier(); SB0(); } while (0)
#define RLX_AGENT __ATOMIC_RELAXED, __HIP_MEMORY_SCOPE_AGENT
template <int V> using IC = std::integral_constant<int, V>;
template <int A, int B, typename F> DI void static_for(F&& f) {
    if constexpr (A < B) { f(IC<A>{}); static_for<A + 1, B>(f); }
}
DI f16x8 mk8h(unsigned a, unsigned b, unsigned c, unsigned d) { u32x4 v = {a, b, c, d}; return __builtin_bit_cast(f16x8, v); }
DI int rho(int a, int hh) { return (a & 3) + 8 * (a >> 2) + 4 * hh; }
DI float red8(float v) {
    v += __builtin_bit_cast(float, __builtin_amdgcn_update_dpp(0, __builtin_bit_cast(int, v), 0xB1, 0xf, 0xf, false));
    v += __builtin_bit_cast(float, __builtin_amdgcn_update_dpp(0, __builtin_bit_cast(int, v), 0x4E, 0xf, 0xf, false));
    v += __builtin_bit_cast(float, __builtin_amdgcn_update_dpp(0, __builtin_bit_cast(int, v), 0x141, 0xf, 0xf, false));
    return v;
}

DI unsigned cmulh(unsigned xu, unsigned tu) {
    f16x2 x = __builtin_bit_cast(f16x2, xu), tw = __builtin_bit_cast(f16x2, tu);
    f16x2 cc = {tw[0], tw[0]}, ss = {tw[1], -tw[1]}, xs = {x[1], x[0]};
    return __builtin_bit_cast(unsigned, __builtin_elementwise_fma(xs, ss, x * cc));
}
#define MFMA16H(a, b, c) __builtin_amdgcn_mfma_f32_16x16x32_f16((a), (b), (c), 0, 0, 0)
DI unsigned rotq(unsigned p) { return __builtin_amdgcn_alignbit(p, p, 16) ^ 0x00008000u; }
struct Dft24h { f16x8 P[3], Q[3]; unsigned tw[12]; };
DI void dft24h_setup(Dft24h& m, int lane) {
    const int col = lane & 31, hh = lane >> 5;
    unsigned p[12];
#pragma unroll
    for (int a = 0; a < 12; a++) {
        float an = (float)(rho(a, hh) * col) * (1.0f / 24.0f);
        an = __builtin_amdgcn_fractf(an + 0.01f) - 0.01f;
        p[a] = pack_f16(__builtin_amdgcn_cosf(an), __builtin_amdgcn_sinf(an));
        const float a2 = (float)(rho(a, hh) * col) * (1.0f / 576.0f);
        m.tw[a] = pack_f16(__builtin_amdgcn_cosf(a2), __builtin_amdgcn_sinf(a2));
    }
#pragma unroll
    for (int s = 0; s < 3; s++) {
        m.P[s] = mk8h(p[4 * s], p[4 * s + 1], p[4 * s + 2], p[4 * s + 3]);
        m.Q[s] = mk8h(rotq(p[4 * s]), rotq(p[4 * s + 1]), rotq(p[4 * s + 2]), rotq(p[4 * s + 3]));
    }
}
DI void dft24h_stage(const Dft24h& m, const unsigned (&h)[12], float (&re)[12], float (&im)[12]) {
    f32x16 dr = {0, 0, 0, 0, 0, 0, 0, 0, 0, 0, 0, 0, 0, 0, 0, 0}, di = dr;
#pragma unroll
    for (int s = 0; s < 3; s++) {
        f16x8 xh = mk8h(h[4 * s], h[4 * s + 1], h[4 * s + 2], h[4 * s + 3]);
        dr = MFMA32H(xh, m.P[s], dr); di = MFMA32H(xh, m.Q[s], di);
    }
#pragma unroll
    for (int a = 0; a < 12; a++) { re[a] = dr[a]; im[a] = di[a]; }
}
DI u32x4 mat16(const float (&ang)[4]) {
    u32x4 p;
#pragma unroll
    for (int e = 0; e < 4; e++) p[e] = pack_f16(0.25f * __builtin_amdgcn_cosf(ang[e]), 0.25f * __builtin_amdgcn_sinf(ang[e]));
    return p;
}
DI u32x4 rotq4(u32x4 p) { u32x4 q = {rotq(p[0]), rotq(p[1]), rotq(p[2]), rotq(p[3])}; return q; }
DI f16x8 ash(u32x4 v) { return __builtin_bit_cast(f16x8, v); }
template <int NW>
DI void wait_flags(GAS u64* f, u64 tag, int idx) {
    for (unsigned spins = 0; spins < (1u << 20); spins++) {
        bool ok = true;
#pragma unroll
        for (int k = 0; k < NW; k++) ok &= (__hip_atomic_load(f + idx + 64 * k, RLX_AGENT) == tag);
        if (__all(ok)) break;
        __builtin_amdgcn_s_sleep(2);
    }
    __builtin_amdgcn_fence(__ATOMIC_ACQUIRE, "wavefront");
}

__global__ __launch_bounds__(512, 2) void k_fused(const uint16_t* __restrict__ x, const uint16_t* __restrict__ wr, const uint16_t* __restrict__ wi,
                                                  const uint16_t* __restrict__ w1, const uint16_t* __restrict__ b1, const uint16_t* __restrict__ w2,
                                                  const uint16_t* __restrict__ b2, uint16_t* __restrict__ out, char* __restrict__ ws
                                                  ) {
    extern __shared__ __attribute__((aligned(16))) char smem_g[];
    LAS char* smem = (LAS char*)smem_g;
    const int tid = threadIdx.x, lane = tid & 63;
    const int w = __builtin_amdgcn_readfirstlane(tid >> 6);
    const int xg = blockIdx.x & 7, li = blockIdx.x >> 3;
    const int fs = (xg & 3) * 2 + (li & 1), t = (li >> 1) * 2 + (xg >> 2), tl = li >> 1;
    const int jb = 8 * t + fs;
    const unsigned lds0 = (unsigned)(uintptr_t)smem;
    const int rh = w & 1, ct = w >> 1;
    const __amdgpu_buffer_rsrc_t rs = __builtin_amdgcn_make_buffer_rsrc(ws, 0, WS_END, 0x00020000);
    STAMP(0);
    int sbk[5];
#pragma unroll
    for (int i = 0; i < 4; i++) sbk[i] = 2 * ((i + tl) & 3);
    sbk[4] = 8;
    const int rot9 = tl % 9;
    const char* w1b = (const char*)w1 + fs * 1152;
    const char* w2b = (const char*)w2 + (size_t)fs * 576 * 1152;
    const unsigned ldsw = lds0 + w * 1024;
    auto mkvo = [&](bool full, unsigned pitch) {
        int ln = tid; PIN(ln); ln &= 63;
        unsigned r = full ? 4 * w + (ln >> 4) : 8 * w + (ln >> 3);
        unsigned c = full ? ((((ln >> 2) & 3) ^ (ln >> 4)) * 64) + (ln & 3) * 16 : ((((ln >> 2) & 1) ^ ((ln >> 4) & 1)) * 64) + (ln & 3) * 16;
        return r * pitch + c;
    };
    unsigned vo;
    auto gl = [&](auto U_, auto J_) {
        constexpr int U = decltype(U_)::value, J = decltype(J_)::value;
        constexpr int DST = (U % 6) * RING_SLOT + J * 8192;
        if constexpr (U < 3) glds16<DST>(w1b + (unsigned)(8 * 128 + (192 * U + 64 * J) * 9216), vo, ldsw);
        else if constexpr (U < 27) glds16<DST>(w1b + (unsigned)__builtin_amdgcn_readfirstlane(sbk[(U - 3) / 6] * 128 + (96 * ((U - 3) % 6) + 32 * J) * 9216), vo, ldsw);
        else if constexpr (U < 30) glds16<DST>(w2b + (unsigned)__builtin_amdgcn_readfirstlane(rot9 * 128 + (192 * (U - 27) + 64 * J) * 1152), vo, ldsw);
    };
    auto setvo = [&](auto U_) {
        constexpr int U = decltype(U_)::value;
        if constexpr (U == 0) vo = mkvo(false, 9216);
        if constexpr (U == 3) vo = mkvo(true, 9216);
        if constexpr (U == 27) {
            int ln = tid; PIN(ln); ln &= 63;
            vo = (unsigned)(8 * w + (ln >> 3)) * 1152 + ((((ln >> 1) & 3) ^ ((ln >> 4) & 3)) * 32) + (ln & 1) * 16;
        }
    };
    auto gl3 = [&](auto U_) { setvo(U_); static_for<0, 3>([&](auto J_) { gl(U_, J_); }); };

    Dft24h m24;
    {
        const int i = lane >> 3, c = lane & 7;
        const char* xr = (const char*)x + (size_t)(8 * t + w) * 1152;
        const unsigned xo = (unsigned)i * (256 * 1152) + c * 16;
        u32x4 q[9];
        static_for<0, 9>([&](auto N_) {
            constexpr int n = decltype(N_)::value;
            q[n] = gload16<n * 128>(xr, xo);
        });
        const u32x2 ep = __builtin_amdgcn_raw_buffer_load_b64(rs, tid == 0 ? 0u : OOB, WS_EPOCH, 16);
        const bool dob = tid >= 384 && tid < 384 + 72;
        const u32x4 bb = *(const u32x4*)((const char*)b1 + fs * 1152 + (dob ? tid - 384 : 0) * 16);
        SB0();
        unsigned tw;
        {
            float an = (float)(((tid >> 4) & 15) * (8 * (tid & 15) + fs)) * (1.0f / 2048.0f);
            tw = pack_f16(__builtin_amdgcn_cosf(an), __builtin_amdgcn_sinf(an));
        }
        if (w >= 4) dft24h_setup(m24, lane); else __builtin_amdgcn_s_sleep(6);
        SB0();
        gl3(IC<0>{}); gl3(IC<1>{}); gl3(IC<2>{});
        SB0();
        asm volatile("s_waitcnt vmcnt(11)" : "+v"(q[0]), "+v"(q[1]), "+v"(q[2]), "+v"(q[3]), "+v"(q[4]), "+v"(q[5]), "+v"(q[6]), "+v"(q[7]), "+v"(q[8]) :: "memory");
        STAMP(13);
        f32x2 d[36];
        f32x2 s2 = {0.f, 0.f};
#pragma unroll
        for (int n = 0; n < 9; n++)
#pragma unroll
            for (int e = 0; e < 4; e++) { f32x2 v = {bf_lo(q[n][e]), bf_hi(q[n][e])}; d[4 * n + e] = v; s2 += v; }
        const float mu = red8(s2[0] + s2[1]) * (1.0f / 576.0f);
        const f32x2 mu2 = {mu, mu};
        f32x2 q2 = {0.f, 0.f};
#pragma unroll
        for (int e = 0; e < 36; e++) { d[e] -= mu2; q2 = __builtin_elementwise_fma(d[e], d[e], q2); }
        const float var = red8(q2[0] + q2[1]) * (1.0f / 576.0f);
        const float m1 = rbf(mu);
        const float is1 = 1.0f / rbf(__builtin_sqrtf(rbf(rbf(var) + 1e-5f)));
        const float mean2 = (mu - m1) * is1;
        const float var2 = var * is1 * is1;
        const float is2 = 1.0f / rbf(__builtin_sqrtf(rbf(rbf(var2) + 1e-5f)));
        const float ya = is1 * is2, yb = (mean2 - rbf(mean2)) * is2;
        const f32x2 ya2 = {ya, ya}, yb2 = {yb, yb};
        const int mloc = 8 * w + i;
        LAS char* yw = smem + LDS_Y + (mloc >> 5) * 36 * 1024 + (c >> 1) * 1024 + (((mloc & 31) ^ c) + 32 * (c & 1)) * 16;
#pragma unroll
        for (int n = 0; n < 9; n++) {
            u32x4 o;
#pragma unroll
            for (int e = 0; e < 4; e++) { f32x2 v = __builtin_elementwise_fma(d[4 * n + e], ya2, yb2); o[e] = pack_bf16(v[0], v[1]); }
            *(LAS u32x4*)(yw + n * 4096) = o;
        }
        if (w == fs) {
            const f32x2 xa = {is1, is1}, xb = {mean2, mean2};
            const int pos = (i & 1) ? 4 + (i >> 1) : ((i >> 2) | (i & 2));
            LAS char* xw = smem + LDS_XS + pos * 1152 + c * 16;
#pragma unroll
            for (int n = 0; n < 9; n++) {
                u32x4 o;
#pragma unroll
                for (int e = 0; e < 4; e++) { f32x2 v = __builtin_elementwise_fma(d[4 * n + e], xa, xb); o[e] = pack_bf16(v[0], v[1]); }
                *(LAS u32x4*)(xw + n * 128) = o;
            }
        }
        if (dob) {
            f32x4 lo = {bf_lo(bb[0]), bf_hi(bb[0]), bf_lo(bb[1]), bf_hi(bb[1])}, hi = {bf_lo(bb[2]), bf_hi(bb[2]), bf_lo(bb[3]), bf_hi(bb[3])};
            *(LAS f32x4*)(smem + LDS_BIAS + (tid - 384) * 32) = lo;
            *(LAS f32x4*)(smem + LDS_BIAS + (tid - 384) * 32 + 16) = hi;
        }
        if (tid == 0) *(LAS u32x2*)(smem + LDS_TAG) = ep;
        if (tid < 256) *(LAS unsigned*)(smem + LDS_T256 + tid * 4) = tw;
        STAMP(14);
        waitv<0>();
        WAITL0();
    }
    BAR();
    STAMP(1);
    u64 epoch;
    {
        u32x2 e2 = *(LAS u32x2*)(smem + LDS_TAG);
        epoch = ((u64)(unsigned)__builtin_amdgcn_readfirstlane(e2[1]) << 32) | (unsigned)__builtin_amdgcn_readfirstlane(e2[0]);
    }
    const u64 tag = (epoch + 1) * 0x9E3779B97F4A7C15ull + 0x632BE59BD9B4E019ull;

    auto put_flag = [&](bool me, unsigned off, u64 val) {
        u32x2 o = {(unsigned)val, (unsigned)(val >> 32)};
        __builtin_amdgcn_raw_buffer_store_b64(o, rs, me ? 0u : OOB, off, 16);
    };
    LAS char* trc;
    auto settr = [&](auto U_) {
        constexpr int U = decltype(U_)::value;
        if constexpr (U == 0 || U == 3 || U == 27) {
            int ln = tid; PIN(ln); ln &= 63;
            int G = ln >> 4, q = (ln >> 2) & 3, p = ln & 3;
            int row = 8 * (G >> 1) + q;
            if constexpr (U == 3) trc = smem + row * 256 + ((ct ^ q) * 64) + (G & 1) * 32 + p * 8 + ((U % 6) / 2) * 2 * RING_SLOT;
            else if constexpr (U == 0) trc = smem + row * 128 + (((ct & 1) ^ ((q >> 1) & 1)) * 64) + (G & 1) * 32 + p * 8;
            else {
                row = 16 * (G >> 1) + 4 * (G & 1) + q;
                trc = smem + row * 128 + ((ct ^ ((row >> 1) & 3)) * 32) + p * 8 + ((U % 6) / 2) * 2 * RING_SLOT;
            }
            PIN(trc);
        } else if constexpr (U % 2 == 0) {
            trc += (U % 6 == 0) ? -4 * RING_SLOT : 2 * RING_SLOT;
            PIN(trc);
        }
    };
    bf16x8 af[36];
    auto load_y = [&]() {
        LAS char* ab = smem + LDS_Y + rh * 36 * 1024;
#pragma unroll
        for (int j = 0; j < 36; j++) af[j] = *(LAS bf16x8*)(ab + j * 1024 + (lane ^ (2 * (j & 3) + (lane >> 5))) * 16);
    };
    LAS char* biasp = smem + LDS_BIAS + (32 * ct + 4 * (lane >> 5)) * 4;
    f32x16 acc[2];
    s16x4 bl[6], bh[6];
    float gt[4];
    unsigned hq[5][8];
    auto ldbias = [&](auto P_, auto Q_, int blk) {
        constexpr int P = decltype(P_)::value, Q = decltype(Q_)::value;
        f32x4 b = *(LAS f32x4*)(biasp + blk * 256 + Q * 32);
        acc[P][4 * Q] = b[0]; acc[P][4 * Q + 1] = b[1]; acc[P][4 * Q + 2] = b[2]; acc[P][4 * Q + 3] = b[3];
    };
    auto gop = [&](auto C_, auto K_) {
        constexpr int C = decltype(C_)::value, K = decltype(K_)::value, P = C == 4 ? 1 : (C & 1);
        constexpr int NB = C == 4 ? 1 : C + 2;
        const float c0 = -2.0f * 0.7978845608028654f * 1.4426950408889634f;
        const float c1 = c0 * 0.044715f;
        if constexpr (K < 124) {
            constexpr int Q = K / 31, k = K % 31;
            if constexpr (k < 28) {
                constexpr int st = k / 4, e = k % 4;
                float xx = acc[P][4 * Q + e];
                if constexpr (st == 0) gt[e] = xx * xx;
                else if constexpr (st == 1) gt[e] = __builtin_fmaf(gt[e], c1, c0);
                else if constexpr (st == 2) gt[e] = xx * gt[e];
                else if constexpr (st == 3) gt[e] = __builtin_amdgcn_exp2f(gt[e]);
                else if constexpr (st == 4) gt[e] = 1.0f + gt[e];
                else if constexpr (st == 5) gt[e] = __builtin_amdgcn_rcpf(gt[e]);
                else gt[e] = xx * gt[e];
                PIN(gt[e]);
            } else if constexpr (k == 28) { hq[C][2 * Q] = pack_bf16(gt[0], gt[1]); PIN(hq[C][2 * Q]); }
            else if constexpr (k == 29) { hq[C][2 * Q + 1] = pack_bf16(gt[2], gt[3]); PIN(hq[C][2 * Q + 1]); }
            else { if constexpr (NB <= 3) ldbias(IC<P>{}, IC<Q>{}, sbk[NB]); }
        }
    };
    auto grange = [&](auto C_, auto A_, auto B_) {
        static_for<decltype(A_)::value, decltype(B_)::value>([&](auto K_) { gop(C_, K_); });
    };
    const f32x16 zero16 = {0, 0, 0, 0, 0, 0, 0, 0, 0, 0, 0, 0, 0, 0, 0, 0};
    auto bfrag = [&](auto J_) {
        constexpr int J = decltype(J_)::value;
        return __builtin_bit_cast(bf16x8, __builtin_shufflevector(bl[J], bh[J], 0, 1, 2, 3, 4, 5, 6, 7));
    };
    auto fslot = [&](auto U_, auto LO_, auto GI_, auto&& fill) {
        constexpr int U = decltype(U_)::value, v = U - 3, c = v / 6, s = v % 6;
        constexpr bool LO = decltype(LO_)::value;
        constexpr int PB = c & 1;
        constexpr int PA = s == 0 ? PB ^ 1 : PB;
        constexpr int sp = (s + 5) % 6;
        settr(U_);
        LAS char* sb = trc + (U % 2) * RING_SLOT;
        setvo(GI_);
        static_for<0, 6>([&](auto G_) {
            constexpr int G = decltype(G_)::value;
            if constexpr (G < 3) {
                if constexpr (LO) acc[PA] = MFMA32(bfrag(IC<G + 3>{}), af[6 * sp + G + 3], acc[PA]);
            } else {
                acc[PB] = MFMA32(bfrag(IC<G - 3>{}), af[6 * s + G - 3], acc[PB]);
            }
            SB0();
            bl[G] = tr_rd(sb + G * 4096); bh[G] = tr_rd(sb + G * 4096 + 1024);
            if constexpr (G < 3) gl(GI_, G_);
            fill(G_);
            SB0();
        });
    };
    auto hslot = [&](auto U_, auto PB_, auto LO_, auto&& fill) {
        constexpr int U = decltype(U_)::value, ph = U / 27, s = U % 3, PB = decltype(PB_)::value;
        constexpr int PA = s == 0 ? PB ^ 1 : PB;
        constexpr int R2 = ph == 0 ? 512 : 1024;
        settr(U_);
        LAS char* sb = trc + (U % 2) * RING_SLOT;
        static_for<0, 12>([&](auto G_) {
            constexpr int G = decltype(G_)::value;
            if constexpr (G < 3) {
                if constexpr (decltype(LO_)::value) acc[PA] = MFMA32(bfrag(IC<G + 3>{}), af[(12 * s + 33) % 36 + G], acc[PA]);
            } else {
                acc[PB] = MFMA32(bfrag(IC<(G - 3) % 6>{}), af[12 * s + G - 3], (ph == 1 && s == 0 && G == 3) ? zero16 : acc[PB]);
            }
            SB0();
            bl[G % 6] = tr_rd(sb + G * 2048); bh[G % 6] = tr_rd(sb + G * 2048 + R2);
            fill(G_);
            SB0();
        });
    };
    auto iss2 = [&](auto GI_, auto GI2_) {
        return [&, GI_, GI2_](auto G_) {
            constexpr int G = decltype(G_)::value;
            if constexpr (G == 0) setvo(GI_);
            if constexpr (G < 6 && G % 2 == 0) gl(GI_, IC<G / 2>{});
            if constexpr (G >= 6 && G % 2 == 0) gl(GI2_, IC<G / 2 - 3>{});
        };
    };
    using T = std::true_type; using F = std::false_type;
    auto nofill = [&](auto) {};

    if (w < 4) {
        load_y();
        static_for<0, 4>([&](auto Q_) { ldbias(IC<1>{}, Q_, sbk[4]); ldbias(IC<0>{}, Q_, sbk[0]); });
        hslot(IC<0>{}, IC<1>{}, F{}, nofill); WAITL0(); BAR();
        hslot(IC<1>{}, IC<1>{}, T{}, iss2(IC<3>{}, IC<4>{})); BAR();
        hslot(IC<2>{}, IC<1>{}, T{}, iss2(IC<5>{}, IC<99>{})); waitv<6>(); BAR();
        STAMP(9);
        static_for<3, 9>([&](auto U_) {
            constexpr int U = decltype(U_)::value, s = U - 3;
            fslot(U_, T{}, IC<U + 3>{}, [&](auto G_) {
                constexpr int gi = 6 * s + decltype(G_)::value - 4;
                if constexpr (gi >= 0) grange(IC<4>{}, IC<(gi * 124) / 32>{}, IC<((gi + 1) * 124) / 32>{});
            });
            waitv<6>(); BAR();
        });
    } else {
        const int v = w - 4;
        const unsigned PP = 0x3F803F80u, PM = 0xBF803F80u  , MP = 0x3F80BF80u  , MM = 0xBF80BF80u;
        unsigned A0, A1, A2, A3, B0, B1, B2, B3; float cc;
        const float c8 = 0.70710678118654752f;
        if (v == 0) { A0 = PP; A1 = PP; A2 = PP; A3 = PP; B0 = PP; B1 = PP; B2 = MM; B3 = MM; cc = 1.f; }
        else if (v == 1) { A0 = PM; A1 = 0; A2 = PM; A3 = MP; B0 = 0; B1 = MP; B2 = MM; B3 = PP; cc = c8; }
        else if (v == 2) { A0 = PP; A1 = MM; A2 = 0; A3 = 0; B0 = 0; B1 = 0; B2 = MP; B3 = MP; cc = 1.f; }
        else { A0 = PM; A1 = 0; A2 = MP; A3 = PM; B0 = 0; B1 = PM; B2 = MM; B3 = PP; cc = c8; }
        float re[12], im[12];
        unsigned hw_[12];
        auto dot = [](unsigned a, unsigned b, float c) { return __builtin_amdgcn_fdot2_f32_bf16(__builtin_bit_cast(bf16x2, a), __builtin_bit_cast(bf16x2, b), c, false); };
        {
            const int G = lane >> 4, q = (lane >> 2) & 3, p = lane & 3;
            LAS char* xb = smem + LDS_XS + q * 1152 + (96 * (G >> 1) + 16 * (G & 1) + 4 * p) * 2;
            u32x2 ev[12], od[12];
#pragma unroll
            for (int a = 0; a < 12; a++) {
                ev[a] = __builtin_bit_cast(u32x2, tr_rd(xb + rho(a, 0) * 48));
                od[a] = __builtin_bit_cast(u32x2, tr_rd(xb + rho(a, 0) * 48 + 4 * 1152));
            }
            SB0();
            load_y();
            SB0();
#pragma unroll
            for (int a = 0; a < 12; a++) {
                float r_ = dot(ev[a][0], A0, dot(ev[a][1], A1, cc * dot(od[a][0], A2, dot(od[a][1], A3, 0.f))));
                float i_ = dot(ev[a][0], B0, dot(ev[a][1], B1, cc * dot(od[a][0], B2, dot(od[a][1], B3, 0.f))));
                hw_[a] = pack_f16(r_, i_);
            }
#pragma unroll
            for (int a = 0; a < 12; a++) PIN(hw_[a]);
            SB0();
        }
        WAITL0(); SB0(); STAMPB(0); BAR();
        dft24h_stage(m24, hw_, re, im);
        gl3(IC<3>{}); gl3(IC<4>{});
#pragma unroll
        for (int a = 0; a < 12; a++) { hw_[a] = cmulh(pack_f16(re[a], im[a]), m24.tw[a]); PIN(hw_[a]); }
        SB0(); STAMPB(1); BAR();
        dft24h_stage(m24, hw_, re, im);
        gl3(IC<5>{});
        {
            const int hh = lane >> 5;
            const unsigned zo = (lane & 31) < 24 ? (unsigned)(((lane & 31) * 24 + 4 * hh) * 4) : OOB;
            const unsigned zb = WS_ZV + (unsigned)(jb * 4 + v) * ZV_PITCH;
            unsigned z0 = 0;
#pragma unroll
            for (int qd = 0; qd < 3; qd++) {
                u32x4 o;
#pragma unroll
                for (int e = 0; e < 4; e++) o[e] = pack_f16(re[4 * qd + e] * (1.0f / 64.0f), im[4 * qd + e] * (1.0f / 64.0f));
                if (qd == 0) z0 = o[0];
                __builtin_amdgcn_raw_buffer_store_b128(o, rs, zo, zb + qd * 32, 16);
            }
            __builtin_amdgcn_raw_buffer_store_b32(z0, rs, lane == 0 ? 0u : OOB, zb + 576 * 4, 16);
        }
        static_for<0, 4>([&](auto Q_) { ldbias(IC<0>{}, Q_, sbk[0]); ldbias(IC<1>{}, Q_, sbk[1]); });
        STAMPB(2); waitv<10>(); STAMPB(3); BAR();
        static_for<3, 9>([&](auto U_) {
            constexpr int U = decltype(U_)::value;
            if constexpr (U == 3) fslot(U_, F{}, IC<U + 3>{}, nofill); else fslot(U_, T{}, IC<U + 3>{}, nofill);
            if constexpr (U <= 4) waitv<10>(); else waitv<6>();
            BAR();
            if constexpr (U == 5) __builtin_amdgcn_raw_buffer_store_b32((unsigned)(tag >> 32), rs, tid == 256 ? 0u : OOB, WS_ROWF + jb * 4, 16);
        });
    }
    STAMP(10);
    u32x4 rf;
    const unsigned tg = (unsigned)(tag >> 32);
    const int wks = t == 31 ? 273 + ((fs + 1) & 1) : 9 * t - ((fs + 9 * t) & 1);
    static_for<9, 27>([&](auto U_) {
        constexpr int U = decltype(U_)::value, c = (U - 3) / 6, s = (U - 3) % 6;
        if constexpr (U == 25) {
            asm volatile("" : "+v"(rf));
            for (unsigned spins = 0; spins < (1u << 20); spins++) {
                if (__all(rf[0] == tg && rf[1] == tg && rf[2] == tg && rf[3] == tg)) break;
                __builtin_amdgcn_s_sleep(8);
                asm volatile("" ::: "memory");
                rf = __builtin_amdgcn_raw_buffer_load_b128(rs, lane * 16, WS_ROWF, 16);
            }
            SB0();
        }
        fslot(U_, T{}, IC<(U < 25 ? U + 3 : 99)>{}, [&](auto G_) {
            constexpr int G = decltype(G_)::value;
            constexpr int gi = 6 * s + G - 4;
            if constexpr (gi >= 0) grange(IC<c - 1>{}, IC<(gi * 124) / 32>{}, IC<((gi + 1) * 124) / 32>{});
            if constexpr (U == 21 && G == 4) {
                int ln = tid; PIN(ln); ln &= 63;
                rf = gload16<WS_ROWF>(ws, ln * 16);
            }
            if constexpr (U == 25 && G < 3) {
                int ln = tid; PIN(ln); ln &= 63;
                const int un = ((w & 3) * 3 + G) * 64 + ln, j = un / 3, part = un - 3 * j;
                const bool use = w < 4 ? fs <= 4 : (fs == 0 || fs >= 4);
                const int vv = w < 4 ? (fs <= 3 ? fs : 0) : (fs >= 5 ? 8 - fs : 0);
                const unsigned so = WS_ZV + vv * ZV_PITCH + (w < 4 ? 9 * t : 565 - 9 * t) * 4;
                glds16s(ws + so, use ? (unsigned)j * (4 * ZV_PITCH) + part * 16 : 0u, lds0 + LDS_ZS + (w * 3 + G) * 1024);
            }
            if constexpr (U == 26 && G < 2) {
                int ln = tid; PIN(ln); ln &= 63;
                const int un = ((w & 3) * 2 + G) * 64 + ln;
                glds16r((const char*)(w < 4 ? wr : wi) + (fs * 289 + wks) * 2, (unsigned)(un >> 1) * (8 * 289 * 2) + (un & 1) * 16, lds0 + LDS_WS + (w * 2 + G) * 1024);
            }
        });
        if constexpr (U == 21 || U == 22) { waitv<7>(); BAR(); }
        else if constexpr (U < 26) { waitv<6>(); BAR(); }
    });
    static_for<0, 3>([&](auto G_) { constexpr int G = decltype(G_)::value; acc[1] = MFMA32(bfrag(IC<G + 3>{}), af[33 + G], acc[1]); });
    STAMP(2);
    WAITL0();
    waitv<0>();
    BAR();
    {
        int ln = tid; PIN(ln); ln &= 63;
        const int r = fs, col = ln & 15, g = ln >> 4, cc = col < 10 ? col : 9;
        const f32x4 z4 = {0.f, 0.f, 0.f, 0.f};
        u32x4 za[2], zb[2], tf[2];
        u16x2 wp[2][4];
        {
            LAS char* zp = smem + LDS_ZS + (64 * g + 2 * w) * 48;
            LAS char* wq = smem + LDS_WS + (64 * g + 2 * w) * 32 + (9 * t - wks + cc) * 2;
#pragma unroll
            for (int tl_ = 0; tl_ < 2; tl_++) {
#pragma unroll
                for (int e = 0; e < 4; e++) {
                    za[tl_][e] = *(LAS unsigned*)(zp + (16 * e + tl_) * 48 + cc * 4);
                    zb[tl_][e] = *(LAS unsigned*)(zp + 12288 + (16 * e + tl_) * 48 + (11 - cc) * 4);
                }
                tf[tl_] = *(LAS u32x4*)(smem + LDS_T256 + ((2 * w + tl_) * 16 + 4 * g) * 4);
            }
#pragma unroll
            for (int tl_ = 0; tl_ < 2; tl_++)
#pragma unroll
                for (int e = 0; e < 4; e++) {
                    wp[tl_][e][0] = *(LAS unsigned short*)(wq + (16 * e + tl_) * 32);
                    wp[tl_][e][1] = *(LAS unsigned short*)(wq + 8192 + (16 * e + tl_) * 32);
                }
        }
        SB0();
        LAS char* hw = smem + LDS_H + ((2 * rh + ((ln >> 4) & 1)) * 18 + ct) * 1024 + ((ln & 15) + 16 * (ln >> 5)) * 16;
        auto hst = [&](auto C_) {
            constexpr int C = decltype(C_)::value;
            u32x4 o0 = {hq[C][0], hq[C][1], hq[C][2], hq[C][3]}, o1 = {hq[C][4], hq[C][5], hq[C][6], hq[C][7]};
            *(LAS u32x4*)(hw + sbk[C] * 2048) = o0; *(LAS u32x4*)(hw + sbk[C] * 2048 + 512) = o1;
        };
        hst(IC<0>{}); hst(IC<1>{}); hst(IC<2>{});
        if (w < 4) hst(IC<4>{});
        put_flag(jb == 0 && tid == 0, WS_EPOCH, epoch + 1);
        SB0();
        LAS char* xw = smem + LDS_XA + ((4 * g * 4 + (w >> 1)) * 10 + cc) * 16 + (w & 1) * 8;
        LAS char* xr = smem + LDS_XA + ((2 * w * 4 + g) * 10 + cc) * 16;
        f32x4 dr[2], di[2];
        u32x4 P3, Q3, P4, Q4;
        {
            float an[4];
#pragma unroll
            for (int e = 0; e < 4; e++) an[e] = (float)(((4 * g + e) * (8 * col + r)) & 127) * (1.0f / 128.0f) + (r == 4 ? 0.25f : 0.f);
            const u32x4 P1 = mat16(an), Q1 = rotq4(P1);
            const unsigned fl = r == 4 ? 0x00008000u : 0x80000000u;
            const u32x4 P2 = P1 ^ fl, Q2 = Q1 ^ fl;
            const unsigned mA = r <= 4 ? ~0u : 0u, mB = (r == 0 || r >= 4) ? ~0u : 0u;
#pragma unroll
            for (int tl_ = 0; tl_ < 2; tl_++) {
                za[tl_] &= mA; zb[tl_] &= mB;
                dr[tl_] = MFMA16H(ash(P1), ash(za[tl_]), z4); di[tl_] = MFMA16H(ash(Q1), ash(za[tl_]), z4);
                dr[tl_] = MFMA16H(ash(P2), ash(zb[tl_]), dr[tl_]); di[tl_] = MFMA16H(ash(Q2), ash(zb[tl_]), di[tl_]);
            }
            u16x2 mx = {0, 0};
#pragma unroll
            for (int tl_ = 0; tl_ < 2; tl_++)
#pragma unroll
                for (int e = 0; e < 4; e++) mx = __builtin_elementwise_max(mx, __builtin_bit_cast(u16x2, __builtin_bit_cast(unsigned, wp[tl_][e]) & 0x7fff7fffu));
            unsigned m1 = mx[0] > mx[1] ? mx[0] : mx[1];
            { auto q = __builtin_amdgcn_permlane16_swap(m1, m1, false, false); m1 = q[0] > q[1] ? q[0] : q[1]; }
            { auto q = __builtin_amdgcn_permlane32_swap(m1, m1, false, false); m1 = q[0] > q[1] ? q[0] : q[1]; }
            *(LAS unsigned*)(smem + LDS_WMAX + col * 32 + w * 4) = m1;
            u32x2 o[4];
#pragma unroll
            for (int e = 0; e < 4; e++)
#pragma unroll
                for (int tl_ = 0; tl_ < 2; tl_++) o[e][tl_] = cmulh(pack_f16(dr[tl_][e], di[tl_][e]), tf[tl_][e]);
            if (col < 10) {
#pragma unroll
                for (int e = 0; e < 4; e++) *(LAS u32x2*)(xw + e * 640) = o[e];
            }
            SB0();
            grange(IC<3>{}, IC<0>{}, IC<62>{});
#pragma unroll
            for (int e = 0; e < 4; e++) an[e] = (float)(((4 * g + e) * col) & 15) * (1.0f / 16.0f);
            P3 = mat16(an); Q3 = rotq4(P3);
            WAITL0(); STAMPB(5); BAR();
        }
        float inv;
        {
            const u32x4 xa0 = *(LAS u32x4*)xr, xa1 = *(LAS u32x4*)(xr + 640);
            const u32x4 m0 = *(LAS u32x4*)(smem + LDS_WMAX + col * 32), m1 = *(LAS u32x4*)(smem + LDS_WMAX + col * 32 + 16);
            u32x2 tt[4];
#pragma unroll
            for (int e = 0; e < 4; e++) tt[e] = *(LAS u32x2*)(smem + LDS_T256 + ((4 * g + e) * 16 + 2 * w) * 4);
            SB0();
            grange(IC<3>{}, IC<62>{}, IC<93>{});
            SB0();
            dr[0] = MFMA16H(ash(P3), ash(xa0), z4); di[0] = MFMA16H(ash(Q3), ash(xa0), z4);
            dr[1] = MFMA16H(ash(P3), ash(xa1), z4); di[1] = MFMA16H(ash(Q3), ash(xa1), z4);
            WAITL0(); SB0(); BAR();
            unsigned ex = max(max(max(m0[0], m0[1]), max(m0[2], m0[3])), max(max(m1[0], m1[1]), max(m1[2], m1[3]))) >> 7;
            ex = min(max(ex, 1u), 252u);
            const float wsc = __uint_as_float((253u - ex) << 23);
            inv = __uint_as_float((ex + 1u) << 23);
#pragma unroll
            for (int tl_ = 0; tl_ < 2; tl_++) {
                u32x4 y;
#pragma unroll
                for (int e = 0; e < 4; e++) {
                    const unsigned wv = __builtin_bit_cast(unsigned, wp[tl_][e]);
                    const float wre = bf_lo(wv) * wsc, wim = bf_hi(wv) * wsc;
                    y[e] = pack_f16(dr[tl_][e] * wre - di[tl_][e] * wim, -(dr[tl_][e] * wim + di[tl_][e] * wre));
                }
                dr[tl_] = MFMA16H(ash(P3), ash(y), z4); di[tl_] = MFMA16H(ash(Q3), ash(y), z4);
            }
            SB0();
            grange(IC<3>{}, IC<93>{}, IC<124>{});
            hst(IC<3>{});
            SB0();
            if (col < 10) {
#pragma unroll
                for (int e = 0; e < 4; e++) {
                    u32x2 o = {cmulh(pack_f16(dr[0][e], di[0][e]), tt[e][0]), cmulh(pack_f16(dr[1][e], di[1][e]), tt[e][1])};
                    *(LAS u32x2*)(xw + e * 640) = o;
                }
            }
            float an[4];
#pragma unroll
            for (int e = 0; e < 4; e++) an[e] = (float)((col * (8 * (4 * g + e) + r)) & 127) * (1.0f / 128.0f);
            P4 = mat16(an); Q4 = rotq4(P4);
            WAITL0();
            gl3(IC<28>{}); gl3(IC<29>{});
            STAMPB(6); BAR();
        }
        {
            const u32x4 xa0 = *(LAS u32x4*)xr, xa1 = *(LAS u32x4*)(xr + 640);
            SB0();
            {
                LAS char* ab = smem + LDS_H + 2 * rh * 18 * 1024 + ln * 16;
#pragma unroll
                for (int j = 0; j < 18; j++) { af[j] = *(LAS bf16x8*)(ab + j * 1024); af[18 + j] = *(LAS bf16x8*)(ab + (18 + j) * 1024); }
            }
            SB0();
            const float fsc = (64.0f * 256.0f / (2048.0f * 576.0f)) * ((r & 3) == 0 ? 0.5f : 1.0f);
            const unsigned vo_ = col < 10 ? (unsigned)(64 * g * 80 + col * 8) : OOB;
            const unsigned vb = WS_VP + (unsigned)(r * 32 + t) * (256 * 80) + w * 2 * 80;
            dr[0] = MFMA16H(ash(P4), ash(xa0), z4); di[0] = MFMA16H(ash(Q4), ash(xa0), z4);
            dr[1] = MFMA16H(ash(P4), ash(xa1), z4); di[1] = MFMA16H(ash(Q4), ash(xa1), z4);
#pragma unroll
            for (int tl_ = 0; tl_ < 2; tl_++)
#pragma unroll
                for (int e = 0; e < 4; e++) {
                    u32x2 o = {__float_as_uint(dr[tl_][e] * fsc * inv), __float_as_uint(di[tl_][e] * fsc * inv)};
                    __builtin_amdgcn_raw_buffer_store_b64(o, rs, vo_, vb + (16 * e + tl_) * 80, 16);
                }
            SB0();
        }
    }
    STAMP(3);
    f32x4 ac[2][2];
    unsigned plane;
    { int ln = tid; PIN(ln); plane = (ln & 63) * 16; }
    const unsigned pbase = WS_PART + (unsigned)((fs * 32 + t) * 72 + w) * 1024;
    auto pstore = [&](auto P_, int blk) {
        constexpr int P = decltype(P_)::value;
        unsigned a0 = pack_bf16(ac[P][0][0], ac[P][0][1]), a1 = pack_bf16(ac[P][0][2], ac[P][0][3]);
        unsigned b0 = pack_bf16(ac[P][1][0], ac[P][1][1]), b1_ = pack_bf16(ac[P][1][2], ac[P][1][3]);
        auto s0 = __builtin_amdgcn_permlane16_swap(a0, b0, false, false);
        auto s1 = __builtin_amdgcn_permlane16_swap(a1, b1_, false, false);
        u32x4 o = {s0[0], s1[0], s0[1], s1[1]};
        __builtin_amdgcn_raw_buffer_store_b128(o, rs, plane, pbase + blk * 8192, 16);
    };
    const f32x4 zero4 = {0.f, 0.f, 0.f, 0.f};
    auto qslot = [&](auto U_, auto LO_, auto&& fill) {
        constexpr int U = decltype(U_)::value, s = U % 3, PB = ((U / 3) & 1);
        constexpr int PA = s == 0 ? PB ^ 1 : PB;
        constexpr int sp = (s + 2) % 3;
        settr(U_);
        LAS char* sb = trc + (U % 2) * RING_SLOT;
        static_for<0, 6>([&](auto G_) {
            constexpr int G = decltype(G_)::value;
            if constexpr (G < 3) {
                if constexpr (decltype(LO_)::value) {
                    ac[PA][0] = MFMA16(bfrag(IC<G + 3>{}), af[6 * sp + G + 3], ac[PA][0]);
                    ac[PA][1] = MFMA16(bfrag(IC<G + 3>{}), af[18 + 6 * sp + G + 3], ac[PA][1]);
                }
            } else {
                ac[PB][0] = MFMA16(bfrag(IC<G - 3>{}), af[6 * s + G - 3], (s == 0 && G == 3) ? zero4 : ac[PB][0]);
                ac[PB][1] = MFMA16(bfrag(IC<G - 3>{}), af[18 + 6 * s + G - 3], (s == 0 && G == 3) ? zero4 : ac[PB][1]);
            }
            SB0();
            bl[G] = tr_rd(sb + G * 4096); bh[G] = tr_rd(sb + G * 4096 + 1024);
            fill(G_);
            SB0();
        });
    };
    auto g2 = [&](auto RS_, auto S_, auto J_, int blk) {
        constexpr int RS = decltype(RS_)::value, S = decltype(S_)::value, J = decltype(J_)::value;
        glds16<RS * RING_SLOT + J * 8192>(w2b + (unsigned)__builtin_amdgcn_readfirstlane(blk * 128 + (192 * S + 64 * J) * 1152), vo, ldsw);
    };
    int cprev = rot9, cn = rot9 == 8 ? 0 : rot9 + 1;
    qslot(IC<27>{}, F{}, nofill); WAITL0(); waitv<11>(); BAR();
    qslot(IC<28>{}, T{}, [&](auto G_) { constexpr int G = decltype(G_)::value; g2(IC<G / 3>{}, IC<G / 3>{}, IC<G % 3>{}, cn); }); waitv<14>(); BAR();
    qslot(IC<29>{}, T{}, [&](auto G_) { constexpr int G = decltype(G_)::value; if constexpr (G < 3) g2(IC<2>{}, IC<2>{}, G_, cn); }); waitv<6>(); BAR();
    __builtin_amdgcn_raw_buffer_store_b32(tg, rs, tid == 0 ? 0u : OOB, WS_COLF + (fs * 32 + t) * 4, 16);
    STAMP(11);
    u32x4 cf = {0u, 0u, 0u, 0u};
    for (int it = 0; it < 4; it++) {
        cn = cn == 8 ? 0 : cn + 1;
        static_for<30, 33>([&](auto U_) {
            constexpr int U = decltype(U_)::value;
            if constexpr (U == 31) { if (it == 3) asm volatile("global_load_dwordx4 %0, %1, %2 offset:%3" : "+v"(cf) : "v"(plane), "s"(ws), "i"(WS_COLF) : "memory"); }
            qslot(U_, T{}, [&](auto G_) {
                constexpr int G = decltype(G_)::value;
                if constexpr (G < 3) g2(IC<3 + U % 3>{}, IC<U % 3>{}, G_, cn);
                if constexpr (U == 30 && G == 5) pstore(IC<1>{}, cprev);
            });
            waitv<7>(); BAR();
        });
        cprev = cprev == 8 ? 0 : cprev + 1;
        cn = cn == 8 ? 0 : cn + 1;
        if (it < 3) {
            static_for<33, 36>([&](auto U_) {
                constexpr int U = decltype(U_)::value;
                qslot(U_, T{}, [&](auto G_) {
                    constexpr int G = decltype(G_)::value;
                    if constexpr (G < 3) g2(IC<U % 3>{}, IC<U % 3>{}, G_, cn);
                    if constexpr (U == 33 && G == 5) pstore(IC<0>{}, cprev);
                });
                waitv<7>(); BAR();
            });
            cprev = cprev == 8 ? 0 : cprev + 1;
        }
    }
    qslot(IC<33>{}, T{}, [&](auto G_) {
        constexpr int G = decltype(G_)::value;
        if constexpr (G == 5) pstore(IC<0>{}, cprev);
    });
    waitv<4>(); BAR();
    cprev = cprev == 8 ? 0 : cprev + 1;
    asm volatile("" : "+v"(cf));
    for (unsigned spins = 0; spins < (1u << 20); spins++) {
        if (__all(cf[0] == tg && cf[1] == tg && cf[2] == tg && cf[3] == tg)) break;
        __builtin_amdgcn_s_sleep(8);
        asm volatile("" ::: "memory");
        cf = __builtin_amdgcn_raw_buffer_load_b128(rs, plane, WS_COLF, 16);
    }
    SB0();
    qslot(IC<34>{}, T{}, [&](auto G_) {
        constexpr int G = decltype(G_)::value;
        if constexpr (G == 0) {
            int ln = tid; PIN(ln);
            const int row = ln / 72, cc = ln % 72;
            glds16r((const char*)x, (unsigned)(jb + 256 * row) * 1152 + cc * 16, lds0 + LDS_XR + w * 1024);
        }
        if constexpr (G == 1) {
            int ln = tid; PIN(ln); ln &= 63;
            const int un = 512 + ln, row = un / 72, cc = un % 72;
            const char* src = w == 0 ? (const char*)x : (const char*)b2;
            const unsigned off = w == 0 ? (unsigned)(jb + 256 * row) * 1152 + cc * 16 : (w == 1 ? 0u : 128u) + ln * 16;
            glds16r(src, off, lds0 + LDS_XR + (w == 0 ? 8192 : w == 1 ? 9216 : 9216 + 128));
        }
        if constexpr (G >= 2 && G < 5) {
            int ln = tid; PIN(ln); ln &= 63;
            int un = (w * 3 + G - 2) * 64 + ln; if (un > 1279) un = 1279;
            const int seg = un / 5, part = un - 5 * seg;
            glds16s(ws + WS_VP + jb * 80, (unsigned)seg * (256 * 80) + part * 16, lds0 + LDS_VS + (w * 3 + G - 2) * 1024);
        }
    });
    waitv<6>(); BAR();
    qslot(IC<35>{}, T{}, nofill);
    static_for<0, 3>([&](auto G_) {
        constexpr int G = decltype(G_)::value;
        ac[1][0] = MFMA16(bfrag(IC<G + 3>{}), af[15 + G], ac[1][0]);
        ac[1][1] = MFMA16(bfrag(IC<G + 3>{}), af[33 + G], ac[1][1]);
    });
    pstore(IC<1>{}, cprev);
    STAMP(4);
    {
    int ln = tid; PIN(ln);
    waitv<1>(); BAR();
    if (w < 4) {
        waitv<0>();
        put_flag((ln & 63) == 0, WS_PARTF + (t * 64 + fs * 8 + w) * 8, tag);
        STAMP(5);
    } else {
        const int l6 = ln & 63, v = w - 4, hh = l6 >> 5, rB = v == 0 ? 4 : 8 - v;
        int n2 = l6 & 31; if (n2 > 23) n2 = 23;
        STAMPB(11);
        u32x2 A[12], B[12];
#pragma unroll
        for (int a = 0; a < 12; a++) {
            const int k = 24 * rho(a, hh) + n2;
            const bool mir = k > 288; const int kk = mir ? 576 - k : k;
            int gq = (kk * 57) >> 9; if (gq > 31) gq = 31;
            LAS char* vp = smem + LDS_VS + (kk + gq) * 8;
            A[a] = *(LAS u32x2*)(vp + ((v != 0 && mir) ? rB : v) * 2560);
            B[a] = *(LAS u32x2*)(vp + rB * 2560);
        }
        SB0();
        waitv<0>();
        put_flag(l6 == 0, WS_PARTF + (t * 64 + fs * 8 + w) * 8, tag);
        Dft24h mi; dft24h_setup(mi, l6);
        SB0();
        float re[12], im[12];
        float mx = 0.f;
#pragma unroll
        for (int a = 0; a < 12; a++) {
            const int k = 24 * rho(a, hh) + n2;
            const bool mir = k > 288, spec = (k == 0 || k == 288);
            float ax = __uint_as_float(A[a][0]), ay = __uint_as_float(A[a][1]), bx = __uint_as_float(B[a][0]), by = __uint_as_float(B[a][1]);
            if (mir) { ay = -ay; by = -by; }
            if (v == 0) {
                if (spec) { ay = 0.f; by = 0.f; }
                re[a] = ax + by; im[a] = ay - bx;
            } else {
                re[a] = spec ? 0.5f * (ax + bx) : ax; im[a] = spec ? 0.5f * (ay - by) : ay;
            }
            mx = fmaxf(mx, fmaxf(fabsf(re[a]), fabsf(im[a])));
        }
        STAMPB(12);
        unsigned mu_ = __float_as_uint(mx);
        mu_ = max(mu_, (unsigned)__builtin_amdgcn_update_dpp(0, (int)mu_, 0xB1, 0xf, 0xf, false));
        mu_ = max(mu_, (unsigned)__builtin_amdgcn_update_dpp(0, (int)mu_, 0x4E, 0xf, 0xf, false));
        mu_ = max(mu_, (unsigned)__builtin_amdgcn_update_dpp(0, (int)mu_, 0x141, 0xf, 0xf, false));
        mu_ = max(mu_, (unsigned)__builtin_amdgcn_update_dpp(0, (int)mu_, 0x128, 0xf, 0xf, false));
        { auto q = __builtin_amdgcn_permlane16_swap(mu_, mu_, false, false); mu_ = max(q[0], q[1]); }
        { auto q = __builtin_amdgcn_permlane32_swap(mu_, mu_, false, false); mu_ = max(q[0], q[1]); }
        unsigned ex = min(max(mu_ >> 23, 10u), 250u);
        const float sc = __uint_as_float((262u - ex) << 23), isc = __uint_as_float((ex - 8u) << 23);
        unsigned hw_[12];
#pragma unroll
        for (int a = 0; a < 12; a++) hw_[a] = pack_f16(re[a] * sc, im[a] * sc);
        dft24h_stage(mi, hw_, re, im);
#pragma unroll
        for (int a = 0; a < 12; a++) hw_[a] = cmulh(pack_f16(re[a], im[a]), mi.tw[a]);
        dft24h_stage(mi, hw_, re, im);
        if ((l6 & 31) < 24) {
#pragma unroll
            for (int a = 0; a < 12; a += 2) {
                f32x4 o = {re[a] * isc, -im[a] * isc, re[a + 1] * isc, -im[a + 1] * isc};
                *(LAS f32x4*)(smem + LDS_CB + (v * 576 + 24 * (l6 & 31) + rho(a, hh)) * 8) = o;
            }
        }
        WAITL0();
        STAMPB(13);
    }
    if (w == 0) wait_flags<1>((GAS u64*)(ws + WS_PARTF) + t * 64, tag, ln & 63);
    SB0(); BAR();
    STAMP(6);
#pragma unroll
    for (int rep = 0; rep < 2; rep++) {
        if (rep == 1 && w != 1) break;
        const int pid = rep == 0 ? ln : 512 + (ln & 63);
        const int i = pid & 7, pc = pid >> 3;
        u32x4 pv[8];
        const unsigned po = (unsigned)((pc >> 3) * 8 + ((pc >> 1) & 3) * 2 + (fs >> 2)) * 1024 + (16 * (2 * (pc & 1) + ((fs >> 1) & 1)) + 8 * (fs & 1) + i) * 16;
#pragma unroll
        for (int f = 0; f < 8; f++) pv[f] = __builtin_amdgcn_raw_buffer_load_b128(rs, po, WS_PART + (unsigned)(f * 32 + t) * 73728, 16);
        SB0();
        float cr[4], ci[4];
        cr[0] = 1.f; ci[0] = (i & 1) ? -1.f : 1.f;
#pragma unroll
        for (int r = 1; r < 4; r++) {
            float ang = (float)((i * r) & 7) * 0.125f;
            cr[r] = 2.f * __builtin_amdgcn_cosf(ang); ci[r] = -2.f * __builtin_amdgcn_sinf(ang);
        }
        float sv[8] = {0.f, 0.f, 0.f, 0.f, 0.f, 0.f, 0.f, 0.f};
#pragma unroll
        for (int r = 0; r < 4; r++)
#pragma unroll
            for (int h2 = 0; h2 < 4; h2++) {
                const f32x4 c0 = *(LAS f32x4*)(smem + LDS_CB + (r * 576 + 8 * pc + 2 * h2) * 8);
                sv[2 * h2] += cr[r] * c0[0] + ci[r] * c0[1]; sv[2 * h2 + 1] += cr[r] * c0[2] + ci[r] * c0[3];
            }
        const u32x4 xq = *(LAS u32x4*)(smem + LDS_XR + i * 1152 + pc * 16), bq = *(LAS u32x4*)(smem + LDS_XR + 9216 + pc * 16);
#pragma unroll
        for (int e = 0; e < 8; e++) { sv[e] = rbf(sv[e]); PIN(sv[e]); }
        SB0();
        float res[8];
#pragma unroll
        for (int e = 0; e < 8; e++) {
            float o = 0.f;
#pragma unroll
            for (int f = 0; f < 8; f++) o += (e & 1) ? bf_hi(pv[f][e >> 1]) : bf_lo(pv[f][e >> 1]);
            const float bv = (e & 1) ? bf_hi(bq[e >> 1]) : bf_lo(bq[e >> 1]), xv = (e & 1) ? bf_hi(xq[e >> 1]) : bf_lo(xq[e >> 1]);
            float ov = rbf(rbf(o) + bv);
            float gv = rbf(ov * __builtin_amdgcn_rcpf(1.0f + __builtin_amdgcn_exp2f(-1.4426950408889634f * ov)));
            res[e] = xv + rbf(sv[e] * gv);
        }
        u32x4 oo = {pack_bf16(res[0], res[1]), pack_bf16(res[2], res[3]), pack_bf16(res[4], res[5]), pack_bf16(res[6], res[7])};
        *(LAS u32x4*)(smem + LDS_OUT + i * 1152 + pc * 16) = oo;
    }
    WAITL0();
    STAMP(15);
    BAR();
#pragma unroll
    for (int rep = 0; rep < 2; rep++) {
        if (rep == 1 && w != 0) break;
        const int un = ln + 512 * rep;
        const int row = un / 72, cc = un % 72;
        *(u32x4*)((char*)out + (size_t)(jb + 256 * row) * 1152 + cc * 16) = *(LAS u32x4*)(smem + LDS_OUT + un * 16);
    }
    }
}

extern "C" void kernel_launch(void* const* d_in, const int* in_sizes, int n_in,
                              void* d_out, int out_size, void* d_ws, size_t ws_size,
                              hipStream_t stream) {
    (void)hipFuncSetAttribute((const void*)k_fused, hipFuncAttributeMaxDynamicSharedMemorySize, LDS_TOTAL);
    k_fused<<<256, 512, LDS_TOTAL, stream>>>((const uint16_t*)d_in[0], (const uint16_t*)d_in[1], (const uint16_t*)d_in[2],
                                             (const uint16_t*)d_in[4], (const uint16_t*)d_in[5], (const uint16_t*)d_in[6],
                                             (const uint16_t*)d_in[7], (uint16_t*)d_out, (char*)d_ws
                                             );
}
```

```cpp
#include <hip/hip_runtime.h>
#include <stdint.h>
#include <type_traits>

typedef __attribute__((ext_vector_type(8))) __bf16 bf16x8;
typedef __attribute__((ext_vector_type(2))) __bf16 bf16x2;
typedef __attribute__((ext_vector_type(8))) _Float16 f16x8;
typedef __attribute__((ext_vector_type(2))) _Float16 f16x2;
typedef __attribute__((ext_vector_type(4))) short s16x4;
typedef __attribute__((ext_vector_type(2))) float f32x2;
typedef __attribute__((ext_vector_type(4))) float f32x4;
typedef __attribute__((ext_vector_type(16))) float f32x16;
typedef __attribute__((ext_vector_type(4))) unsigned u32x4;
typedef __attribute__((ext_vector_type(2))) unsigned u32x2;
typedef __attribute__((ext_vector_type(2))) unsigned short u16x2;
typedef unsigned long long u64;
#define LAS __attribute__((address_space(3)))
#define GAS __attribute__((address_space(1)))
#define DI __device__ __forceinline__

constexpr int RING_SLOT = 24576;
constexpr int LDS_Y = 3 * RING_SLOT;
constexpr int LDS_H = 0;
constexpr int LDS_CB = 0;
constexpr int LDS_XR = RING_SLOT;
constexpr int LDS_G = RING_SLOT + 12288;
constexpr int LDS_S = 3 * RING_SLOT;
constexpr int LDS_WS = 4 * RING_SLOT;
constexpr int LDS_ZS = 5 * RING_SLOT;
constexpr int LDS_XB = 5 * RING_SLOT;
constexpr int LDS_VS = 2 * RING_SLOT;
constexpr int LDS_TAG = 6 * RING_SLOT;
constexpr int LDS_T256 = LDS_TAG + 16;
constexpr int LDS_WMAX = LDS_T256 + 1024;
constexpr int LDS_BIAS = LDS_WMAX + 512;
constexpr int LDS_XS = LDS_BIAS + 2304;
constexpr int LDS_XA = LDS_BIAS;
constexpr int LDS_TOTAL = LDS_XS + 8 * 1152 + 64;
constexpr unsigned WS_EPOCH = 0;
constexpr unsigned WS_ROWF = 256;
constexpr unsigned WS_COLF = 2048;
constexpr unsigned WS_PARTF = 4096;
constexpr unsigned ZV_PITCH = 580 * 4;
constexpr unsigned WS_ZV = 4096 + 16384;
constexpr unsigned WS_VP = WS_ZV + 256 * 4 * ZV_PITCH;
constexpr unsigned WS_PART = WS_VP + 8 * 32 * 256 * 80;
constexpr unsigned WS_END = WS_PART + 8u * 2048 * 576 * 2;
constexpr unsigned OOB = 0xF0000000u;

template <int IMM>
DI void glds16(const char* sbase, unsigned voff, unsigned ldsbase) {
    asm volatile("s_add_u32 m0, %2, %3\n\ts_nop 0\n\tglobal_load_lds_dwordx4 %0, %1" ::"v"(voff), "s"(sbase), "s"(ldsbase), "i"(IMM) : "memory", "scc");
}
DI void glds16r(const char* sbase, unsigned voff, unsigned ldsaddr) {
    asm volatile("s_mov_b32 m0, %2\n\ts_nop 0\n\tglobal_load_lds_dwordx4 %0, %1" ::"v"(voff), "s"(sbase), "s"(ldsaddr) : "memory");
}
DI void glds16s(const char* sbase, unsigned voff, unsigned ldsaddr) {
    asm volatile("s_mov_b32 m0, %2\n\ts_nop 0\n\tglobal_load_lds_dwordx4 %0, %1 sc1" ::"v"(voff), "s"(sbase), "s"(ldsaddr) : "memory");
}
template <int OFF>
DI u32x4 gload16(const char* sbase, unsigned voff) {
    u32x4 r;
    asm volatile("global_load_dwordx4 %0, %1, %2 offset:%3" : "=v"(r) : "v"(voff), "s"(sbase), "i"(OFF) : "memory");
    return r;
}
#define SB0() __builtin_amdgcn_sched_barrier(0)
#define PIN(x) asm volatile("" : "+v"(x))
DI s16x4 tr_rd(LAS char* p) { return __builtin_amdgcn_ds_read_tr16_b64_v4i16((LAS s16x4*)p); }
DI unsigned pack_bf16(float a, float b) { bf16x2 p; p[0] = (__bf16)a; p[1] = (__bf16)b; return __builtin_bit_cast(unsigned, p); }
DI unsigned pack_f16(float a, float b) { f16x2 p; p[0] = (_Float16)a; p[1] = (_Float16)b; return __builtin_bit_cast(unsigned, p); }
DI float bf_lo(unsigned u) { return __uint_as_float(u << 16); }
DI float bf_hi(unsigned u) { return __uint_as_float(u & 0xffff0000u); }
DI float rbf(float f) { return bf_lo(pack_bf16(f, 0.f)); }
#define STAMP(i)
#define STAMPB(i)
#define MFMA32(a, b, c) __builtin_amdgcn_mfma_f32_32x32x16_bf16((a), (b), (c), 0, 0, 0)
#define MFMA32H(a, b, c) __builtin_amdgcn_mfma_f32_32x32x16_f16((a), (b), (c), 0, 0, 0)
#define MFMA16(a, b, c) __builtin_amdgcn_mfma_f32_16x16x32_bf16((a), (b), (c), 0, 0, 0)
template <int N> DI void waitv() { asm volatile("s_waitcnt vmcnt(%0)" ::"i"(N) : "memory"); }
#define WAITL0() asm volatile("s_waitcnt lgkmcnt(0)" ::: "memory")
#define BAR() do { __builtin_amdgcn_s_barrier(); SB0(); } while (0)
#define RLX_AGENT __ATOMIC_RELAXED, __HIP_MEMORY_SCOPE_AGENT
template <int V> using IC = std::integral_constant<int, V>;
template <int A, int B, typename F> DI void static_for(F&& f) {
    if constexpr (A < B) { f(IC<A>{}); static_for<A + 1, B>(f); }
}
DI f16x8 mk8h(unsigned a, unsigned b, unsigned c, unsigned d) { u32x4 v = {a, b, c, d}; return __builtin_bit_cast(f16x8, v); }
DI int rho(int a, int hh) { return (a & 3) + 8 * (a >> 2) + 4 * hh; }
DI float red8(float v) {
    v += __builtin_bit_cast(float, __builtin_amdgcn_update_dpp(0, __builtin_bit_cast(int, v), 0xB1, 0xf, 0xf, false));
    v += __builtin_bit_cast(float, __builtin_amdgcn_update_dpp(0, __builtin_bit_cast(int, v), 0x4E, 0xf, 0xf, false));
    v += __builtin_bit_cast(float, __builtin_amdgcn_update_dpp(0, __builtin_bit_cast(int, v), 0x141, 0xf, 0xf, false));
    return v;
}

DI unsigned cmulh(unsigned xu, unsigned tu) {
    f16x2 x = __builtin_bit_cast(f16x2, xu), tw = __builtin_bit_cast(f16x2, tu);
    f16x2 cc = {tw[0], tw[0]}, ss = {tw[1], -tw[1]}, xs = {x[1], x[0]};
    return __builtin_bit_cast(unsigned, __builtin_elementwise_fma(xs, ss, x * cc));
}
#define MFMA16H(a, b, c) __builtin_amdgcn_mfma_f32_16x16x32_f16((a), (b), (c), 0, 0, 0)
DI unsigned rotq(unsigned p) { return __builtin_amdgcn_alignbit(p, p, 16) ^ 0x00008000u; }
struct Dft24h { f16x8 P[3], Q[3]; unsigned tw[12]; };
DI void dft24h_setup(Dft24h& m, int lane) {
    const int col = lane & 31, hh = lane >> 5;
    unsigned p[12];
#pragma unroll
    for (int a = 0; a < 12; a++) {
        float an = (float)(rho(a, hh) * col) * (1.0f / 24.0f);
        an = __builtin_amdgcn_fractf(an + 0.01f) - 0.01f;
        p[a] = pack_f16(__builtin_amdgcn_cosf(an), __builtin_amdgcn_sinf(an));
        const float a2 = (float)(rho(a, hh) * col) * (1.0f / 576.0f);
        m.tw[a] = pack_f16(__builtin_amdgcn_cosf(a2), __builtin_amdgcn_sinf(a2));
    }
#pragma unroll
    for (int s = 0; s < 3; s++) {
        m.P[s] = mk8h(p[4 * s], p[4 * s + 1], p[4 * s + 2], p[4 * s + 3]);
        m.Q[s] = mk8h(rotq(p[4 * s]), rotq(p[4 * s + 1]), rotq(p[4 * s + 2]), rotq(p[4 * s + 3]));
    }
}
DI void dft24h_stage(const Dft24h& m, const unsigned (&h)[12], float (&re)[12], float (&im)[12]) {
    f32x16 dr = {0, 0, 0, 0, 0, 0, 0, 0, 0, 0, 0, 0, 0, 0, 0, 0}, di = dr;
#pragma unroll
    for (int s = 0; s < 3; s++) {
        f16x8 xh = mk8h(h[4 * s], h[4 * s + 1], h[4 * s + 2], h[4 * s + 3]);
        dr = MFMA32H(xh, m.P[s], dr); di = MFMA32H(xh, m.Q[s], di);
    }
#pragma unroll
    for (int a = 0; a < 12; a++) { re[a] = dr[a]; im[a] = di[a]; }
}
DI u32x4 mat16(const float (&ang)[4]) {
    u32x4 p;
#pragma unroll
    for (int e = 0; e < 4; e++) p[e] = pack_f16(0.25f * __builtin_amdgcn_cosf(ang[e]), 0.25f * __builtin_amdgcn_sinf(ang[e]));
    return p;
}
DI u32x4 rotq4(u32x4 p) { u32x4 q = {rotq(p[0]), rotq(p[1]), rotq(p[2]), rotq(p[3])}; return q; }
DI f16x8 ash(u32x4 v) { return __builtin_bit_cast(f16x8, v); }
template <int NW>
DI void wait_flags(GAS u64* f, u64 tag, int idx) {
    for (unsigned spins = 0; spins < (1u << 20); spins++) {
        bool ok = true;
#pragma unroll
        for (int k = 0; k < NW; k++) ok &= (__hip_atomic_load(f + idx + 64 * k, RLX_AGENT) == tag);
        if (__all(ok)) break;
        __builtin_amdgcn_s_sleep(2);
    }
    __builtin_amdgcn_fence(__ATOMIC_ACQUIRE, "wavefront");
}

__global__ __launch_bounds__(512, 2) void k_fused(const uint16_t* __restrict__ x, const uint16_t* __restrict__ wr, const uint16_t* __restrict__ wi,
                                                  const uint16_t* __restrict__ w1, const uint16_t* __restrict__ b1, const uint16_t* __restrict__ w2,
                                                  const uint16_t* __restrict__ b2, uint16_t* __restrict__ out, char* __restrict__ ws
                                                  ) {
    extern __shared__ __attribute__((aligned(16))) char smem_g[];
    LAS char* smem = (LAS char*)smem_g;
    const int tid = threadIdx.x, lane = tid & 63;
    const int w = __builtin_amdgcn_readfirstlane(tid >> 6);
    const int xg = blockIdx.x & 7, li = blockIdx.x >> 3;
    const int fs = (xg & 3) * 2 + (li & 1), t = (li >> 1) * 2 + (xg >> 2), tl = li >> 1;
    const int jb = 8 * t + fs;
    const unsigned lds0 = (unsigned)(uintptr_t)smem;
    const int rh = w & 1, ct = w >> 1;
    const __amdgpu_buffer_rsrc_t rs = __builtin_amdgcn_make_buffer_rsrc(ws, 0, WS_END, 0x00020000);
    STAMP(0);
    int sbk[5];
#pragma unroll
    for (int i = 0; i < 4; i++) sbk[i] = 2 * ((i + tl) & 3);
    sbk[4] = 8;
    const int rot9 = tl % 9;
    const char* w1b = (const char*)w1 + fs * 1152;
    const char* w2b = (const char*)w2 + (size_t)fs * 576 * 1152;
    const unsigned ldsw = lds0 + w * 1024;
    auto mkvo = [&](bool full, unsigned pitch) {
        int ln = tid; PIN(ln); ln &= 63;
        unsigned r = full ? 4 * w + (ln >> 4) : 8 * w + (ln >> 3);
        unsigned c = full ? ((((ln >> 2) & 3) ^ (ln >> 4)) * 64) + (ln & 3) * 16 : ((((ln >> 2) & 1) ^ ((ln >> 4) & 1)) * 64) + (ln & 3) * 16;
        return r * pitch + c;
    };
    unsigned vo;
    auto gl = [&](auto U_, auto J_) {
        constexpr int U = decltype(U_)::value, J = decltype(J_)::value;
        constexpr int DST = (U % 6) * RING_SLOT + J * 8192;
        if constexpr (U < 3) glds16<DST>(w1b + (unsigned)(8 * 128 + (192 * U + 64 * J) * 9216), vo, ldsw);
        else if constexpr (U < 27) glds16<DST>(w1b + (unsigned)__builtin_amdgcn_readfirstlane(sbk[(U - 3) / 6] * 128 + (96 * ((U - 3) % 6) + 32 * J) * 9216), vo, ldsw);
        else if constexpr (U < 30) glds16<DST>(w2b + (unsigned)__builtin_amdgcn_readfirstlane(rot9 * 128 + (192 * (U - 27) + 64 * J) * 1152), vo, ldsw);
    };
    auto setvo = [&](auto U_) {
        constexpr int U = decltype(U_)::value;
        if constexpr (U == 0) vo = mkvo(false, 9216);
        if constexpr (U == 3) vo = mkvo(true, 9216);
        if constexpr (U == 27) {
            int ln = tid; PIN(ln); ln &= 63;
            vo = (unsigned)(8 * w + (ln >> 3)) * 1152 + ((((ln >> 1) & 3) ^ ((ln >> 4) & 3)) * 32) + (ln & 1) * 16;
        }
    };
    auto gl3 = [&](auto U_) { setvo(U_); static_for<0, 3>([&](auto J_) { gl(U_, J_); }); };

    Dft24h m24;
    {
        const int i = lane >> 3, c = lane & 7;
        const char* xr = (const char*)x + (size_t)(8 * t + w) * 1152;
        const unsigned xo = (unsigned)i * (256 * 1152) + c * 16;
        u32x4 q[9];
        static_for<0, 9>([&](auto N_) {
            constexpr int n = decltype(N_)::value;
            q[n] = gload16<n * 128>(xr, xo);
        });
        SB0(); BAR();
        gl3(IC<0>{}); gl3(IC<1>{}); gl3(IC<2>{});
        const u32x2 ep = __builtin_amdgcn_raw_buffer_load_b64(rs, tid == 0 ? 0u : OOB, WS_EPOCH, 16);
        const bool dob = tid >= 384 && tid < 384 + 72;
        const u32x4 bb = *(const u32x4*)((const char*)b1 + fs * 1152 + (dob ? tid - 384 : 0) * 16);
        SB0();
        unsigned tw;
        {
            float an = (float)(((tid >> 4) & 15) * (8 * (tid & 15) + fs)) * (1.0f / 2048.0f);
            tw = pack_f16(__builtin_amdgcn_cosf(an), __builtin_amdgcn_sinf(an));
        }
        if (w >= 4) dft24h_setup(m24, lane);
        SB0();
        asm volatile("s_waitcnt vmcnt(11)" : "+v"(q[0]), "+v"(q[1]), "+v"(q[2]), "+v"(q[3]), "+v"(q[4]), "+v"(q[5]), "+v"(q[6]), "+v"(q[7]), "+v"(q[8]) :: "memory");
        STAMP(13);
        f32x2 d[36];
        f32x2 s2 = {0.f, 0.f};
#pragma unroll
        for (int n = 0; n < 9; n++)
#pragma unroll
            for (int e = 0; e < 4; e++) { f32x2 v = {bf_lo(q[n][e]), bf_hi(q[n][e])}; d[4 * n + e] = v; s2 += v; }
        const float mu = red8(s2[0] + s2[1]) * (1.0f / 576.0f);
        const f32x2 mu2 = {mu, mu};
        f32x2 q2 = {0.f, 0.f};
#pragma unroll
        for (int e = 0; e < 36; e++) { d[e] -= mu2; q2 = __builtin_elementwise_fma(d[e], d[e], q2); }
        const float var = red8(q2[0] + q2[1]) * (1.0f / 576.0f);
        const float m1 = rbf(mu);
        const float is1 = 1.0f / rbf(__builtin_sqrtf(rbf(rbf(var) + 1e-5f)));
        const float mean2 = (mu - m1) * is1;
        const float var2 = var * is1 * is1;
        const float is2 = 1.0f / rbf(__builtin_sqrtf(rbf(rbf(var2) + 1e-5f)));
        const float ya = is1 * is2, yb = (mean2 - rbf(mean2)) * is2;
        const f32x2 ya2 = {ya, ya}, yb2 = {yb, yb};
        const int mloc = 8 * w + i;
        LAS char* yw = smem + LDS_Y + (mloc >> 5) * 36 * 1024 + (c >> 1) * 1024 + (((mloc & 31) ^ c) + 32 * (c & 1)) * 16;
#pragma unroll
        for (int n = 0; n < 9; n++) {
            u32x4 o;
#pragma unroll
            for (int e = 0; e < 4; e++) { f32x2 v = __builtin_elementwise_fma(d[4 * n + e], ya2, yb2); o[e] = pack_bf16(v[0], v[1]); }
            *(LAS u32x4*)(yw + n * 4096) = o;
        }
        if (w == fs) {
            const f32x2 xa = {is1, is1}, xb = {mean2, mean2};
            const int pos = (i & 1) ? 4 + (i >> 1) : ((i >> 2) | (i & 2));
            LAS char* xw = smem + LDS_XS + pos * 1152 + c * 16;
#pragma unroll
            for (int n = 0; n < 9; n++) {
                u32x4 o;
#pragma unroll
                for (int e = 0; e < 4; e++) { f32x2 v = __builtin_elementwise_fma(d[4 * n + e], xa, xb); o[e] = pack_bf16(v[0], v[1]); }
                *(LAS u32x4*)(xw + n * 128) = o;
            }
        }
        if (dob) {
            f32x4 lo = {bf_lo(bb[0]), bf_hi(bb[0]), bf_lo(bb[1]), bf_hi(bb[1])}, hi = {bf_lo(bb[2]), bf_hi(bb[2]), bf_lo(bb[3]), bf_hi(bb[3])};
            *(LAS f32x4*)(smem + LDS_BIAS + (tid - 384) * 32) = lo;
            *(LAS f32x4*)(smem + LDS_BIAS + (tid - 384) * 32 + 16) = hi;
        }
        if (tid == 0) *(LAS u32x2*)(smem + LDS_TAG) = ep;
        if (tid < 256) *(LAS unsigned*)(smem + LDS_T256 + tid * 4) = tw;
        STAMP(14);
        waitv<0>();
        WAITL0();
    }
    BAR();
    STAMP(1);
    u64 epoch;
    {
        u32x2 e2 = *(LAS u32x2*)(smem + LDS_TAG);
        epoch = ((u64)(unsigned)__builtin_amdgcn_readfirstlane(e2[1]) << 32) | (unsigned)__builtin_amdgcn_readfirstlane(e2[0]);
    }
    const u64 tag = (epoch + 1) * 0x9E3779B97F4A7C15ull + 0x632BE59BD9B4E019ull;

    auto put_flag = [&](bool me, unsigned off, u64 val) {
        u32x2 o = {(unsigned)val, (unsigned)(val >> 32)};
        __builtin_amdgcn_raw_buffer_store_b64(o, rs, me ? 0u : OOB, off, 16);
    };
    LAS char* trc;
    auto settr = [&](auto U_) {
        constexpr int U = decltype(U_)::value;
        if constexpr (U == 0 || U == 3 || U == 27) {
            int ln = tid; PIN(ln); ln &= 63;
            int G = ln >> 4, q = (ln >> 2) & 3, p = ln & 3;
            int row = 8 * (G >> 1) + q;
            if constexpr (U == 3) trc = smem + row * 256 + ((ct ^ q) * 64) + (G & 1) * 32 + p * 8 + ((U % 6) / 2) * 2 * RING_SLOT;
            else if constexpr (U == 0) trc = smem + row * 128 + (((ct & 1) ^ ((q >> 1) & 1)) * 64) + (G & 1) * 32 + p * 8;
            else {
                row = 16 * (G >> 1) + 4 * (G & 1) + q;
                trc = smem + row * 128 + ((ct ^ ((row >> 1) & 3)) * 32) + p * 8 + ((U % 6) / 2) * 2 * RING_SLOT;
            }
            PIN(trc);
        } else if constexpr (U % 2 == 0) {
            trc += (U % 6 == 0) ? -4 * RING_SLOT : 2 * RING_SLOT;
            PIN(trc);
        }
    };
    bf16x8 af[36];
    auto load_y = [&]() {
        LAS char* ab = smem + LDS_Y + rh * 36 * 1024;
#pragma unroll
        for (int j = 0; j < 36; j++) af[j] = *(LAS bf16x8*)(ab + j * 1024 + (lane ^ (2 * (j & 3) + (lane >> 5))) * 16);
    };
    LAS char* biasp = smem + LDS_BIAS + (32 * ct + 4 * (lane >> 5)) * 4;
    f32x16 acc[2];
    s16x4 bl[6], bh[6];
    float gt[4];
    unsigned hq[5][8];
    auto ldbias = [&](auto P_, auto Q_, int blk) {
        constexpr int P = decltype(P_)::value, Q = decltype(Q_)::value;
        f32x4 b = *(LAS f32x4*)(biasp + blk * 256 + Q * 32);
        acc[P][4 * Q] = b[0]; acc[P][4 * Q + 1] = b[1]; acc[P][4 * Q + 2] = b[2]; acc[P][4 * Q + 3] = b[3];
    };
    auto gop = [&](auto C_, auto K_) {
        constexpr int C = decltype(C_)::value, K = decltype(K_)::value, P = C == 4 ? 1 : (C & 1);
        constexpr int NB = C == 4 ? 1 : C + 2;
        const float c0 = -2.0f * 0.7978845608028654f * 1.4426950408889634f;
        const float c1 = c0 * 0.044715f;
        if constexpr (K < 124) {
            constexpr int Q = K / 31, k = K % 31;
            if constexpr (k < 28) {
                constexpr int st = k / 4, e = k % 4;
                float xx = acc[P][4 * Q + e];
                if constexpr (st == 0) gt[e] = xx * xx;
                else if constexpr (st == 1) gt[e] = __builtin_fmaf(gt[e], c1, c0);
                else if constexpr (st == 2) gt[e] = xx * gt[e];
                else if constexpr (st == 3) gt[e] = __builtin_amdgcn_exp2f(gt[e]);
                else if constexpr (st == 4) gt[e] = 1.0f + gt[e];
                else if constexpr (st == 5) gt[e] = __builtin_amdgcn_rcpf(gt[e]);
                else gt[e] = xx * gt[e];
                PIN(gt[e]);
            } else if constexpr (k == 28) { hq[C][2 * Q] = pack_bf16(gt[0], gt[1]); PIN(hq[C][2 * Q]); }
            else if constexpr (k == 29) { hq[C][2 * Q + 1] = pack_bf16(gt[2], gt[3]); PIN(hq[C][2 * Q + 1]); }
            else { if constexpr (NB <= 3) ldbias(IC<P>{}, IC<Q>{}, sbk[NB]); }
        }
    };
    auto grange = [&](auto C_, auto A_, auto B_) {
        static_for<decltype(A_)::value, decltype(B_)::value>([&](auto K_) { gop(C_, K_); });
    };
    const f32x16 zero16 = {0, 0, 0, 0, 0, 0, 0, 0, 0, 0, 0, 0, 0, 0, 0, 0};
    auto bfrag = [&](auto J_) {
        constexpr int J = decltype(J_)::value;
        return __builtin_bit_cast(bf16x8, __builtin_shufflevector(bl[J], bh[J], 0, 1, 2, 3, 4, 5, 6, 7));
    };
    auto fslot = [&](auto U_, auto LO_, auto GI_, auto&& fill) {
        constexpr int U = decltype(U_)::value, v = U - 3, c = v / 6, s = v % 6;
        constexpr bool LO = decltype(LO_)::value;
        constexpr int PB = c & 1;
        constexpr int PA = s == 0 ? PB ^ 1 : PB;
        constexpr int sp = (s + 5) % 6;
        settr(U_);
        LAS char* sb = trc + (U % 2) * RING_SLOT;
        setvo(GI_);
        static_for<0, 6>([&](auto G_) {
            constexpr int G = decltype(G_)::value;
            if constexpr (G < 3) {
                if constexpr (LO) acc[PA] = MFMA32(bfrag(IC<G + 3>{}), af[6 * sp + G + 3], acc[PA]);
            } else {
                acc[PB] = MFMA32(bfrag(IC<G - 3>{}), af[6 * s + G - 3], acc[PB]);
            }
            SB0();
            bl[G] = tr_rd(sb + G * 4096); bh[G] = tr_rd(sb + G * 4096 + 1024);
            if constexpr (G < 3) gl(GI_, G_);
            fill(G_);
            SB0();
        });
    };
    auto hslot = [&](auto U_, auto PB_, auto LO_, auto&& fill) {
        constexpr int U = decltype(U_)::value, ph = U / 27, s = U % 3, PB = decltype(PB_)::value;
        constexpr int PA = s == 0 ? PB ^ 1 : PB;
        constexpr int R2 = ph == 0 ? 512 : 1024;
        settr(U_);
        LAS char* sb = trc + (U % 2) * RING_SLOT;
        static_for<0, 12>([&](auto G_) {
            constexpr int G = decltype(G_)::value;
            if constexpr (G < 3) {
                if constexpr (decltype(LO_)::value) acc[PA] = MFMA32(bfrag(IC<G + 3>{}), af[(12 * s + 33) % 36 + G], acc[PA]);
            } else {
                acc[PB] = MFMA32(bfrag(IC<(G - 3) % 6>{}), af[12 * s + G - 3], (ph == 1 && s == 0 && G == 3) ? zero16 : acc[PB]);
            }
            SB0();
            bl[G % 6] = tr_rd(sb + G * 2048); bh[G % 6] = tr_rd(sb + G * 2048 + R2);
            fill(G_);
            SB0();
        });
    };
    auto iss2 = [&](auto GI_, auto GI2_) {
        return [&, GI_, GI2_](auto G_) {
            constexpr int G = decltype(G_)::value;
            if constexpr (G == 0) setvo(GI_);
            if constexpr (G < 6 && G % 2 == 0) gl(GI_, IC<G / 2>{});
            if constexpr (G >= 6 && G % 2 == 0) gl(GI2_, IC<G / 2 - 3>{});
        };
    };
    using T = std::true_type; using F = std::false_type;
    auto nofill = [&](auto) {};

    if (w < 4) {
        load_y();
        static_for<0, 4>([&](auto Q_) { ldbias(IC<1>{}, Q_, sbk[4]); ldbias(IC<0>{}, Q_, sbk[0]); });
        hslot(IC<0>{}, IC<1>{}, F{}, nofill); WAITL0(); BAR();
        hslot(IC<1>{}, IC<1>{}, T{}, iss2(IC<3>{}, IC<4>{})); BAR();
        hslot(IC<2>{}, IC<1>{}, T{}, iss2(IC<5>{}, IC<99>{})); waitv<6>(); BAR();
        STAMP(9);
        static_for<3, 9>([&](auto U_) {
            constexpr int U = decltype(U_)::value, s = U - 3;
            fslot(U_, T{}, IC<U + 3>{}, [&](auto G_) {
                constexpr int gi = 6 * s + decltype(G_)::value - 4;
                if constexpr (gi >= 0) grange(IC<4>{}, IC<(gi * 124) / 32>{}, IC<((gi + 1) * 124) / 32>{});
            });
            waitv<6>(); BAR();
        });
    } else {
        const int v = w - 4;
        const unsigned PP = 0x3F803F80u, PM = 0xBF803F80u  , MP = 0x3F80BF80u  , MM = 0xBF80BF80u;
        unsigned A0, A1, A2, A3, B0, B1, B2, B3; float cc;
        const float c8 = 0.70710678118654752f;
        if (v == 0) { A0 = PP; A1 = PP; A2 = PP; A3 = PP; B0 = PP; B1 = PP; B2 = MM; B3 = MM; cc = 1.f; }
        else if (v == 1) { A0 = PM; A1 = 0; A2 = PM; A3 = MP; B0 = 0; B1 = MP; B2 = MM; B3 = PP; cc = c8; }
        else if (v == 2) { A0 = PP; A1 = MM; A2 = 0; A3 = 0; B0 = 0; B1 = 0; B2 = MP; B3 = MP; cc = 1.f; }
        else { A0 = PM; A1 = 0; A2 = MP; A3 = PM; B0 = 0; B1 = PM; B2 = MM; B3 = PP; cc = c8; }
        float re[12], im[12];
        unsigned hw_[12];
        auto dot = [](unsigned a, unsigned b, float c) { return __builtin_amdgcn_fdot2_f32_bf16(__builtin_bit_cast(bf16x2, a), __builtin_bit_cast(bf16x2, b), c, false); };
        {
            const int G = lane >> 4, q = (lane >> 2) & 3, p = lane & 3;
            LAS char* xb = smem + LDS_XS + q * 1152 + (96 * (G >> 1) + 16 * (G & 1) + 4 * p) * 2;
            u32x2 ev[12], od[12];
#pragma unroll
            for (int a = 0; a < 12; a++) {
                ev[a] = __builtin_bit_cast(u32x2, tr_rd(xb + rho(a, 0) * 48));
                od[a] = __builtin_bit_cast(u32x2, tr_rd(xb + rho(a, 0) * 48 + 4 * 1152));
            }
            SB0();
            load_y();
            SB0();
#pragma unroll
            for (int a = 0; a < 12; a++) {
                float r_ = dot(ev[a][0], A0, dot(ev[a][1], A1, cc * dot(od[a][0], A2, dot(od[a][1], A3, 0.f))));
                float i_ = dot(ev[a][0], B0, dot(ev[a][1], B1, cc * dot(od[a][0], B2, dot(od[a][1], B3, 0.f))));
                hw_[a] = pack_f16(r_, i_);
            }
#pragma unroll
            for (int a = 0; a < 12; a++) PIN(hw_[a]);
            SB0();
        }
        WAITL0(); SB0(); STAMPB(0); BAR();
        dft24h_stage(m24, hw_, re, im);
        gl3(IC<3>{}); gl3(IC<4>{});
#pragma unroll
        for (int a = 0; a < 12; a++) { hw_[a] = cmulh(pack_f16(re[a], im[a]), m24.tw[a]); PIN(hw_[a]); }
        SB0(); STAMPB(1); BAR();
        dft24h_stage(m24, hw_, re, im);
        gl3(IC<5>{});
        {
            const int hh = lane >> 5;
            const unsigned zo = (lane & 31) < 24 ? (unsigned)(((lane & 31) * 24 + 4 * hh) * 4) : OOB;
            const unsigned zb = WS_ZV + (unsigned)(jb * 4 + v) * ZV_PITCH;
            unsigned z0 = 0;
#pragma unroll
            for (int qd = 0; qd < 3; qd++) {
                u32x4 o;
#pragma unroll
                for (int e = 0; e < 4; e++) o[e] = pack_f16(re[4 * qd + e] * (1.0f / 64.0f), im[4 * qd + e] * (1.0f / 64.0f));
                if (qd == 0) z0 = o[0];
                __builtin_amdgcn_raw_buffer_store_b128(o, rs, zo, zb + qd * 32, 16);
            }
            __builtin_amdgcn_raw_buffer_store_b32(z0, rs, lane == 0 ? 0u : OOB, zb + 576 * 4, 16);
        }
        static_for<0, 4>([&](auto Q_) { ldbias(IC<0>{}, Q_, sbk[0]); ldbias(IC<1>{}, Q_, sbk[1]); });
        STAMPB(2); waitv<10>(); BAR();
        static_for<3, 9>([&](auto U_) {
            constexpr int U = decltype(U_)::value;
            if constexpr (U == 3) fslot(U_, F{}, IC<U + 3>{}, nofill); else fslot(U_, T{}, IC<U + 3>{}, nofill);
            if constexpr (U <= 4) waitv<10>(); else waitv<6>();
            BAR();
            if constexpr (U == 5) __builtin_amdgcn_raw_buffer_store_b32((unsigned)(tag >> 32), rs, tid == 256 ? 0u : OOB, WS_ROWF + jb * 4, 16);
        });
    }
    STAMP(10);
    u32x4 rf;
    const unsigned tg = (unsigned)(tag >> 32);
    const int wks = t == 31 ? 273 + ((fs + 1) & 1) : 9 * t - ((fs + 9 * t) & 1);
    static_for<9, 27>([&](auto U_) {
        constexpr int U = decltype(U_)::value, c = (U - 3) / 6, s = (U - 3) % 6;
        if constexpr (U == 25) {
            asm volatile("" : "+v"(rf));
            for (unsigned spins = 0; spins < (1u << 20); spins++) {
                if (__all(rf[0] == tg && rf[1] == tg && rf[2] == tg && rf[3] == tg)) break;
                __builtin_amdgcn_s_sleep(8);
                asm volatile("" ::: "memory");
                rf = __builtin_amdgcn_raw_buffer_load_b128(rs, lane * 16, WS_ROWF, 16);
            }
            SB0();
        }
        fslot(U_, T{}, IC<(U < 24 ? U + 3 : 99)>{}, [&](auto G_) {
            constexpr int G = decltype(G_)::value;
            constexpr int gi = 6 * s + G - 4;
            if constexpr (gi >= 0) grange(IC<c - 1>{}, IC<(gi * 124) / 32>{}, IC<((gi + 1) * 124) / 32>{});
            if constexpr (U == 21 && G == 4) {
                int ln = tid; PIN(ln); ln &= 63;
                rf = gload16<WS_ROWF>(ws, ln * 16);
            }
            if constexpr (U == 23 && G >= 3) { if constexpr (G == 3) setvo(IC<27>{}); gl(IC<27>{}, IC<G - 3>{}); }
            if constexpr (U == 24 && G < 2) {
                int ln = tid; PIN(ln); ln &= 63;
                const int pos = ((w & 3) * 2 + G) * 32 + (ln >> 1), qq = 64 * (pos & 3) + (pos >> 2);
                glds16r((const char*)(w < 4 ? wr : wi) + (fs * 289 + wks) * 2, (unsigned)qq * (8 * 289 * 2) + (ln & 1) * 16, lds0 + LDS_WS + (w * 2 + G) * 1024);
            }
            if constexpr (U == 25 && G < 3) {
                int ln = tid; PIN(ln); ln &= 63;
                const int un = ((w & 3) * 3 + G) * 64 + ln, pos = un / 3, part = un - 3 * pos, j = 64 * (pos & 3) + (pos >> 2);
                const bool use = w < 4 ? fs <= 4 : (fs == 0 || fs >= 4);
                const int vv = w < 4 ? (fs <= 3 ? fs : 0) : (fs >= 5 ? 8 - fs : 0);
                const unsigned so = WS_ZV + vv * ZV_PITCH + (w < 4 ? 9 * t : 565 - 9 * t) * 4;
                glds16s(ws + so, use ? (unsigned)j * (4 * ZV_PITCH) + part * 16 : 0u, lds0 + LDS_ZS + (w * 3 + G) * 1024);
            }
        });
        if constexpr (U == 21 || U == 22) { waitv<7>(); BAR(); }
        else if constexpr (U == 23) { waitv<9>(); BAR(); }
        else if constexpr (U == 24) { waitv<8>(); BAR(); }
        else if constexpr (U == 25) { waitv<8>(); BAR(); }
        else if constexpr (U < 26) { waitv<6>(); BAR(); }
    });
    static_for<0, 3>([&](auto G_) { constexpr int G = decltype(G_)::value; acc[1] = MFMA32(bfrag(IC<G + 3>{}), af[33 + G], acc[1]); });
    STAMP(2);
    {
        int ln = tid; PIN(ln); ln &= 63;
        const int r = fs, col = ln & 15, g = ln >> 4, cc = col < 10 ? col : 9;
        const f32x4 z4 = {0.f, 0.f, 0.f, 0.f};
        u32x4 P1, Q1, P3, Q3, P4, Q4;
        {
            float an[4];
#pragma unroll
            for (int e = 0; e < 4; e++) an[e] = (float)(((4 * g + e) * (8 * col + r)) & 127) * (1.0f / 128.0f) + (r == 4 ? 0.25f : 0.f);
            P1 = mat16(an); Q1 = rotq4(P1);
            PIN(P1); PIN(Q1);
        }
        SB0();
        grange(IC<3>{}, IC<0>{}, IC<62>{});
        STAMPB(3);
        WAITL0();
        waitv<0>();
        STAMPB(4);
        BAR();
        u32x4 za[2] = {{0u, 0u, 0u, 0u}, {0u, 0u, 0u, 0u}}, zb[2] = {{0u, 0u, 0u, 0u}, {0u, 0u, 0u, 0u}}, tf[2];
        u16x2 wp[2][4];
        {
            LAS char* zp = smem + LDS_ZS + (8 * w + g) * 48;
            LAS char* wq = smem + LDS_WS + (8 * w + g) * 32 + (9 * t - wks + cc) * 2;
            if (r <= 4) {
#pragma unroll
                for (int tl_ = 0; tl_ < 2; tl_++)
#pragma unroll
                    for (int e = 0; e < 4; e++) za[tl_][e] = *(LAS unsigned*)(zp + (16 * e + tl_) * 192 + cc * 4);
            }
            if (r == 0 || r >= 4) {
#pragma unroll
                for (int tl_ = 0; tl_ < 2; tl_++)
#pragma unroll
                    for (int e = 0; e < 4; e++) zb[tl_][e] = *(LAS unsigned*)(zp + 12288 + (16 * e + tl_) * 192 + (11 - cc) * 4);
            }
#pragma unroll
            for (int tl_ = 0; tl_ < 2; tl_++) {
                tf[tl_] = *(LAS u32x4*)(smem + LDS_T256 + ((2 * w + tl_) * 16 + 4 * g) * 4);
#pragma unroll
                for (int e = 0; e < 4; e++) {
                    wp[tl_][e][0] = *(LAS unsigned short*)(wq + (16 * e + tl_) * 128);
                    wp[tl_][e][1] = *(LAS unsigned short*)(wq + 8192 + (16 * e + tl_) * 128);
                }
            }
        }
        SB0();
        LAS char* hw = smem + LDS_H + ((2 * rh + ((ln >> 4) & 1)) * 18 + ct) * 1024 + ((ln & 15) + 16 * (ln >> 5)) * 16;
        auto hst = [&](auto C_) {
            constexpr int C = decltype(C_)::value;
            u32x4 o0 = {hq[C][0], hq[C][1], hq[C][2], hq[C][3]}, o1 = {hq[C][4], hq[C][5], hq[C][6], hq[C][7]};
            *(LAS u32x4*)(hw + sbk[C] * 2048) = o0; *(LAS u32x4*)(hw + sbk[C] * 2048 + 512) = o1;
        };
        if (w < 4) hst(IC<4>{});
        hst(IC<0>{}); SB0();
        grange(IC<3>{}, IC<62>{}, IC<83>{}); SB0();
        hst(IC<1>{}); SB0();
        grange(IC<3>{}, IC<83>{}, IC<104>{}); SB0();
        hst(IC<2>{}); SB0();
        grange(IC<3>{}, IC<104>{}, IC<124>{}); SB0();
        put_flag(jb == 0 && tid == 0, WS_EPOCH, epoch + 1);
        STAMPB(8);
        const int xwo = ((4 * g * 4 + (w >> 1)) * 10 + cc) * 16 + (w & 1) * 8;
        const int xro = ((2 * w * 4 + g) * 10 + cc) * 16;
        f32x4 dr[2], di[2];
        {
            const unsigned fl = r == 4 ? 0x00008000u : 0x80000000u;
            const u32x4 P2 = P1 ^ fl, Q2 = Q1 ^ fl;
#pragma unroll
            for (int tl_ = 0; tl_ < 2; tl_++) {
                dr[tl_] = MFMA16H(ash(P1), ash(za[tl_]), z4); di[tl_] = MFMA16H(ash(Q1), ash(za[tl_]), z4);
                dr[tl_] = MFMA16H(ash(P2), ash(zb[tl_]), dr[tl_]); di[tl_] = MFMA16H(ash(Q2), ash(zb[tl_]), di[tl_]);
            }
            hst(IC<3>{});
            u16x2 mx = {0, 0};
#pragma unroll
            for (int tl_ = 0; tl_ < 2; tl_++)
#pragma unroll
                for (int e = 0; e < 4; e++) mx = __builtin_elementwise_max(mx, __builtin_bit_cast(u16x2, __builtin_bit_cast(unsigned, wp[tl_][e]) & 0x7fff7fffu));
            unsigned m1 = mx[0] > mx[1] ? mx[0] : mx[1];
            { auto q = __builtin_amdgcn_permlane16_swap(m1, m1, false, false); m1 = q[0] > q[1] ? q[0] : q[1]; }
            { auto q = __builtin_amdgcn_permlane32_swap(m1, m1, false, false); m1 = q[0] > q[1] ? q[0] : q[1]; }
            *(LAS unsigned*)(smem + LDS_WMAX + col * 32 + w * 4) = m1;
            u32x2 o[4];
#pragma unroll
            for (int e = 0; e < 4; e++)
#pragma unroll
                for (int tl_ = 0; tl_ < 2; tl_++) o[e][tl_] = cmulh(pack_f16(dr[tl_][e], di[tl_][e]), tf[tl_][e]);
            if (col < 10) {
#pragma unroll
                for (int e = 0; e < 4; e++) *(LAS u32x2*)(smem + LDS_XA + xwo + e * 640) = o[e];
            }
            SB0();
            float an[4];
#pragma unroll
            for (int e = 0; e < 4; e++) an[e] = (float)(((4 * g + e) * col) & 15) * (1.0f / 16.0f);
            P3 = mat16(an); Q3 = rotq4(P3);
            PIN(P3); PIN(Q3);
            WAITL0(); STAMPB(5); BAR();
        }
        float inv;
        {
            const u32x4 xa0 = *(LAS u32x4*)(smem + LDS_XA + xro), xa1 = *(LAS u32x4*)(smem + LDS_XA + xro + 640);
            const u32x4 m0 = *(LAS u32x4*)(smem + LDS_WMAX + col * 32), m1 = *(LAS u32x4*)(smem + LDS_WMAX + col * 32 + 16);
            u32x2 tt[4];
#pragma unroll
            for (int e = 0; e < 4; e++) tt[e] = *(LAS u32x2*)(smem + LDS_T256 + ((4 * g + e) * 16 + 2 * w) * 4);
            SB0();
            {
                LAS char* ab = smem + LDS_H + 2 * rh * 18 * 1024 + ln * 16;
#pragma unroll
                for (int j = 0; j < 18; j++) { af[j] = *(LAS bf16x8*)(ab + j * 1024); af[18 + j] = *(LAS bf16x8*)(ab + (18 + j) * 1024); }
            }
            SB0();
            dr[0] = MFMA16H(ash(P3), ash(xa0), z4); di[0] = MFMA16H(ash(Q3), ash(xa0), z4);
            dr[1] = MFMA16H(ash(P3), ash(xa1), z4); di[1] = MFMA16H(ash(Q3), ash(xa1), z4);
            unsigned ex = max(max(max(m0[0], m0[1]), max(m0[2], m0[3])), max(max(m1[0], m1[1]), max(m1[2], m1[3]))) >> 7;
            ex = min(max(ex, 1u), 252u);
            const float wsc = __uint_as_float((253u - ex) << 23);
            inv = __uint_as_float((ex + 1u) << 23);
#pragma unroll
            for (int tl_ = 0; tl_ < 2; tl_++) {
                u32x4 y;
#pragma unroll
                for (int e = 0; e < 4; e++) {
                    const unsigned wv = __builtin_bit_cast(unsigned, wp[tl_][e]);
                    const float wre = bf_lo(wv) * wsc, wim = bf_hi(wv) * wsc;
                    y[e] = pack_f16(dr[tl_][e] * wre - di[tl_][e] * wim, -(dr[tl_][e] * wim + di[tl_][e] * wre));
                }
                dr[tl_] = MFMA16H(ash(P3), ash(y), z4); di[tl_] = MFMA16H(ash(Q3), ash(y), z4);
            }
            float an[4];
#pragma unroll
            for (int e = 0; e < 4; e++) an[e] = (float)((col * (8 * (4 * g + e) + r)) & 127) * (1.0f / 128.0f);
            P4 = mat16(an); Q4 = rotq4(P4);
            PIN(P4); PIN(Q4);
            if (col < 10) {
#pragma unroll
                for (int e = 0; e < 4; e++) {
                    u32x2 o = {cmulh(pack_f16(dr[0][e], di[0][e]), tt[e][0]), cmulh(pack_f16(dr[1][e], di[1][e]), tt[e][1])};
                    *(LAS u32x2*)(smem + LDS_XB + xwo + e * 640) = o;
                }
            }
            WAITL0();
            gl3(IC<28>{});
            STAMPB(6); BAR();
        }
        {
            const u32x4 xa0 = *(LAS u32x4*)(smem + LDS_XB + xro), xa1 = *(LAS u32x4*)(smem + LDS_XB + xro + 640);
            const float fsc = (64.0f * 256.0f / (2048.0f * 576.0f)) * ((r & 3) == 0 ? 0.5f : 1.0f);
            const unsigned vo_ = col < 10 ? (unsigned)(64 * g * 80 + col * 8) : OOB;
            const unsigned vb = WS_VP + (unsigned)(r * 32 + t) * (256 * 80) + w * 2 * 80;
            dr[0] = MFMA16H(ash(P4), ash(xa0), z4); di[0] = MFMA16H(ash(Q4), ash(xa0), z4);
            dr[1] = MFMA16H(ash(P4), ash(xa1), z4); di[1] = MFMA16H(ash(Q4), ash(xa1), z4);
#pragma unroll
            for (int tl_ = 0; tl_ < 2; tl_++)
#pragma unroll
                for (int e = 0; e < 4; e++) {
                    u32x2 o = {__float_as_uint(dr[tl_][e] * fsc * inv), __float_as_uint(di[tl_][e] * fsc * inv)};
                    __builtin_amdgcn_raw_buffer_store_b64(o, rs, vo_, vb + (16 * e + tl_) * 80, 16);
                }
            SB0();
        }
    }
    STAMP(3);
    f32x4 ac[2][2];
    unsigned plane;
    { int ln = tid; PIN(ln); plane = (ln & 63) * 16; }
    const unsigned pbase = WS_PART + (unsigned)((fs * 32 + t) * 72 + w) * 1024;
    auto pstore = [&](auto P_, int blk) {
        constexpr int P = decltype(P_)::value;
        unsigned a0 = pack_bf16(ac[P][0][0], ac[P][0][1]), a1 = pack_bf16(ac[P][0][2], ac[P][0][3]);
        unsigned b0 = pack_bf16(ac[P][1][0], ac[P][1][1]), b1_ = pack_bf16(ac[P][1][2], ac[P][1][3]);
        auto s0 = __builtin_amdgcn_permlane16_swap(a0, b0, false, false);
        auto s1 = __builtin_amdgcn_permlane16_swap(a1, b1_, false, false);
        u32x4 o = {s0[0], s1[0], s0[1], s1[1]};
        __builtin_amdgcn_raw_buffer_store_b128(o, rs, plane, pbase + blk * 8192, 16);
    };
    const f32x4 zero4 = {0.f, 0.f, 0.f, 0.f};
    auto qslot = [&](auto U_, auto LO_, auto&& fill) {
        constexpr int U = decltype(U_)::value, s = U % 3, PB = ((U / 3) & 1);
        constexpr int PA = s == 0 ? PB ^ 1 : PB;
        constexpr int sp = (s + 2) % 3;
        settr(U_);
        LAS char* sb = trc + (U % 2) * RING_SLOT;
        static_for<0, 6>([&](auto G_) {
            constexpr int G = decltype(G_)::value;
            if constexpr (G < 3) {
                if constexpr (decltype(LO_)::value) {
                    ac[PA][0] = MFMA16(bfrag(IC<G + 3>{}), af[6 * sp + G + 3], ac[PA][0]);
                    ac[PA][1] = MFMA16(bfrag(IC<G + 3>{}), af[18 + 6 * sp + G + 3], ac[PA][1]);
                }
            } else {
                ac[PB][0] = MFMA16(bfrag(IC<G - 3>{}), af[6 * s + G - 3], (s == 0 && G == 3) ? zero4 : ac[PB][0]);
                ac[PB][1] = MFMA16(bfrag(IC<G - 3>{}), af[18 + 6 * s + G - 3], (s == 0 && G == 3) ? zero4 : ac[PB][1]);
            }
            SB0();
            bl[G] = tr_rd(sb + G * 4096); bh[G] = tr_rd(sb + G * 4096 + 1024);
            fill(G_);
            SB0();
        });
    };
    auto g2 = [&](auto RS_, auto S_, auto J_, int blk) {
        constexpr int RS = decltype(RS_)::value, S = decltype(S_)::value, J = decltype(J_)::value;
        glds16<RS * RING_SLOT + J * 8192>(w2b + (unsigned)__builtin_amdgcn_readfirstlane(blk * 128 + (192 * S + 64 * J) * 1152), vo, ldsw);
    };
    int cprev = rot9, cn = rot9 == 8 ? 0 : rot9 + 1;
    BAR();
    qslot(IC<27>{}, F{}, [&](auto G_) {
        constexpr int G = decltype(G_)::value;
        if constexpr (G < 3) gl(IC<29>{}, G_); else g2(IC<0>{}, IC<0>{}, IC<G - 3>{}, cn);
    }); waitv<14>(); BAR();
    qslot(IC<28>{}, T{}, [&](auto G_) { constexpr int G = decltype(G_)::value; if constexpr (G < 3) g2(IC<1>{}, IC<1>{}, G_, cn); }); waitv<6>(); BAR();
    qslot(IC<29>{}, T{}, [&](auto G_) { constexpr int G = decltype(G_)::value; if constexpr (G < 3) g2(IC<2>{}, IC<2>{}, G_, cn); }); waitv<6>(); BAR();
    __builtin_amdgcn_raw_buffer_store_b32(tg, rs, tid == 0 ? 0u : OOB, WS_COLF + (fs * 32 + t) * 4, 16);
    STAMP(11);
    u32x4 cf = {0u, 0u, 0u, 0u};
    for (int it = 0; it < 4; it++) {
        cn = cn == 8 ? 0 : cn + 1;
        static_for<30, 33>([&](auto U_) {
            constexpr int U = decltype(U_)::value;
            if constexpr (U == 31) { if (it == 3) asm volatile("global_load_dwordx4 %0, %1, %2 offset:%3" : "+v"(cf) : "v"(plane), "s"(ws), "i"(WS_COLF) : "memory"); }
            qslot(U_, T{}, [&](auto G_) {
                constexpr int G = decltype(G_)::value;
                if constexpr (G < 3) g2(IC<3 + U % 3>{}, IC<U % 3>{}, G_, cn);
                if constexpr (U == 30 && G == 5) pstore(IC<1>{}, cprev);
            });
            waitv<7>(); BAR();
        });
        cprev = cprev == 8 ? 0 : cprev + 1;
        cn = cn == 8 ? 0 : cn + 1;
        if (it < 3) {
            static_for<33, 36>([&](auto U_) {
                constexpr int U = decltype(U_)::value;
                qslot(U_, T{}, [&](auto G_) {
                    constexpr int G = decltype(G_)::value;
                    if constexpr (G < 3) g2(IC<U % 3>{}, IC<U % 3>{}, G_, cn);
                    if constexpr (U == 33 && G == 5) pstore(IC<0>{}, cprev);
                });
                waitv<7>(); BAR();
            });
            cprev = cprev == 8 ? 0 : cprev + 1;
        }
    }
    qslot(IC<33>{}, T{}, [&](auto G_) {
        constexpr int G = decltype(G_)::value;
        if constexpr (G == 5) pstore(IC<0>{}, cprev);
    });
    waitv<4>(); BAR();
    cprev = cprev == 8 ? 0 : cprev + 1;
    asm volatile("" : "+v"(cf));
    for (unsigned spins = 0; spins < (1u << 20); spins++) {
        if (__all(cf[0] == tg && cf[1] == tg && cf[2] == tg && cf[3] == tg)) break;
        __builtin_amdgcn_s_sleep(8);
        asm volatile("" ::: "memory");
        cf = __builtin_amdgcn_raw_buffer_load_b128(rs, plane, WS_COLF, 16);
    }
    SB0();
    qslot(IC<34>{}, T{}, [&](auto G_) {
        constexpr int G = decltype(G_)::value;
        if constexpr (G == 0) {
            int ln = tid; PIN(ln);
            const int row = ln / 72, cc = ln % 72;
            glds16r((const char*)x, (unsigned)(jb + 256 * row) * 1152 + cc * 16, lds0 + LDS_XR + w * 1024);
        }
        if constexpr (G == 1) {
            int ln = tid; PIN(ln); ln &= 63;
            const int un = 512 + ln, row = un / 72, cc = un % 72;
            const char* src = w == 0 ? (const char*)x : (const char*)b2;
            const unsigned off = w == 0 ? (unsigned)(jb + 256 * row) * 1152 + cc * 16 : (w == 1 ? 0u : 128u) + ln * 16;
            glds16r(src, off, lds0 + LDS_XR + (w == 0 ? 8192 : w == 1 ? 9216 : 9216 + 128));
        }
        if constexpr (G >= 2 && G < 5) {
            int ln = tid; PIN(ln); ln &= 63;
            int un = (w * 3 + G - 2) * 64 + ln; if (un > 1279) un = 1279;
            const int seg = un / 5, part = un - 5 * seg;
            glds16s(ws + WS_VP + jb * 80, (unsigned)seg * (256 * 80) + part * 16, lds0 + LDS_VS + (w * 3 + G - 2) * 1024);
        }
    });
    waitv<6>(); BAR();
    qslot(IC<35>{}, T{}, nofill);
    static_for<0, 3>([&](auto G_) {
        constexpr int G = decltype(G_)::value;
        ac[1][0] = MFMA16(bfrag(IC<G + 3>{}), af[15 + G], ac[1][0]);
        ac[1][1] = MFMA16(bfrag(IC<G + 3>{}), af[33 + G], ac[1][1]);
    });
    pstore(IC<1>{}, cprev);
    STAMP(4);
    {
    int ln = tid; PIN(ln);
    waitv<1>(); BAR();
    const int l8 = ln & 255;
    if (w < 4) {
        waitv<0>();
        put_flag((ln & 63) == 0, WS_PARTF + (t * 64 + fs * 8 + w) * 8, tag);
        STAMP(5);
        wait_flags<1>((GAS u64*)(ws + WS_PARTF) + t * 64, tag, ln & 63);
        STAMP(6);
        u32x4 pv[3][8];
#pragma unroll
        for (int rep = 0; rep < 3; rep++) {
            const int pid = l8 + 256 * rep;
            const int i = pid & 7, pc = pid >> 3;
            const unsigned po = (unsigned)((pc >> 3) * 8 + ((pc >> 1) & 3) * 2 + (fs >> 2)) * 1024 + (16 * (2 * (pc & 1) + ((fs >> 1) & 1)) + 8 * (fs & 1) + i) * 16;
            if (rep < 2 || w == 0) {
#pragma unroll
                for (int f = 0; f < 8; f++) pv[rep][f] = __builtin_amdgcn_raw_buffer_load_b128(rs, po, WS_PART + (unsigned)(f * 32 + t) * 73728, 16);
            }
        }
        SB0(); BAR();
#pragma unroll
        for (int rep = 0; rep < 3; rep++) {
            if (rep < 2 || w == 0) {
                const int pid = l8 + 256 * rep;
                const int i = pid & 7, pc = pid >> 3;
                const u32x4 bq = *(LAS u32x4*)(smem + LDS_XR + 9216 + pc * 16);
                float gv[8];
#pragma unroll
                for (int e = 0; e < 8; e++) {
                    float o = 0.f;
#pragma unroll
                    for (int f = 0; f < 8; f++) o += (e & 1) ? bf_hi(pv[rep][f][e >> 1]) : bf_lo(pv[rep][f][e >> 1]);
                    const float bv = (e & 1) ? bf_hi(bq[e >> 1]) : bf_lo(bq[e >> 1]);
                    const float ov = rbf(rbf(o) + bv);
                    gv[e] = ov * __builtin_amdgcn_rcpf(1.0f + __builtin_amdgcn_exp2f(-1.4426950408889634f * ov));
                }
                u32x4 oo = {pack_bf16(gv[0], gv[1]), pack_bf16(gv[2], gv[3]), pack_bf16(gv[4], gv[5]), pack_bf16(gv[6], gv[7])};
                *(LAS u32x4*)(smem + LDS_G + i * 1168 + pc * 16) = oo;
            }
        }
        STAMP(15);
    } else {
        const int l6 = ln & 63, v = w - 4, hh = l6 >> 5, rB = v == 0 ? 4 : 8 - v;
        int n2 = l6 & 31; if (n2 > 23) n2 = 23;
        STAMPB(11);
        u32x2 A[12], B[12];
#pragma unroll
        for (int a = 0; a < 12; a++) {
            const int k = 24 * rho(a, hh) + n2;
            const bool mir = k > 288; const int kk = mir ? 576 - k : k;
            int gq = (kk * 57) >> 9; if (gq > 31) gq = 31;
            LAS char* vp = smem + LDS_VS + (kk + gq) * 8;
            A[a] = *(LAS u32x2*)(vp + ((v != 0 && mir) ? rB : v) * 2560);
            B[a] = *(LAS u32x2*)(vp + rB * 2560);
        }
        SB0();
        waitv<0>();
        put_flag(l6 == 0, WS_PARTF + (t * 64 + fs * 8 + w) * 8, tag);
        Dft24h mi; dft24h_setup(mi, l6);
        SB0();
        float re[12], im[12];
        float mx = 0.f;
#pragma unroll
        for (int a = 0; a < 12; a++) {
            const int k = 24 * rho(a, hh) + n2;
            const bool mir = k > 288, spec = (k == 0 || k == 288);
            float ax = __uint_as_float(A[a][0]), ay = __uint_as_float(A[a][1]), bx = __uint_as_float(B[a][0]), by = __uint_as_float(B[a][1]);
            if (mir) { ay = -ay; by = -by; }
            if (v == 0) {
                if (spec) { ay = 0.f; by = 0.f; }
                re[a] = ax + by; im[a] = ay - bx;
            } else {
                re[a] = spec ? 0.5f * (ax + bx) : ax; im[a] = spec ? 0.5f * (ay - by) : ay;
            }
            mx = fmaxf(mx, fmaxf(fabsf(re[a]), fabsf(im[a])));
        }
        STAMPB(12);
        unsigned mu_ = __float_as_uint(mx);
        mu_ = max(mu_, (unsigned)__builtin_amdgcn_update_dpp(0, (int)mu_, 0xB1, 0xf, 0xf, false));
        mu_ = max(mu_, (unsigned)__builtin_amdgcn_update_dpp(0, (int)mu_, 0x4E, 0xf, 0xf, false));
        mu_ = max(mu_, (unsigned)__builtin_amdgcn_update_dpp(0, (int)mu_, 0x141, 0xf, 0xf, false));
        mu_ = max(mu_, (unsigned)__builtin_amdgcn_update_dpp(0, (int)mu_, 0x128, 0xf, 0xf, false));
        { auto q = __builtin_amdgcn_permlane16_swap(mu_, mu_, false, false); mu_ = max(q[0], q[1]); }
        { auto q = __builtin_amdgcn_permlane32_swap(mu_, mu_, false, false); mu_ = max(q[0], q[1]); }
        unsigned ex = min(max(mu_ >> 23, 10u), 250u);
        const float sc = __uint_as_float((262u - ex) << 23), isc = __uint_as_float((ex - 8u) << 23);
        unsigned hw_[12];
#pragma unroll
        for (int a = 0; a < 12; a++) hw_[a] = pack_f16(re[a] * sc, im[a] * sc);
        dft24h_stage(mi, hw_, re, im);
#pragma unroll
        for (int a = 0; a < 12; a++) hw_[a] = cmulh(pack_f16(re[a], im[a]), mi.tw[a]);
        dft24h_stage(mi, hw_, re, im);
        if ((l6 & 31) < 24) {
#pragma unroll
            for (int a = 0; a < 12; a += 2) {
                f32x4 o = {re[a] * isc, -im[a] * isc, re[a + 1] * isc, -im[a + 1] * isc};
                *(LAS f32x4*)(smem + LDS_CB + (v * 576 + 24 * (l6 & 31) + rho(a, hh)) * 8) = o;
            }
        }
        WAITL0();
        STAMPB(13);
            SB0(); BAR();
#pragma unroll
        for (int rep = 0; rep < 3; rep++) {
            if (rep < 2 || w == 4) {
                const int pid = l8 + 256 * rep;
                const int i = pid & 7, pc = pid >> 3;
                float cr[4], ci[4];
                cr[0] = 1.f; ci[0] = (i & 1) ? -1.f : 1.f;
#pragma unroll
                for (int r = 1; r < 4; r++) {
                    float ang = (float)((i * r) & 7) * 0.125f;
                    cr[r] = 2.f * __builtin_amdgcn_cosf(ang); ci[r] = -2.f * __builtin_amdgcn_sinf(ang);
                }
                float sv[8] = {0.f, 0.f, 0.f, 0.f, 0.f, 0.f, 0.f, 0.f};
#pragma unroll
                for (int r = 0; r < 4; r++)
#pragma unroll
                    for (int h2 = 0; h2 < 4; h2++) {
                        const f32x4 c0 = *(LAS f32x4*)(smem + LDS_CB + (r * 576 + 8 * pc + 2 * h2) * 8);
                        sv[2 * h2] += cr[r] * c0[0] + ci[r] * c0[1]; sv[2 * h2 + 1] += cr[r] * c0[2] + ci[r] * c0[3];
                    }
                u32x4 oo = {pack_bf16(sv[0], sv[1]), pack_bf16(sv[2], sv[3]), pack_bf16(sv[4], sv[5]), pack_bf16(sv[6], sv[7])};
                *(LAS u32x4*)(smem + LDS_S + i * 1168 + pc * 16) = oo;
            }
        }
        STAMPB(10);
    }
    WAITL0();
    BAR();
#pragma unroll
    for (int rep = 0; rep < 2; rep++) {
        if (rep == 1 && w != 7) break;
        const int un = rep == 0 ? ln : 512 + (ln & 63);
        const int row = un / 72, cc = un % 72;
        const u32x4 xq = *(LAS u32x4*)(smem + LDS_XR + un * 16), sq = *(LAS u32x4*)(smem + LDS_S + row * 1168 + cc * 16), gq = *(LAS u32x4*)(smem + LDS_G + row * 1168 + cc * 16);
        u32x4 oo;
#pragma unroll
        for (int e = 0; e < 4; e++) oo[e] = pack_bf16(bf_lo(xq[e]) + rbf(bf_lo(sq[e]) * bf_lo(gq[e])), bf_hi(xq[e]) + rbf(bf_hi(sq[e]) * bf_hi(gq[e])));
        *(u32x4*)((char*)out + (size_t)(jb + 256 * row) * 1152 + cc * 16) = oo;
    }
    }
}

extern "C" void kernel_launch(void* const* d_in, const int* in_sizes, int n_in,
                              void* d_out, int out_size, void* d_ws, size_t ws_size,
                              hipStream_t stream) {
    (void)hipFuncSetAttribute((const void*)k_fused, hipFuncAttributeMaxDynamicSharedMemorySize, LDS_TOTAL);
    k_fused<<<256, 512, LDS_TOTAL, stream>>>((const uint16_t*)d_in[0], (const uint16_t*)d_in[1], (const uint16_t*)d_in[2],
                                             (const uint16_t*)d_in[4], (const uint16_t*)d_in[5], (const uint16_t*)d_in[6],
                                             (const uint16_t*)d_in[7], (uint16_t*)d_out, (char*)d_ws
                                             );
}
```

```cpp
#include <hip/hip_runtime.h>
#include <stdint.h>
#include <type_traits>

typedef __attribute__((ext_vector_type(8))) __bf16 bf16x8;
typedef __attribute__((ext_vector_type(2))) __bf16 bf16x2;
typedef __attribute__((ext_vector_type(8))) _Float16 f16x8;
typedef __attribute__((ext_vector_type(2))) _Float16 f16x2;
typedef __attribute__((ext_vector_type(4))) short s16x4;
typedef __attribute__((ext_vector_type(2))) float f32x2;
typedef __attribute__((ext_vector_type(4))) float f32x4;
typedef __attribute__((ext_vector_type(16))) float f32x16;
typedef __attribute__((ext_vector_type(4))) unsigned u32x4;
typedef __attribute__((ext_vector_type(2))) unsigned u32x2;
typedef __attribute__((ext_vector_type(2))) unsigned short u16x2;
typedef unsigned long long u64;
#define LAS __attribute__((address_space(3)))
#define GAS __attribute__((address_space(1)))
#define DI __device__ __forceinline__

constexpr int RING_SLOT = 24576;
constexpr int LDS_Y = 3 * RING_SLOT;
constexpr int LDS_H = 0;
constexpr int LDS_CB = 0;
constexpr int LDS_XR = RING_SLOT;
constexpr int LDS_OUT = RING_SLOT + 12288;
constexpr int LDS_TAG = 6 * RING_SLOT;
constexpr int LDS_T256 = LDS_TAG + 16;
constexpr int LDS_WMAX = LDS_T256 + 2048;
constexpr int LDS_BIAS = LDS_WMAX + 512;
constexpr int LDS_XS = LDS_BIAS + 2304;
constexpr int LDS_XA = LDS_BIAS;
constexpr int LDS_TOTAL = LDS_XS + 8 * 1152 + 64;
constexpr unsigned WS_EPOCH = 0;
constexpr unsigned WS_ROWF = 256;
constexpr unsigned WS_COLF = 2048;
constexpr unsigned WS_PARTF = 4096;
constexpr unsigned ZV_PITCH = 580 * 4;
constexpr unsigned WS_ZV = 4096 + 16384;
constexpr unsigned WS_VP = WS_ZV + 256 * 4 * ZV_PITCH;
constexpr unsigned WS_PART = WS_VP + 8 * 32 * 256 * 80;
constexpr unsigned WS_END = WS_PART + 8u * 2048 * 576 * 2;
constexpr unsigned OOB = 0xF0000000u;

template <int IMM>
DI void glds16(const char* sbase, unsigned voff, unsigned ldsbase) {
    asm volatile("s_add_u32 m0, %2, %3\n\ts_nop 0\n\tglobal_load_lds_dwordx4 %0, %1" ::"v"(voff), "s"(sbase), "s"(ldsbase), "i"(IMM) : "memory", "scc");
}
DI void glds16r(const char* sbase, unsigned voff, unsigned ldsaddr) {
    asm volatile("s_mov_b32 m0, %2\n\ts_nop 0\n\tglobal_load_lds_dwordx4 %0, %1" ::"v"(voff), "s"(sbase), "s"(ldsaddr) : "memory");
}
#define GL1(n) "s_add_u32 m0, m0, 0x400\n\ts_nop 0\n\tglobal_load_lds_dwordx4 %" #n ", %12\n\t"
DI void glds16x12(const char* sbase, const unsigned (&vo)[12], unsigned ldsaddr) {
    asm volatile("s_mov_b32 m0, %13\n\ts_nop 0\n\tglobal_load_lds_dwordx4 %0, %12\n\t"
                 GL1(1) GL1(2) GL1(3) GL1(4) GL1(5) GL1(6) GL1(7) GL1(8) GL1(9) GL1(10) GL1(11)
                 ::"v"(vo[0]), "v"(vo[1]), "v"(vo[2]), "v"(vo[3]), "v"(vo[4]), "v"(vo[5]), "v"(vo[6]), "v"(vo[7]), "v"(vo[8]), "v"(vo[9]), "v"(vo[10]), "v"(vo[11]),
                 "s"(sbase), "s"(ldsaddr) : "memory", "scc");
}
template <int OFF>
DI u32x4 gload16(const char* sbase, unsigned voff) {
    u32x4 r;
    asm volatile("global_load_dwordx4 %0, %1, %2 offset:%3" : "=v"(r) : "v"(voff), "s"(sbase), "i"(OFF) : "memory");
    return r;
}
#define SB0() __builtin_amdgcn_sched_barrier(0)
#define PIN(x) asm volatile("" : "+v"(x))
DI s16x4 tr_rd(LAS char* p) { return __builtin_amdgcn_ds_read_tr16_b64_v4i16((LAS s16x4*)p); }
DI unsigned pack_bf16(float a, float b) { bf16x2 p; p[0] = (__bf16)a; p[1] = (__bf16)b; return __builtin_bit_cast(unsigned, p); }
DI unsigned pack_f16(float a, float b) { f16x2 p; p[0] = (_Float16)a; p[1] = (_Float16)b; return __builtin_bit_cast(unsigned, p); }
DI float bf_lo(unsigned u) { return __uint_as_float(u << 16); }
DI float bf_hi(unsigned u) { return __uint_as_float(u & 0xffff0000u); }
DI float rbf(float f) { return bf_lo(pack_bf16(f, 0.f)); }
#define STAMP(i)
#define STAMPB(i)
#define MFMA32(a, b, c) __builtin_amdgcn_mfma_f32_32x32x16_bf16((a), (b), (c), 0, 0, 0)
#define MFMA32H(a, b, c) __builtin_amdgcn_mfma_f32_32x32x16_f16((a), (b), (c), 0, 0, 0)
#define MFMA16(a, b, c) __builtin_amdgcn_mfma_f32_16x16x32_bf16((a), (b), (c), 0, 0, 0)
template <int N> DI void waitv() { asm volatile("s_waitcnt vmcnt(%0)" ::"i"(N) : "memory"); }
#define WAITL0() asm volatile("s_waitcnt lgkmcnt(0)" ::: "memory")
#define BAR() do { __builtin_amdgcn_s_barrier(); SB0(); } while (0)
#define RLX_AGENT __ATOMIC_RELAXED, __HIP_MEMORY_SCOPE_AGENT
template <int V> using IC = std::integral_constant<int, V>;
template <int A, int B, typename F> DI void static_for(F&& f) {
    if constexpr (A < B) { f(IC<A>{}); static_for<A + 1, B>(f); }
}
DI f16x8 mk8h(unsigned a, unsigned b, unsigned c, unsigned d) { u32x4 v = {a, b, c, d}; return __builtin_bit_cast(f16x8, v); }
DI int rho(int a, int hh) { return (a & 3) + 8 * (a >> 2) + 4 * hh; }
DI float red8(float v) {
    v += __builtin_bit_cast(float, __builtin_amdgcn_update_dpp(0, __builtin_bit_cast(int, v), 0xB1, 0xf, 0xf, false));
    v += __builtin_bit_cast(float, __builtin_amdgcn_update_dpp(0, __builtin_bit_cast(int, v), 0x4E, 0xf, 0xf, false));
    v += __builtin_bit_cast(float, __builtin_amdgcn_update_dpp(0, __builtin_bit_cast(int, v), 0x141, 0xf, 0xf, false));
    return v;
}

DI void tw576(float (&re)[12], float (&im)[12], int lane) {
    const float b0 = (float)(lane & 31) * (1.0f / 576.0f), b1_ = (float)(4 * (lane >> 5)) * b0;
#pragma unroll
    for (int a = 0; a < 12; a++) {
        float an = (float)rho(a, 0) * b0 + b1_;
        float c = __builtin_amdgcn_cosf(an), s = __builtin_amdgcn_sinf(an);
        float r = re[a] * c + im[a] * s;
        float i = im[a] * c - re[a] * s;
        re[a] = r; im[a] = i;
    }
}
#define MFMA16H(a, b, c) __builtin_amdgcn_mfma_f32_16x16x32_f16((a), (b), (c), 0, 0, 0)
DI unsigned rotq(unsigned p) { return __builtin_amdgcn_alignbit(p, p, 16) ^ 0x00008000u; }
struct Dft24h { f16x8 P[3], Q[3]; };
DI void dft24h_setup(Dft24h& m, int lane) {
    const int col = lane & 31, hh = lane >> 5;
    unsigned p[12];
#pragma unroll
    for (int a = 0; a < 12; a++) {
        float an = (float)(rho(a, hh) * col) * (1.0f / 24.0f);
        an = __builtin_amdgcn_fractf(an + 0.01f) - 0.01f;
        p[a] = pack_f16(__builtin_amdgcn_cosf(an), __builtin_amdgcn_sinf(an));
    }
#pragma unroll
    for (int s = 0; s < 3; s++) {
        m.P[s] = mk8h(p[4 * s], p[4 * s + 1], p[4 * s + 2], p[4 * s + 3]);
        m.Q[s] = mk8h(rotq(p[4 * s]), rotq(p[4 * s + 1]), rotq(p[4 * s + 2]), rotq(p[4 * s + 3]));
    }
}
DI void dft24h_stage(const Dft24h& m, const unsigned (&h)[12], float (&re)[12], float (&im)[12]) {
    f32x16 dr = {0, 0, 0, 0, 0, 0, 0, 0, 0, 0, 0, 0, 0, 0, 0, 0}, di = dr;
#pragma unroll
    for (int s = 0; s < 3; s++) {
        f16x8 xh = mk8h(h[4 * s], h[4 * s + 1], h[4 * s + 2], h[4 * s + 3]);
        dr = MFMA32H(xh, m.P[s], dr); di = MFMA32H(xh, m.Q[s], di);
    }
#pragma unroll
    for (int a = 0; a < 12; a++) { re[a] = dr[a]; im[a] = di[a]; }
}
DI u32x4 mat16(const float (&ang)[4]) {
    u32x4 p;
#pragma unroll
    for (int e = 0; e < 4; e++) p[e] = pack_f16(0.25f * __builtin_amdgcn_cosf(ang[e]), 0.25f * __builtin_amdgcn_sinf(ang[e]));
    return p;
}
DI u32x4 rotq4(u32x4 p) { u32x4 q = {rotq(p[0]), rotq(p[1]), rotq(p[2]), rotq(p[3])}; return q; }
DI f16x8 ash(u32x4 v) { return __builtin_bit_cast(f16x8, v); }
template <int NW>
DI void wait_flags(GAS u64* f, u64 tag, int idx) {
    for (unsigned spins = 0; spins < (1u << 20); spins++) {
        bool ok = true;
#pragma unroll
        for (int k = 0; k < NW; k++) ok &= (__hip_atomic_load(f + idx + 64 * k, RLX_AGENT) == tag);
        if (__all(ok)) break;
        __builtin_amdgcn_s_sleep(2);
    }
    __builtin_amdgcn_fence(__ATOMIC_ACQUIRE, "wavefront");
}

__global__ __launch_bounds__(512, 2) void k_fused(const uint16_t* __restrict__ x, const uint16_t* __restrict__ wr, const uint16_t* __restrict__ wi,
                                                  const uint16_t* __restrict__ w1, const uint16_t* __restrict__ b1, const uint16_t* __restrict__ w2,
                                                  const uint16_t* __restrict__ b2, uint16_t* __restrict__ out, char* __restrict__ ws
                                                  ) {
    extern __shared__ __attribute__((aligned(16))) char smem_g[];
    LAS char* smem = (LAS char*)smem_g;
    const int tid = threadIdx.x, lane = tid & 63;
    const int w = __builtin_amdgcn_readfirstlane(tid >> 6);
    const int xg = blockIdx.x & 7, li = blockIdx.x >> 3;
    const int fs = (xg & 3) * 2 + (li & 1), t = (li >> 1) * 2 + (xg >> 2), tl = li >> 1;
    const int jb = 8 * t + fs;
    const unsigned lds0 = (unsigned)(uintptr_t)smem;
    const int rh = w & 1, ct = w >> 1;
    const __amdgpu_buffer_rsrc_t rs = __builtin_amdgcn_make_buffer_rsrc(ws, 0, WS_END, 0x00020000);
    STAMP(0);
    int sbk[5];
#pragma unroll
    for (int i = 0; i < 4; i++) sbk[i] = 2 * ((i + tl) & 3);
    sbk[4] = 8;
    const int rot9 = tl % 9;
    const char* w1b = (const char*)w1 + fs * 1152;
    const char* w2b = (const char*)w2 + (size_t)fs * 576 * 1152;
    const unsigned ldsw = lds0 + w * 1024;
    auto mkvo = [&](bool full, unsigned pitch) {
        int ln = tid; PIN(ln); ln &= 63;
        unsigned r = full ? 4 * w + (ln >> 4) : 8 * w + (ln >> 3);
        unsigned c = full ? ((((ln >> 2) & 3) ^ (ln >> 4)) * 64) + (ln & 3) * 16 : ((((ln >> 2) & 1) ^ ((ln >> 4) & 1)) * 64) + (ln & 3) * 16;
        return r * pitch + c;
    };
    unsigned vo;
    auto gl = [&](auto U_, auto J_) {
        constexpr int U = decltype(U_)::value, J = decltype(J_)::value;
        constexpr int DST = (U % 6) * RING_SLOT + J * 8192;
        if constexpr (U < 3) glds16<DST>(w1b + (unsigned)(8 * 128 + (192 * U + 64 * J) * 9216), vo, ldsw);
        else if constexpr (U < 27) glds16<DST>(w1b + (unsigned)__builtin_amdgcn_readfirstlane(sbk[(U - 3) / 6] * 128 + (96 * ((U - 3) % 6) + 32 * J) * 9216), vo, ldsw);
        else if constexpr (U < 30) glds16<DST>(w2b + (unsigned)__builtin_amdgcn_readfirstlane(rot9 * 128 + (192 * (U - 27) + 64 * J) * 1152), vo, ldsw);
    };
    auto setvo = [&](auto U_) {
        constexpr int U = decltype(U_)::value;
        if constexpr (U == 0) vo = mkvo(false, 9216);
        if constexpr (U == 3) vo = mkvo(true, 9216);
        if constexpr (U == 27) {
            int ln = tid; PIN(ln); ln &= 63;
            vo = (unsigned)(8 * w + (ln >> 3)) * 1152 + ((((ln >> 1) & 3) ^ ((ln >> 4) & 3)) * 32) + (ln & 1) * 16;
        }
    };
    auto gl3 = [&](auto U_) { setvo(U_); static_for<0, 3>([&](auto J_) { gl(U_, J_); }); };

    Dft24h m24;
    {
        const int i = lane >> 3, c = lane & 7;
        const char* xr = (const char*)x + (size_t)(8 * t + w) * 1152;
        const unsigned xo = (unsigned)i * (256 * 1152) + c * 16;
        u32x4 q[9];
        static_for<0, 9>([&](auto N_) {
            constexpr int n = decltype(N_)::value;
            q[n] = gload16<n * 128>(xr, xo);
        });
        const u32x2 ep = __builtin_amdgcn_raw_buffer_load_b64(rs, tid == 0 ? 0u : OOB, WS_EPOCH, 16);
        const bool dob = tid >= 384 && tid < 384 + 72;
        const u32x4 bb = *(const u32x4*)((const char*)b1 + fs * 1152 + (dob ? tid - 384 : 0) * 16);
        SB0();
        f32x2 tw;
        {
            float an = (float)(((tid >> 4) & 15) * (8 * (tid & 15) + fs)) * (1.0f / 2048.0f);
            tw[0] = __builtin_amdgcn_cosf(an); tw[1] = __builtin_amdgcn_sinf(an);
        }
        if (w >= 4) dft24h_setup(m24, lane); else __builtin_amdgcn_s_sleep(6);
        SB0();
        gl3(IC<0>{}); gl3(IC<1>{}); gl3(IC<2>{});
        SB0();
        asm volatile("s_waitcnt vmcnt(11)" : "+v"(q[0]), "+v"(q[1]), "+v"(q[2]), "+v"(q[3]), "+v"(q[4]), "+v"(q[5]), "+v"(q[6]), "+v"(q[7]), "+v"(q[8]) :: "memory");
        STAMP(13);
        f32x2 d[36];
        f32x2 s2 = {0.f, 0.f};
#pragma unroll
        for (int n = 0; n < 9; n++)
#pragma unroll
            for (int e = 0; e < 4; e++) { f32x2 v = {bf_lo(q[n][e]), bf_hi(q[n][e])}; d[4 * n + e] = v; s2 += v; }
        const float mu = red8(s2[0] + s2[1]) * (1.0f / 576.0f);
        const f32x2 mu2 = {mu, mu};
        f32x2 q2 = {0.f, 0.f};
#pragma unroll
        for (int e = 0; e < 36; e++) { d[e] -= mu2; q2 = __builtin_elementwise_fma(d[e], d[e], q2); }
        const float var = red8(q2[0] + q2[1]) * (1.0f / 576.0f);
        const float m1 = rbf(mu);
        const float is1 = 1.0f / rbf(__builtin_sqrtf(rbf(rbf(var) + 1e-5f)));
        const float mean2 = (mu - m1) * is1;
        const float var2 = var * is1 * is1;
        const float is2 = 1.0f / rbf(__builtin_sqrtf(rbf(rbf(var2) + 1e-5f)));
        const float ya = is1 * is2, yb = (mean2 - rbf(mean2)) * is2;
        const f32x2 ya2 = {ya, ya}, yb2 = {yb, yb};
        const int mloc = 8 * w + i;
        LAS char* yw = smem + LDS_Y + (mloc >> 5) * 36 * 1024 + (c >> 1) * 1024 + (((mloc & 31) ^ c) + 32 * (c & 1)) * 16;
#pragma unroll
        for (int n = 0; n < 9; n++) {
            u32x4 o;
#pragma unroll
            for (int e = 0; e < 4; e++) { f32x2 v = __builtin_elementwise_fma(d[4 * n + e], ya2, yb2); o[e] = pack_bf16(v[0], v[1]); }
            *(LAS u32x4*)(yw + n * 4096) = o;
        }
        if (w == fs) {
            const f32x2 xa = {is1, is1}, xb = {mean2, mean2};
            const int pos = (i & 1) ? 4 + (i >> 1) : ((i >> 2) | (i & 2));
            LAS char* xw = smem + LDS_XS + pos * 1152 + c * 16;
#pragma unroll
            for (int n = 0; n < 9; n++) {
                u32x4 o;
#pragma unroll
                for (int e = 0; e < 4; e++) { f32x2 v = __builtin_elementwise_fma(d[4 * n + e], xa, xb); o[e] = pack_bf16(v[0], v[1]); }
                *(LAS u32x4*)(xw + n * 128) = o;
            }
        }
        if (dob) {
            f32x4 lo = {bf_lo(bb[0]), bf_hi(bb[0]), bf_lo(bb[1]), bf_hi(bb[1])}, hi = {bf_lo(bb[2]), bf_hi(bb[2]), bf_lo(bb[3]), bf_hi(bb[3])};
            *(LAS f32x4*)(smem + LDS_BIAS + (tid - 384) * 32) = lo;
            *(LAS f32x4*)(smem + LDS_BIAS + (tid - 384) * 32 + 16) = hi;
        }
        if (tid == 0) *(LAS u32x2*)(smem + LDS_TAG) = ep;
        if (tid < 256) *(LAS f32x2*)(smem + LDS_T256 + tid * 8) = tw;
        STAMP(14);
        waitv<0>();
        WAITL0();
    }
    BAR();
    STAMP(1);
    u64 epoch;
    {
        u32x2 e2 = *(LAS u32x2*)(smem + LDS_TAG);
        epoch = ((u64)(unsigned)__builtin_amdgcn_readfirstlane(e2[1]) << 32) | (unsigned)__builtin_amdgcn_readfirstlane(e2[0]);
    }
    const u64 tag = (epoch + 1) * 0x9E3779B97F4A7C15ull + 0x632BE59BD9B4E019ull;

    auto put_flag = [&](bool me, unsigned off, u64 val) {
        u32x2 o = {(unsigned)val, (unsigned)(val >> 32)};
        __builtin_amdgcn_raw_buffer_store_b64(o, rs, me ? 0u : OOB, off, 16);
    };
    LAS char* trc;
    auto settr = [&](auto U_) {
        constexpr int U = decltype(U_)::value;
        if constexpr (U == 0 || U == 3 || U == 27) {
            int ln = tid; PIN(ln); ln &= 63;
            int G = ln >> 4, q = (ln >> 2) & 3, p = ln & 3;
            int row = 8 * (G >> 1) + q;
            if constexpr (U == 3) trc = smem + row * 256 + ((ct ^ q) * 64) + (G & 1) * 32 + p * 8 + ((U % 6) / 2) * 2 * RING_SLOT;
            else if constexpr (U == 0) trc = smem + row * 128 + (((ct & 1) ^ ((q >> 1) & 1)) * 64) + (G & 1) * 32 + p * 8;
            else {
                row = 16 * (G >> 1) + 4 * (G & 1) + q;
                trc = smem + row * 128 + ((ct ^ ((row >> 1) & 3)) * 32) + p * 8 + ((U % 6) / 2) * 2 * RING_SLOT;
            }
            PIN(trc);
        } else if constexpr (U % 2 == 0) {
            trc += (U % 6 == 0) ? -4 * RING_SLOT : 2 * RING_SLOT;
            PIN(trc);
        }
    };
    bf16x8 af[36];
    auto load_y = [&]() {
        LAS char* ab = smem + LDS_Y + rh * 36 * 1024;
#pragma unroll
        for (int j = 0; j < 36; j++) af[j] = *(LAS bf16x8*)(ab + j * 1024 + (lane ^ (2 * (j & 3) + (lane >> 5))) * 16);
    };
    LAS char* biasp = smem + LDS_BIAS + (32 * ct + 4 * (lane >> 5)) * 4;
    f32x16 acc[2];
    s16x4 bl[6], bh[6];
    float gt[4];
    unsigned hq[5][8];
    auto ldbias = [&](auto P_, auto Q_, int blk) {
        constexpr int P = decltype(P_)::value, Q = decltype(Q_)::value;
        f32x4 b = *(LAS f32x4*)(biasp + blk * 256 + Q * 32);
        acc[P][4 * Q] = b[0]; acc[P][4 * Q + 1] = b[1]; acc[P][4 * Q + 2] = b[2]; acc[P][4 * Q + 3] = b[3];
    };
    auto gop = [&](auto C_, auto K_) {
        constexpr int C = decltype(C_)::value, K = decltype(K_)::value, P = C == 4 ? 1 : (C & 1);
        constexpr int NB = C == 4 ? 1 : C + 2;
        const float c0 = -2.0f * 0.7978845608028654f * 1.4426950408889634f;
        const float c1 = c0 * 0.044715f;
        if constexpr (K < 124) {
            constexpr int Q = K / 31, k = K % 31;
            if constexpr (k < 28) {
                constexpr int st = k / 4, e = k % 4;
                float xx = acc[P][4 * Q + e];
                if constexpr (st == 0) gt[e] = xx * xx;
                else if constexpr (st == 1) gt[e] = __builtin_fmaf(gt[e], c1, c0);
                else if constexpr (st == 2) gt[e] = xx * gt[e];
                else if constexpr (st == 3) gt[e] = __builtin_amdgcn_exp2f(gt[e]);
                else if constexpr (st == 4) gt[e] = 1.0f + gt[e];
                else if constexpr (st == 5) gt[e] = __builtin_amdgcn_rcpf(gt[e]);
                else gt[e] = xx * gt[e];
                PIN(gt[e]);
            } else if constexpr (k == 28) { hq[C][2 * Q] = pack_bf16(gt[0], gt[1]); PIN(hq[C][2 * Q]); }
            else if constexpr (k == 29) { hq[C][2 * Q + 1] = pack_bf16(gt[2], gt[3]); PIN(hq[C][2 * Q + 1]); }
            else { if constexpr (NB <= 3) ldbias(IC<P>{}, IC<Q>{}, sbk[NB]); }
        }
    };
    auto grange = [&](auto C_, auto A_, auto B_) {
        static_for<decltype(A_)::value, decltype(B_)::value>([&](auto K_) { gop(C_, K_); });
    };
    const f32x16 zero16 = {0, 0, 0, 0, 0, 0, 0, 0, 0, 0, 0, 0, 0, 0, 0, 0};
    auto bfrag = [&](auto J_) {
        constexpr int J = decltype(J_)::value;
        return __builtin_bit_cast(bf16x8, __builtin_shufflevector(bl[J], bh[J], 0, 1, 2, 3, 4, 5, 6, 7));
    };
    auto fslot = [&](auto U_, auto LO_, auto GI_, auto&& fill) {
        constexpr int U = decltype(U_)::value, v = U - 3, c = v / 6, s = v % 6;
        constexpr bool LO = decltype(LO_)::value;
        constexpr int PB = c & 1;
        constexpr int PA = s == 0 ? PB ^ 1 : PB;
        constexpr int sp = (s + 5) % 6;
        settr(U_);
        LAS char* sb = trc + (U % 2) * RING_SLOT;
        setvo(GI_);
        static_for<0, 6>([&](auto G_) {
            constexpr int G = decltype(G_)::value;
            if constexpr (G < 3) {
                if constexpr (LO) acc[PA] = MFMA32(bfrag(IC<G + 3>{}), af[6 * sp + G + 3], acc[PA]);
            } else {
                acc[PB] = MFMA32(bfrag(IC<G - 3>{}), af[6 * s + G - 3], acc[PB]);
            }
            SB0();
            bl[G] = tr_rd(sb + G * 4096); bh[G] = tr_rd(sb + G * 4096 + 1024);
            if constexpr (G < 3) gl(GI_, G_);
            fill(G_);
            SB0();
        });
    };
    auto hslot = [&](auto U_, auto PB_, auto LO_, auto&& fill) {
        constexpr int U = decltype(U_)::value, ph = U / 27, s = U % 3, PB = decltype(PB_)::value;
        constexpr int PA = s == 0 ? PB ^ 1 : PB;
        constexpr int R2 = ph == 0 ? 512 : 1024;
        settr(U_);
        LAS char* sb = trc + (U % 2) * RING_SLOT;
        static_for<0, 12>([&](auto G_) {
            constexpr int G = decltype(G_)::value;
            if constexpr (G < 3) {
                if constexpr (decltype(LO_)::value) acc[PA] = MFMA32(bfrag(IC<G + 3>{}), af[(12 * s + 33) % 36 + G], acc[PA]);
            } else {
                acc[PB] = MFMA32(bfrag(IC<(G - 3) % 6>{}), af[12 * s + G - 3], (ph == 1 && s == 0 && G == 3) ? zero16 : acc[PB]);
            }
            SB0();
            bl[G % 6] = tr_rd(sb + G * 2048); bh[G % 6] = tr_rd(sb + G * 2048 + R2);
            fill(G_);
            SB0();
        });
    };
    auto iss2 = [&](auto GI_, auto GI2_) {
        return [&, GI_, GI2_](auto G_) {
            constexpr int G = decltype(G_)::value;
            if constexpr (G == 0) setvo(GI_);
            if constexpr (G < 6 && G % 2 == 0) gl(GI_, IC<G / 2>{});
            if constexpr (G >= 6 && G % 2 == 0) gl(GI2_, IC<G / 2 - 3>{});
        };
    };
    using T = std::true_type; using F = std::false_type;
    auto nofill = [&](auto) {};

    if (w < 4) {
        load_y();
        static_for<0, 4>([&](auto Q_) { ldbias(IC<1>{}, Q_, sbk[4]); ldbias(IC<0>{}, Q_, sbk[0]); });
        hslot(IC<0>{}, IC<1>{}, F{}, nofill); WAITL0(); BAR();
        hslot(IC<1>{}, IC<1>{}, T{}, iss2(IC<3>{}, IC<4>{})); BAR();
        hslot(IC<2>{}, IC<1>{}, T{}, iss2(IC<5>{}, IC<99>{})); waitv<6>(); BAR();
        STAMP(9);
        static_for<3, 9>([&](auto U_) {
            constexpr int U = decltype(U_)::value, s = U - 3;
            fslot(U_, T{}, IC<U + 3>{}, [&](auto G_) {
                constexpr int gi = 6 * s + decltype(G_)::value - 4;
                if constexpr (gi >= 0) grange(IC<4>{}, IC<(gi * 124) / 32>{}, IC<((gi + 1) * 124) / 32>{});
            });
            waitv<6>(); BAR();
        });
    } else {
        const int v = w - 4;
        const unsigned PP = 0x3F803F80u, PM = 0xBF803F80u  , MP = 0x3F80BF80u  , MM = 0xBF80BF80u;
        unsigned A0, A1, A2, A3, B0, B1, B2, B3; float cc;
        const float c8 = 0.70710678118654752f;
        if (v == 0) { A0 = PP; A1 = PP; A2 = PP; A3 = PP; B0 = PP; B1 = PP; B2 = MM; B3 = MM; cc = 1.f; }
        else if (v == 1) { A0 = PM; A1 = 0; A2 = PM; A3 = MP; B0 = 0; B1 = MP; B2 = MM; B3 = PP; cc = c8; }
        else if (v == 2) { A0 = PP; A1 = MM; A2 = 0; A3 = 0; B0 = 0; B1 = 0; B2 = MP; B3 = MP; cc = 1.f; }
        else { A0 = PM; A1 = 0; A2 = MP; A3 = PM; B0 = 0; B1 = PM; B2 = MM; B3 = PP; cc = c8; }
        float re[12], im[12];
        unsigned hw_[12];
        auto dot = [](unsigned a, unsigned b, float c) { return __builtin_amdgcn_fdot2_f32_bf16(__builtin_bit_cast(bf16x2, a), __builtin_bit_cast(bf16x2, b), c, false); };
        {
            const int G = lane >> 4, q = (lane >> 2) & 3, p = lane & 3;
            LAS char* xb = smem + LDS_XS + q * 1152 + (96 * (G >> 1) + 16 * (G & 1) + 4 * p) * 2;
            u32x2 ev[12], od[12];
#pragma unroll
            for (int a = 0; a < 12; a++) {
                ev[a] = __builtin_bit_cast(u32x2, tr_rd(xb + rho(a, 0) * 48));
                od[a] = __builtin_bit_cast(u32x2, tr_rd(xb + rho(a, 0) * 48 + 4 * 1152));
            }
            SB0();
#pragma unroll
            for (int a = 0; a < 12; a++) {
                float r_ = dot(ev[a][0], A0, dot(ev[a][1], A1, cc * dot(od[a][0], A2, dot(od[a][1], A3, 0.f))));
                float i_ = dot(ev[a][0], B0, dot(ev[a][1], B1, cc * dot(od[a][0], B2, dot(od[a][1], B3, 0.f))));
                hw_[a] = pack_f16(r_, i_);
            }
#pragma unroll
            for (int a = 0; a < 12; a++) PIN(hw_[a]);
            SB0();
        }
        load_y();
        WAITL0(); SB0(); STAMPB(0); BAR();
        dft24h_stage(m24, hw_, re, im);
        gl3(IC<3>{}); gl3(IC<4>{});
        tw576(re, im, lane);
#pragma unroll
        for (int a = 0; a < 12; a++) { hw_[a] = pack_f16(re[a], im[a]); PIN(hw_[a]); }
        SB0(); STAMPB(1); BAR();
        dft24h_stage(m24, hw_, re, im);
        gl3(IC<5>{});
        {
            const int hh = lane >> 5;
            const unsigned zo = (lane & 31) < 24 ? (unsigned)(((lane & 31) * 24 + 4 * hh) * 4) : OOB;
            const unsigned zb = WS_ZV + (unsigned)(jb * 4 + v) * ZV_PITCH;
            unsigned z0 = 0;
#pragma unroll
            for (int qd = 0; qd < 3; qd++) {
                u32x4 o;
#pragma unroll
                for (int e = 0; e < 4; e++) o[e] = pack_f16(re[4 * qd + e] * (1.0f / 64.0f), im[4 * qd + e] * (1.0f / 64.0f));
                if (qd == 0) z0 = o[0];
                __builtin_amdgcn_raw_buffer_store_b128(o, rs, zo, zb + qd * 32, 16);
            }
            __builtin_amdgcn_raw_buffer_store_b32(z0, rs, lane == 0 ? 0u : OOB, zb + 576 * 4, 16);
        }
        static_for<0, 4>([&](auto Q_) { ldbias(IC<0>{}, Q_, sbk[0]); ldbias(IC<1>{}, Q_, sbk[1]); });
        STAMPB(2); waitv<10>(); STAMPB(3); BAR();
        static_for<3, 9>([&](auto U_) {
            constexpr int U = decltype(U_)::value;
            if constexpr (U == 3) fslot(U_, F{}, IC<U + 3>{}, nofill); else fslot(U_, T{}, IC<U + 3>{}, nofill);
            if constexpr (U <= 4) waitv<10>(); else waitv<6>();
            BAR();
            if constexpr (U == 5) __builtin_amdgcn_raw_buffer_store_b32((unsigned)(tag >> 32), rs, tid == 256 ? 0u : OOB, WS_ROWF + jb * 4, 16);
        });
    }
    STAMP(10);
    u32x4 rf;
    static_for<9, 27>([&](auto U_) {
        constexpr int U = decltype(U_)::value, c = (U - 3) / 6, s = (U - 3) % 6;
        fslot(U_, T{}, IC<U + 3>{}, [&](auto G_) {
            constexpr int gi = 6 * s + decltype(G_)::value - 4;
            if constexpr (gi >= 0) grange(IC<c - 1>{}, IC<(gi * 124) / 32>{}, IC<((gi + 1) * 124) / 32>{});
            if constexpr (U == 24 && decltype(G_)::value == 4) {
                int ln = tid; PIN(ln); ln &= 63;
                rf = __builtin_amdgcn_raw_buffer_load_b128(rs, ln * 16, WS_ROWF, 0);
            }
        });
        if constexpr (U < 24) { waitv<6>(); BAR(); }
        else if constexpr (U < 26) { waitv<7>(); BAR(); }
    });
    static_for<0, 3>([&](auto G_) { constexpr int G = decltype(G_)::value; acc[1] = MFMA32(bfrag(IC<G + 3>{}), af[33 + G], acc[1]); });
    STAMP(2);
    WAITL0();
    waitv<0>();
    BAR();
    const unsigned tg = (unsigned)(tag >> 32);
    {
        int ln = tid; PIN(ln); ln &= 63;
        LAS char* hw = smem + LDS_H + ((2 * rh + ((ln >> 4) & 1)) * 18 + ct) * 1024 + ((ln & 15) + 16 * (ln >> 5)) * 16;
        auto hst = [&](auto C_) {
            constexpr int C = decltype(C_)::value;
            u32x4 o0 = {hq[C][0], hq[C][1], hq[C][2], hq[C][3]}, o1 = {hq[C][4], hq[C][5], hq[C][6], hq[C][7]};
            *(LAS u32x4*)(hw + sbk[C] * 2048) = o0; *(LAS u32x4*)(hw + sbk[C] * 2048 + 512) = o1;
        };
        hst(IC<0>{}); hst(IC<1>{}); hst(IC<2>{});
        if (w < 4) hst(IC<4>{});
        const int r = fs, col = ln & 15, g = ln >> 4;
        const f32x4 z4 = {0.f, 0.f, 0.f, 0.f};
        for (unsigned spins = 0; spins < (1u << 20); spins++) {
            if (__all(rf[0] == tg && rf[1] == tg && rf[2] == tg && rf[3] == tg)) break;
            __builtin_amdgcn_s_sleep(8);
            rf = __builtin_amdgcn_raw_buffer_load_b128(rs, ln * 16, WS_ROWF, 16);
        }
        __builtin_amdgcn_fence(__ATOMIC_ACQUIRE, "wavefront");
        put_flag(jb == 0 && tid == 0, WS_EPOCH, epoch + 1);
        u32x4 za[2], zb[2];
        unsigned short wa[2][4], wb[2][4];
        const int vA = r <= 3 ? r : 0, vB = r >= 5 ? 8 - r : 0;
        const bool useA = r <= 4 && col < 10, useB = (r == 0 || r >= 4) && col < 10;
        const unsigned lo = (unsigned)(64 * g) * 4 * ZV_PITCH;
        const unsigned loA = useA ? lo + col * 4 : OOB, loB = useB ? lo + (15 - col) * 4 : OOB;
        const unsigned sA = WS_ZV + vA * ZV_PITCH + 9 * t * 4 + w * 8 * ZV_PITCH, sB = WS_ZV + vB * ZV_PITCH + (561 - 9 * t) * 4 + w * 8 * ZV_PITCH;
        const unsigned wo = (unsigned)((8 * (2 * w + 64 * g) + r) * 289 + 9 * t + (col < 10 ? col : 9));
        SB0();
        static_for<0, 32>([&](auto L_) {
            constexpr int L = decltype(L_)::value, tl_ = (L >> 3) & 1, e = (L >> 1) & 3;
            if constexpr (L < 16) {
                if constexpr ((L & 1) == 0) za[tl_][e] = __builtin_amdgcn_raw_buffer_load_b32(rs, loA, sA + (16 * e + tl_) * 4 * ZV_PITCH, 16);
                else zb[tl_][e] = __builtin_amdgcn_raw_buffer_load_b32(rs, loB, sB + (16 * e + tl_) * 4 * ZV_PITCH, 16);
            } else {
                if constexpr ((L & 1) == 0) wa[tl_][e] = wr[wo + (8 * tl_ + 128 * e) * 289];
                else wb[tl_][e] = wi[wo + (8 * tl_ + 128 * e) * 289];
            }
            grange(IC<3>{}, IC<4 * L>{}, IC<4 * L + 4>{});
            SB0();
        });
        hst(IC<3>{});
        STAMPB(4);
        LAS char* xw = smem + LDS_XA + ((4 * g * 4 + (w >> 1)) * 10 + (col < 10 ? col : 9)) * 16 + (w & 1) * 8;
        LAS char* xr = smem + LDS_XA + ((2 * w * 4 + g) * 10 + (col < 10 ? col : 9)) * 16;
        f32x4 dr[2], di[2];
        u32x4 P3, Q3, P4, Q4;
        unsigned wp[2][4];
        {
            float an[4];
#pragma unroll
            for (int e = 0; e < 4; e++) an[e] = (float)(((4 * g + e) * (8 * col + r)) & 127) * (1.0f / 128.0f) + (r == 4 ? 0.25f : 0.f);
            const u32x4 P1 = mat16(an), Q1 = rotq4(P1);
            const unsigned fl = r == 4 ? 0x00008000u : 0x80000000u;
            const u32x4 P2 = P1 ^ fl, Q2 = Q1 ^ fl;
            f32x4 t0[2], t1[2];
#pragma unroll
            for (int tl_ = 0; tl_ < 2; tl_++) {
                LAS char* tp = smem + LDS_T256 + ((2 * w + tl_) * 16 + 4 * g) * 8;
                t0[tl_] = *(LAS f32x4*)tp; t1[tl_] = *(LAS f32x4*)(tp + 16);
                dr[tl_] = MFMA16H(ash(P1), ash(za[tl_]), z4); di[tl_] = MFMA16H(ash(Q1), ash(za[tl_]), z4);
                dr[tl_] = MFMA16H(ash(P2), ash(zb[tl_]), dr[tl_]); di[tl_] = MFMA16H(ash(Q2), ash(zb[tl_]), di[tl_]);
            }
#pragma unroll
            for (int e = 0; e < 4; e++) an[e] = (float)(((4 * g + e) * col) & 15) * (1.0f / 16.0f);
            P3 = mat16(an); Q3 = rotq4(P3);
            u32x2 o[4];
#pragma unroll
            for (int e = 0; e < 4; e++)
#pragma unroll
                for (int tl_ = 0; tl_ < 2; tl_++) {
                    const float c = e < 2 ? t0[tl_][2 * e] : t1[tl_][2 * e - 4], sn = e < 2 ? t0[tl_][2 * e + 1] : t1[tl_][2 * e - 3];
                    o[e][tl_] = pack_f16(dr[tl_][e] * c + di[tl_][e] * sn, di[tl_][e] * c - dr[tl_][e] * sn);
                }
            u16x2 mx = {0, 0};
#pragma unroll
            for (int tl_ = 0; tl_ < 2; tl_++)
#pragma unroll
                for (int e = 0; e < 4; e++) {
                    wp[tl_][e] = (unsigned)wa[tl_][e] | ((unsigned)wb[tl_][e] << 16);
                    mx = __builtin_elementwise_max(mx, __builtin_bit_cast(u16x2, wp[tl_][e] & 0x7fff7fffu));
                }
            unsigned m1 = mx[0] > mx[1] ? mx[0] : mx[1];
            { auto q = __builtin_amdgcn_permlane16_swap(m1, m1, false, false); m1 = q[0] > q[1] ? q[0] : q[1]; }
            { auto q = __builtin_amdgcn_permlane32_swap(m1, m1, false, false); m1 = q[0] > q[1] ? q[0] : q[1]; }
            *(LAS unsigned*)(smem + LDS_WMAX + col * 32 + w * 4) = m1;
            if (col < 10) {
#pragma unroll
                for (int e = 0; e < 4; e++) *(LAS u32x2*)(xw + e * 640) = o[e];
            }
            WAITL0(); STAMPB(5); BAR();
        }
        float inv;
        u32x2 ob[4];
        {
            const u32x4 xa0 = *(LAS u32x4*)xr, xa1 = *(LAS u32x4*)(xr + 640);
            const u32x4 m0 = *(LAS u32x4*)(smem + LDS_WMAX + col * 32), m1 = *(LAS u32x4*)(smem + LDS_WMAX + col * 32 + 16);
            f32x4 tt[4];
#pragma unroll
            for (int e = 0; e < 4; e++) tt[e] = *(LAS f32x4*)(smem + LDS_T256 + ((4 * g + e) * 16 + 2 * w) * 8);
            dr[0] = MFMA16H(ash(P3), ash(xa0), z4); di[0] = MFMA16H(ash(Q3), ash(xa0), z4);
            dr[1] = MFMA16H(ash(P3), ash(xa1), z4); di[1] = MFMA16H(ash(Q3), ash(xa1), z4);
            WAITL0(); SB0(); BAR();
            unsigned ex = max(max(max(m0[0], m0[1]), max(m0[2], m0[3])), max(max(m1[0], m1[1]), max(m1[2], m1[3]))) >> 7;
            ex = min(max(ex, 1u), 252u);
            const float wsc = __uint_as_float((253u - ex) << 23);
            inv = __uint_as_float((ex + 1u) << 23);
#pragma unroll
            for (int tl_ = 0; tl_ < 2; tl_++) {
                u32x4 y;
#pragma unroll
                for (int e = 0; e < 4; e++) {
                    const float wre = bf_lo(wp[tl_][e]) * wsc, wim = bf_hi(wp[tl_][e]) * wsc;
                    y[e] = pack_f16(dr[tl_][e] * wre - di[tl_][e] * wim, -(dr[tl_][e] * wim + di[tl_][e] * wre));
                }
                dr[tl_] = MFMA16H(ash(P3), ash(y), z4); di[tl_] = MFMA16H(ash(Q3), ash(y), z4);
            }
            float an[4];
#pragma unroll
            for (int e = 0; e < 4; e++) an[e] = (float)((col * (8 * (4 * g + e) + r)) & 127) * (1.0f / 128.0f);
            P4 = mat16(an); Q4 = rotq4(P4);
#pragma unroll
            for (int e = 0; e < 4; e++)
#pragma unroll
                for (int tl_ = 0; tl_ < 2; tl_++) {
                    const float c = tt[e][2 * tl_], sn = tt[e][2 * tl_ + 1];
                    ob[e][tl_] = pack_f16(dr[tl_][e] * c + di[tl_][e] * sn, di[tl_][e] * c - dr[tl_][e] * sn);
                }
            if (col < 10) {
#pragma unroll
                for (int e = 0; e < 4; e++) *(LAS u32x2*)(xw + e * 640) = ob[e];
            }
            WAITL0(); STAMPB(6); BAR();
        }
        {
            const u32x4 xa0 = *(LAS u32x4*)xr, xa1 = *(LAS u32x4*)(xr + 640);
            const float fsc = (64.0f * 256.0f / (2048.0f * 576.0f)) * ((r & 3) == 0 ? 0.5f : 1.0f);
            const unsigned vo_ = col < 10 ? (unsigned)(64 * g * 80 + col * 8) : OOB;
            const unsigned vb = WS_VP + (unsigned)(r * 32 + t) * (256 * 80) + w * 2 * 80;
            dr[0] = MFMA16H(ash(P4), ash(xa0), z4); di[0] = MFMA16H(ash(Q4), ash(xa0), z4);
            dr[1] = MFMA16H(ash(P4), ash(xa1), z4); di[1] = MFMA16H(ash(Q4), ash(xa1), z4);
            SB0();
            {
                LAS char* ab = smem + LDS_H + 2 * rh * 18 * 1024 + ln * 16;
#pragma unroll
                for (int j = 0; j < 18; j++) { af[j] = *(LAS bf16x8*)(ab + j * 1024); af[18 + j] = *(LAS bf16x8*)(ab + (18 + j) * 1024); }
            }
            SB0();
#pragma unroll
            for (int tl_ = 0; tl_ < 2; tl_++)
#pragma unroll
                for (int e = 0; e < 4; e++) {
                    u32x2 o = {__float_as_uint(dr[tl_][e] * fsc * inv), __float_as_uint(di[tl_][e] * fsc * inv)};
                    __builtin_amdgcn_raw_buffer_store_b64(o, rs, vo_, vb + (16 * e + tl_) * 80, 16);
                }
            SB0();
        }
    }
    STAMP(3);
    f32x4 ac[2][2];
    unsigned plane;
    { int ln = tid; PIN(ln); plane = (ln & 63) * 16; }
    const unsigned pbase = WS_PART + (unsigned)((fs * 32 + t) * 72 + w) * 1024;
    auto pstore = [&](auto P_, int blk) {
        constexpr int P = decltype(P_)::value;
        unsigned a0 = pack_bf16(ac[P][0][0], ac[P][0][1]), a1 = pack_bf16(ac[P][0][2], ac[P][0][3]);
        unsigned b0 = pack_bf16(ac[P][1][0], ac[P][1][1]), b1_ = pack_bf16(ac[P][1][2], ac[P][1][3]);
        auto s0 = __builtin_amdgcn_permlane16_swap(a0, b0, false, false);
        auto s1 = __builtin_amdgcn_permlane16_swap(a1, b1_, false, false);
        u32x4 o = {s0[0], s1[0], s0[1], s1[1]};
        __builtin_amdgcn_raw_buffer_store_b128(o, rs, plane, pbase + blk * 8192, 16);
    };
    const f32x4 zero4 = {0.f, 0.f, 0.f, 0.f};
    auto qslot = [&](auto U_, auto LO_, auto&& fill) {
        constexpr int U = decltype(U_)::value, s = U % 3, PB = ((U / 3) & 1);
        constexpr int PA = s == 0 ? PB ^ 1 : PB;
        constexpr int sp = (s + 2) % 3;
        settr(U_);
        LAS char* sb = trc + (U % 2) * RING_SLOT;
        static_for<0, 6>([&](auto G_) {
            constexpr int G = decltype(G_)::value;
            if constexpr (G < 3) {
                if constexpr (decltype(LO_)::value) {
                    ac[PA][0] = MFMA16(bfrag(IC<G + 3>{}), af[6 * sp + G + 3], ac[PA][0]);
                    ac[PA][1] = MFMA16(bfrag(IC<G + 3>{}), af[18 + 6 * sp + G + 3], ac[PA][1]);
                }
            } else {
                ac[PB][0] = MFMA16(bfrag(IC<G - 3>{}), af[6 * s + G - 3], (s == 0 && G == 3) ? zero4 : ac[PB][0]);
                ac[PB][1] = MFMA16(bfrag(IC<G - 3>{}), af[18 + 6 * s + G - 3], (s == 0 && G == 3) ? zero4 : ac[PB][1]);
            }
            SB0();
            bl[G] = tr_rd(sb + G * 4096); bh[G] = tr_rd(sb + G * 4096 + 1024);
            fill(G_);
            SB0();
        });
    };
    auto g2 = [&](auto RS_, auto S_, auto J_, int blk) {
        constexpr int RS = decltype(RS_)::value, S = decltype(S_)::value, J = decltype(J_)::value;
        glds16<RS * RING_SLOT + J * 8192>(w2b + (unsigned)__builtin_amdgcn_readfirstlane(blk * 128 + (192 * S + 64 * J) * 1152), vo, ldsw);
    };
    int cprev = rot9, cn = rot9 == 8 ? 0 : rot9 + 1;
    qslot(IC<27>{}, F{}, nofill); WAITL0(); BAR();
    qslot(IC<28>{}, T{}, [&](auto G_) { constexpr int G = decltype(G_)::value; g2(IC<G / 3>{}, IC<G / 3>{}, IC<G % 3>{}, cn); }); BAR();
    qslot(IC<29>{}, T{}, [&](auto G_) { constexpr int G = decltype(G_)::value; if constexpr (G < 3) g2(IC<2>{}, IC<2>{}, G_, cn); }); waitv<6>(); BAR();
    __builtin_amdgcn_raw_buffer_store_b32(tg, rs, tid == 0 ? 0u : OOB, WS_COLF + (fs * 32 + t) * 4, 16);
    STAMP(11);
    for (int it = 0; it < 4; it++) {
        cn = cn == 8 ? 0 : cn + 1;
        static_for<30, 33>([&](auto U_) {
            constexpr int U = decltype(U_)::value;
            qslot(U_, T{}, [&](auto G_) {
                constexpr int G = decltype(G_)::value;
                if constexpr (G < 3) g2(IC<3 + U % 3>{}, IC<U % 3>{}, G_, cn);
                if constexpr (U == 30 && G == 5) pstore(IC<1>{}, cprev);
            });
            waitv<7>(); BAR();
        });
        cprev = cprev == 8 ? 0 : cprev + 1;
        cn = cn == 8 ? 0 : cn + 1;
        if (it < 3) {
            static_for<33, 36>([&](auto U_) {
                constexpr int U = decltype(U_)::value;
                qslot(U_, T{}, [&](auto G_) {
                    constexpr int G = decltype(G_)::value;
                    if constexpr (G < 3) g2(IC<U % 3>{}, IC<U % 3>{}, G_, cn);
                    if constexpr (U == 33 && G == 5) pstore(IC<0>{}, cprev);
                });
                waitv<7>(); BAR();
            });
            cprev = cprev == 8 ? 0 : cprev + 1;
        }
    }
    u32x4 cf;
    qslot(IC<33>{}, T{}, [&](auto G_) {
        constexpr int G = decltype(G_)::value;
        if constexpr (G == 5) pstore(IC<0>{}, cprev);
    });
    waitv<4>(); BAR();
    cprev = cprev == 8 ? 0 : cprev + 1;
    qslot(IC<34>{}, T{}, [&](auto G_) {
        constexpr int G = decltype(G_)::value;
        if constexpr (G == 0) {
            int ln = tid; PIN(ln);
            const int row = ln / 72, cc = ln % 72;
            glds16r((const char*)x, (unsigned)(jb + 256 * row) * 1152 + cc * 16, lds0 + LDS_XR + w * 1024);
        }
        if constexpr (G == 1) {
            int ln = tid; PIN(ln); ln &= 63;
            const int un = 512 + ln, row = un / 72, cc = un % 72;
            const char* src = w == 0 ? (const char*)x : (const char*)b2;
            const unsigned off = w == 0 ? (unsigned)(jb + 256 * row) * 1152 + cc * 16 : (w == 1 ? 0u : 128u) + ln * 16;
            glds16r(src, off, lds0 + LDS_XR + (w == 0 ? 8192 : w == 1 ? 9216 : 9216 + 128));
        }
        if constexpr (G == 2) cf = __builtin_amdgcn_raw_buffer_load_b128(rs, plane, WS_COLF, 0);
    });
    waitv<4>(); BAR();
    qslot(IC<35>{}, T{}, nofill);
    static_for<0, 3>([&](auto G_) {
        constexpr int G = decltype(G_)::value;
        ac[1][0] = MFMA16(bfrag(IC<G + 3>{}), af[15 + G], ac[1][0]);
        ac[1][1] = MFMA16(bfrag(IC<G + 3>{}), af[33 + G], ac[1][1]);
    });
    pstore(IC<1>{}, cprev);
    STAMP(4);
    {
    int ln = tid; PIN(ln);
    if (w < 4) {
        waitv<0>();
        put_flag((ln & 63) == 0, WS_PARTF + (t * 64 + fs * 8 + w) * 8, tag);
        STAMP(5);
    } else {
        const int l6 = ln & 63, v = w - 4, hh = l6 >> 5, rB = v == 0 ? 4 : 8 - v;
        int n2 = l6 & 31; if (n2 > 23) n2 = 23;
        waitv<0>();
        put_flag(l6 == 0, WS_PARTF + (t * 64 + fs * 8 + w) * 8, tag);
        for (unsigned spins = 0; spins < (1u << 20); spins++) {
            if (__all(cf[0] == tg && cf[1] == tg && cf[2] == tg && cf[3] == tg)) break;
            __builtin_amdgcn_s_sleep(8);
            cf = __builtin_amdgcn_raw_buffer_load_b128(rs, plane, WS_COLF, 16);
        }
        __builtin_amdgcn_fence(__ATOMIC_ACQUIRE, "wavefront");
        STAMPB(11);
        u32x2 A[12], B[12];
#pragma unroll
        for (int a = 0; a < 12; a++) {
            const int k = 24 * rho(a, hh) + n2;
            const bool mir = k > 288; const int kk = mir ? 576 - k : k;
            int gq = (kk * 57) >> 9; if (gq > 31) gq = 31;
            const unsigned off = (unsigned)gq * (256 * 80) + (unsigned)(kk - 9 * gq) * 8;
            const unsigned rsel = (v != 0 && mir) ? rB : v;
            A[a] = __builtin_amdgcn_raw_buffer_load_b64(rs, off + rsel * (32 * 256 * 80), WS_VP + jb * 80, 16);
            const bool needB = v == 0 || k == 0 || k == 288;
            B[a] = __builtin_amdgcn_raw_buffer_load_b64(rs, needB ? off + rB * (32 * 256 * 80) : OOB, WS_VP + jb * 80, 16);
        }
        SB0();
        Dft24h mi; dft24h_setup(mi, l6);
        SB0();
        float re[12], im[12];
        float mx = 0.f;
#pragma unroll
        for (int a = 0; a < 12; a++) {
            const int k = 24 * rho(a, hh) + n2;
            const bool mir = k > 288, spec = (k == 0 || k == 288);
            float ax = __uint_as_float(A[a][0]), ay = __uint_as_float(A[a][1]), bx = __uint_as_float(B[a][0]), by = __uint_as_float(B[a][1]);
            if (mir) { ay = -ay; by = -by; }
            if (v == 0) {
                if (spec) { ay = 0.f; by = 0.f; }
                re[a] = ax + by; im[a] = ay - bx;
            } else {
                re[a] = spec ? 0.5f * (ax + bx) : ax; im[a] = spec ? 0.5f * (ay - by) : ay;
            }
            mx = fmaxf(mx, fmaxf(fabsf(re[a]), fabsf(im[a])));
        }
        STAMPB(12);
        unsigned mu_ = __float_as_uint(mx);
        mu_ = max(mu_, (unsigned)__builtin_amdgcn_update_dpp(0, (int)mu_, 0xB1, 0xf, 0xf, false));
        mu_ = max(mu_, (unsigned)__builtin_amdgcn_update_dpp(0, (int)mu_, 0x4E, 0xf, 0xf, false));
        mu_ = max(mu_, (unsigned)__builtin_amdgcn_update_dpp(0, (int)mu_, 0x141, 0xf, 0xf, false));
        mu_ = max(mu_, (unsigned)__builtin_amdgcn_update_dpp(0, (int)mu_, 0x128, 0xf, 0xf, false));
        { auto q = __builtin_amdgcn_permlane16_swap(mu_, mu_, false, false); mu_ = max(q[0], q[1]); }
        { auto q = __builtin_amdgcn_permlane32_swap(mu_, mu_, false, false); mu_ = max(q[0], q[1]); }
        unsigned ex = min(max(mu_ >> 23, 10u), 250u);
        const float sc = __uint_as_float((262u - ex) << 23), isc = __uint_as_float((ex - 8u) << 23);
        unsigned hw_[12];
#pragma unroll
        for (int a = 0; a < 12; a++) hw_[a] = pack_f16(re[a] * sc, im[a] * sc);
        dft24h_stage(mi, hw_, re, im);
        tw576(re, im, l6);
#pragma unroll
        for (int a = 0; a < 12; a++) hw_[a] = pack_f16(re[a], im[a]);
        dft24h_stage(mi, hw_, re, im);
        if ((l6 & 31) < 24) {
#pragma unroll
            for (int a = 0; a < 12; a += 2) {
                f32x4 o = {re[a] * isc, -im[a] * isc, re[a + 1] * isc, -im[a + 1] * isc};
                *(LAS f32x4*)(smem + LDS_CB + (v * 576 + 24 * (l6 & 31) + rho(a, hh)) * 8) = o;
            }
        }
        WAITL0();
        STAMPB(13);
    }
    if (w == 0) wait_flags<1>((GAS u64*)(ws + WS_PARTF) + t * 64, tag, ln & 63);
    SB0(); BAR();
    STAMP(6);
#pragma unroll
    for (int rep = 0; rep < 2; rep++) {
        if (rep == 1 && w != 1) break;
        const int pid = rep == 0 ? ln : 512 + (ln & 63);
        const int i = pid & 7, pc = pid >> 3;
        u32x4 pv[8];
        const unsigned po = (unsigned)((pc >> 3) * 8 + ((pc >> 1) & 3) * 2 + (fs >> 2)) * 1024 + (16 * (2 * (pc & 1) + ((fs >> 1) & 1)) + 8 * (fs & 1) + i) * 16;
#pragma unroll
        for (int f = 0; f < 8; f++) pv[f] = __builtin_amdgcn_raw_buffer_load_b128(rs, po, WS_PART + (unsigned)(f * 32 + t) * 73728, 16);
        SB0();
        float cr[4], ci[4];
        cr[0] = 1.f; ci[0] = (i & 1) ? -1.f : 1.f;
#pragma unroll
        for (int r = 1; r < 4; r++) {
            float ang = (float)((i * r) & 7) * 0.125f;
            cr[r] = 2.f * __builtin_amdgcn_cosf(ang); ci[r] = -2.f * __builtin_amdgcn_sinf(ang);
        }
        float sv[8] = {0.f, 0.f, 0.f, 0.f, 0.f, 0.f, 0.f, 0.f};
#pragma unroll
        for (int r = 0; r < 4; r++)
#pragma unroll
            for (int h2 = 0; h2 < 4; h2++) {
                const f32x4 c0 = *(LAS f32x4*)(smem + LDS_CB + (r * 576 + 8 * pc + 2 * h2) * 8);
                sv[2 * h2] += cr[r] * c0[0] + ci[r] * c0[1]; sv[2 * h2 + 1] += cr[r] * c0[2] + ci[r] * c0[3];
            }
        const u32x4 xq = *(LAS u32x4*)(smem + LDS_XR + i * 1152 + pc * 16), bq = *(LAS u32x4*)(smem + LDS_XR + 9216 + pc * 16);
#pragma unroll
        for (int e = 0; e < 8; e++) { sv[e] = rbf(sv[e]); PIN(sv[e]); }
        SB0();
        float res[8];
#pragma unroll
        for (int e = 0; e < 8; e++) {
            float o = 0.f;
#pragma unroll
            for (int f = 0; f < 8; f++) o += (e & 1) ? bf_hi(pv[f][e >> 1]) : bf_lo(pv[f][e >> 1]);
            const float bv = (e & 1) ? bf_hi(bq[e >> 1]) : bf_lo(bq[e >> 1]), xv = (e & 1) ? bf_hi(xq[e >> 1]) : bf_lo(xq[e >> 1]);
            float ov = rbf(rbf(o) + bv);
            float gv = rbf(ov * __builtin_amdgcn_rcpf(1.0f + __builtin_amdgcn_exp2f(-1.4426950408889634f * ov)));
            res[e] = xv + rbf(sv[e] * gv);
        }
        u32x4 oo = {pack_bf16(res[0], res[1]), pack_bf16(res[2], res[3]), pack_bf16(res[4], res[5]), pack_bf16(res[6], res[7])};
        *(LAS u32x4*)(smem + LDS_OUT + i * 1152 + pc * 16) = oo;
    }
    WAITL0();
    STAMP(15);
    BAR();
#pragma unroll
    for (int rep = 0; rep < 2; rep++) {
        if (rep == 1 && w != 0) break;
        const int un = ln + 512 * rep;
        const int row = un / 72, cc = un % 72;
        *(u32x4*)((char*)out + (size_t)(jb + 256 * row) * 1152 + cc * 16) = *(LAS u32x4*)(smem + LDS_OUT + un * 16);
    }
    }
}

extern "C" void kernel_launch(void* const* d_in, const int* in_sizes, int n_in,
                              void* d_out, int out_size, void* d_ws, size_t ws_size,
                              hipStream_t stream) {
    (void)hipFuncSetAttribute((const void*)k_fused, hipFuncAttributeMaxDynamicSharedMemorySize, LDS_TOTAL);
    k_fused<<<256, 512, LDS_TOTAL, stream>>>((const uint16_t*)d_in[0], (const uint16_t*)d_in[1], (const uint16_t*)d_in[2],
                                             (const uint16_t*)d_in[4], (const uint16_t*)d_in[5], (const uint16_t*)d_in[6],
                                             (const uint16_t*)d_in[7], (uint16_t*)d_out, (char*)d_ws
                                             );
}
```
